```python
import torch

def _softmax(x, axis=-1):
    return torch.softmax(x, dim=axis)
def _gelu(x):
    return torch.nn.functional.gelu(x, approximate="tanh")
def _silu(x):
    return torch.nn.functional.silu(x)
def _sigmoid(x):
    return torch.sigmoid(x)
def _layernorm(x, axis=-1, eps=1e-5):
    m = x.mean(dim=axis, keepdim=True)
    v = x.var(dim=axis, keepdim=True, unbiased=False)
    return (x - m) / torch.sqrt(v + eps)
def _scatter_add(tgt, idx, src, axis=0):
    return tgt.clone().index_add_(axis, idx, src)
def _segment_sum(src, seg, n):
    out = torch.zeros((n,) + src.shape[1:], dtype=src.dtype)
    return out.index_add_(0, seg, src)
def _pad(x, pw):
    flat = tuple(v for pair in reversed(pw) for v in pair)
    return torch.nn.functional.pad(x, flat)

kernel_name = 'recurrent_scan__sliding_local_dilated__dense_activation_relu__prompt'

def setup_inputs():

    _w = (lambda a: (lambda t: t.to(torch.bfloat16) if t.is_floating_point() else t)(torch.as_tensor(a)))
    def _A_setup():
        B, S, D = 1, 2048, 576
        return {
            "x": _w((lambda *s: torch.randn(*s, dtype=torch.bfloat16) * 0.05)(B, S, D)),
            "wqkv": _w((lambda *s: torch.randn(*s, dtype=torch.bfloat16) * 0.05)(D, 3 * D)),
            "wo": _w((lambda *s: torch.randn(*s, dtype=torch.bfloat16) * 0.05)(D, D)),
        }
    _A_in = _A_setup()

    def _B_setup():
        B, S, D, Dff = 1, 2048, 576, 4608
        return {
            "x": _w((lambda *s: torch.randn(*s, dtype=torch.bfloat16) * 0.05)(B, S, D)),
            "w1": _w((lambda *s: torch.randn(*s, dtype=torch.bfloat16) * 0.05)(D, Dff)),
            "w2": _w((lambda *s: torch.randn(*s, dtype=torch.bfloat16) * 0.05)(Dff, D)),
        }
    _B_in = _B_setup()

    return {
        "A_x": _A_in["x"],
        "A_wqkv": _A_in["wqkv"],
        "A_wo": _A_in["wo"],
        "B_x": _B_in["x"],
        "B_w1": _B_in["w1"],
        "B_w2": _B_in["w2"],
    }

def reference(A_x, A_wqkv, A_wo, B_x, B_w1, B_w2):
    def _A_ref(x, wqkv, wo):
        B, S, D = x.shape
        H = 32; Dh = D // H
        W, dil = 8, 2
        qkv = (lambda x, *d: x.permute(*d))((x @ wqkv).reshape(B, S, 3, H, Dh), 2, 0, 3, 1, 4)
        q, k, v = qkv[0], qkv[1], qkv[2]
        scale = Dh ** -0.5
        pos = torch.arange(S)
        off = torch.arange(W) * dil
        idx = pos[:, None] - off[None, :]
        mask = idx >= 0
        idx_c = torch.where(mask, idx, 0).reshape(-1)
        kg = k[:, :, idx_c, :].reshape(B, H, S, W, Dh)
        vg = v[:, :, idx_c, :].reshape(B, H, S, W, Dh)
        logits = torch.einsum("bhsd,bhswd->bhsw", q, kg) * scale
        logits = torch.where(mask[None, None, :, :], logits, -1e9)
        attn = _softmax(logits, axis=-1)
        o = torch.einsum("bhsw,bhswd->bhsd", attn, vg)
        return (lambda x, *d: x.permute(*d))(o, 0, 2, 1, 3).reshape(B, S, D) @ wo

    def _B_ref(x, w1, w2):
        h = torch.einsum("bsd,df->bsf", x, w1)
        zero = torch.zeros_like(h)
        h = torch.maximum(h, zero)
        return torch.einsum("bsf,fd->bsd", h, w2)

    x = A_x
    xn = _layernorm(x, axis=-1)
    s = _A_ref(xn, A_wqkv, A_wo)
    g = _silu(_B_ref(xn, B_w1, B_w2))
    return x + s * g
```

```cpp
#include <hip/hip_runtime.h>
#include <hip/hip_bf16.h>
typedef unsigned short u16;
__device__ __forceinline__ float bf2f(u16 v){ return __uint_as_float(((unsigned)v)<<16); }
__device__ __forceinline__ u16 f2bf(float f){ unsigned u=__float_as_uint(f); u += 0x7fffu + ((u>>16)&1u); return (u16)(u>>16); }
constexpr int S=2048, D=576, DFF=4608, H=32, DH=18;

__global__ void ln_k(const u16* x, u16* xn){
  int row=blockIdx.x; int t=threadIdx.x;
  float v[9]; float s=0;
  for(int i=0;i<9;i++){ v[i]=bf2f(x[row*D+t+64*i]); s+=v[i]; }
  for(int o=32;o>0;o>>=1) s+=__shfl_xor(s,o);
  float m=s/D; float q=0;
  for(int i=0;i<9;i++){ float d=v[i]-m; q+=d*d; }
  for(int o=32;o>0;o>>=1) q+=__shfl_xor(q,o);
  float r=rsqrtf(q/D+1e-5f);
  for(int i=0;i<9;i++) xn[row*D+t+64*i]=f2bf((v[i]-m)*r);
}
__global__ void gemm_k(const u16* A,const u16* B,u16* C,int M,int N,int K,int relu){
  __shared__ float As[16][17], Bs[16][17];
  int tx=threadIdx.x, ty=threadIdx.y;
  int row=blockIdx.y*16+ty, col=blockIdx.x*16+tx;
  float acc=0;
  for(int k0=0;k0<K;k0+=16){
    As[ty][tx]=bf2f(A[row*K+k0+tx]);
    Bs[ty][tx]=bf2f(B[(k0+ty)*N+col]);
    __syncthreads();
    for(int k=0;k<16;k++) acc+=As[ty][k]*Bs[k][tx];
    __syncthreads();
  }
  if(relu) acc=fmaxf(acc,0.f);
  C[row*N+col]=f2bf(acc);
}
__global__ void attn_k(const u16* qkv,u16* o){
  int idx=blockIdx.x*blockDim.x+threadIdx.x;
  int s=idx/H,h=idx%H;
  const u16* q=qkv+s*3*D+h*DH;
  float l[8]; float mx=-1e30f;
  for(int w=0;w<8;w++){
    int p=s-2*w;
    if(p>=0){ const u16* k=qkv+p*3*D+D+h*DH; float a=0; for(int d=0;d<DH;d++) a+=bf2f(q[d])*bf2f(k[d]); l[w]=a*0.23570226f; } else l[w]=-1e9f;
    mx=fmaxf(mx,l[w]);
  }
  float sum=0; for(int w=0;w<8;w++){ l[w]=__expf(l[w]-mx); sum+=l[w]; }
  for(int d=0;d<DH;d++){ float a=0; for(int w=0;w<8;w++){ int p=s-2*w; if(p<0)p=0; a+=l[w]*bf2f(qkv[p*3*D+2*D+h*DH+d]); } o[s*D+h*DH+d]=f2bf(a/sum); }
}
__global__ void fin_k(const u16* x,const u16* s,const u16* f,u16* out){
  int i=blockIdx.x*blockDim.x+threadIdx.x;
  float fv=bf2f(f[i]); float g=fv/(1.f+__expf(-fv));
  out[i]=f2bf(bf2f(x[i])+bf2f(s[i])*g);
}
extern "C" void kernel_launch(void* const* d_in, const int* in_sizes, int n_in,
                              void* d_out, int out_size, void* d_ws, size_t ws_size,
                              hipStream_t stream) {
  const u16* x=(const u16*)d_in[0]; const u16* wqkv=(const u16*)d_in[1]; const u16* wo=(const u16*)d_in[2];
  const u16* w1=(const u16*)d_in[4]; const u16* w2=(const u16*)d_in[5];
  u16* ws=(u16*)d_ws;
  u16* xn=ws; u16* qkv=xn+S*D; u16* o=qkv+S*3*D; u16* sb=o+S*D; u16* h=sb+S*D; u16* f=h+S*DFF;
  ln_k<<<S,64,0,stream>>>(x,xn);
  gemm_k<<<dim3(3*D/16,S/16),dim3(16,16),0,stream>>>(xn,wqkv,qkv,S,3*D,D,0);
  attn_k<<<S*H/256,256,0,stream>>>(qkv,o);
  gemm_k<<<dim3(D/16,S/16),dim3(16,16),0,stream>>>(o,wo,sb,S,D,D,0);
  gemm_k<<<dim3(DFF/16,S/16),dim3(16,16),0,stream>>>(xn,w1,h,S,DFF,D,1);
  gemm_k<<<dim3(D/16,S/16),dim3(16,16),0,stream>>>(h,w2,f,S,D,DFF,0);
  fin_k<<<S*D/256,256,0,stream>>>(x,sb,f,(u16*)d_out);
}
```

```cpp
#include <hip/hip_runtime.h>
#include <utility>

typedef unsigned short u16;
typedef __bf16 bf16x2 __attribute__((ext_vector_type(2)));
typedef __bf16 bf16x4 __attribute__((ext_vector_type(4)));
typedef __bf16 bf16x8 __attribute__((ext_vector_type(8)));
typedef short s16x2 __attribute__((ext_vector_type(2)));
typedef float f32x2 __attribute__((ext_vector_type(2)));
typedef float f32x4 __attribute__((ext_vector_type(4)));
typedef unsigned u32x2 __attribute__((ext_vector_type(2)));
typedef unsigned u32x4 __attribute__((ext_vector_type(4)));
#define LDSP(T) __attribute__((address_space(3))) T*
#define DI __device__ __forceinline__
#define GAP() __builtin_amdgcn_sched_barrier(0)
template <int V> using IC = std::integral_constant<int, V>;
template <class F, int... Is> DI void sfor_(F&& f, std::integer_sequence<int, Is...>) { (f(IC<Is>{}), ...); }
template <int N, class F> DI void sfor(F&& f) { sfor_(f, std::make_integer_sequence<int, N>{}); }

constexpr int SEQ = 2048, DM = 576, DFF = 4608;
constexpr unsigned XN_BYTES = SEQ * DM * 2;
constexpr unsigned WOT_BYTES = DM * DM * 2;
constexpr size_t XN_OFF = 0;
constexpr size_t WOT_OFF = XN_OFF + XN_BYTES;
constexpr size_t OT_OFF = WOT_OFF + WOT_BYTES;
constexpr size_t PART_OFF = OT_OFF + XN_BYTES;
constexpr unsigned PART_BYTES = 12u * XN_BYTES;

DI unsigned cvtpk(float lo, float hi) { f32x2 v = {lo, hi}; return __builtin_bit_cast(unsigned, __builtin_convertvector(v, bf16x2)); }
DI unsigned relupk(unsigned p) { s16x2 z = {0, 0}; return __builtin_bit_cast(unsigned, __builtin_elementwise_max(__builtin_bit_cast(s16x2, p), z)); }
DI float bflo(unsigned p) { return __builtin_bit_cast(float, p << 16); }
DI float bfhi(unsigned p) { return __builtin_bit_cast(float, p & 0xffff0000u); }
DI float dot2(unsigned a, unsigned b, float c) { return __builtin_amdgcn_fdot2_f32_bf16(__builtin_bit_cast(bf16x2, a), __builtin_bit_cast(bf16x2, b), c, false); }
DI bf16x4 lds_tr(unsigned addr) { return __builtin_amdgcn_ds_read_tr16_b64_v4bf16((LDSP(bf16x4))(size_t)addr); }
DI bf16x8 lds_rd128(unsigned addr) { return *(LDSP(bf16x8))(size_t)addr; }
DI u32x2 lds_rd64(unsigned addr) { return *(LDSP(u32x2))(size_t)addr; }
DI void lds_wr128(unsigned addr, u32x4 v) { *(LDSP(u32x4))(size_t)addr = v; }
DI void lds_wr64(unsigned addr, u32x2 v) { *(LDSP(u32x2))(size_t)addr = v; }
DI bf16x8 cat(bf16x4 a, bf16x4 b) { return __builtin_shufflevector(a, b, 0, 1, 2, 3, 4, 5, 6, 7); }
DI f32x4 mfma(bf16x8 a, bf16x8 b, f32x4 c) { return __builtin_amdgcn_mfma_f32_16x16x32_bf16(a, b, c, 0, 0, 0); }
template <int N> DI void waitv() { asm volatile("s_waitcnt vmcnt(%0)" ::"i"(N) : "memory"); }
DI void waitl0() { asm volatile("s_waitcnt lgkmcnt(0)" ::: "memory"); }
DI void bar() { asm volatile("s_barrier" ::: "memory"); }
DI u32x4 make_srd(const void* p, unsigned bytes) {
  size_t a = (size_t)p; u32x4 s;
  s[0] = __builtin_amdgcn_readfirstlane((unsigned)a); s[1] = __builtin_amdgcn_readfirstlane((unsigned)(a >> 32));
  s[2] = bytes; s[3] = 0x00020000u; return s;
}
DI void dma(unsigned lds_dst, unsigned voff, u32x4 srd, unsigned soff) {
  asm volatile("s_mov_b32 m0, %0\n\ts_nop 0\n\tbuffer_load_dwordx4 %1, %2, %3 offen lds" ::"s"(lds_dst), "v"(voff), "s"(srd), "s"(soff) : "memory");
}
template <int CTRL> DI float dppadd(float v) { return v + __builtin_bit_cast(float, __builtin_amdgcn_update_dpp(0, __builtin_bit_cast(int, v), CTRL, 0xF, 0xF, true)); }
DI float red32(float v) {
  v = dppadd<0xB1>(v); v = dppadd<0x4E>(v); v = dppadd<0x141>(v); v = dppadd<0x140>(v);
  float a = v, b = v;
  asm volatile("s_nop 1\n\tv_permlane16_swap_b32 %0, %1" : "+v"(a), "+v"(b));
  return a + b;
}

__global__ __launch_bounds__(256) void k0_ln(const u16* __restrict__ x, const u16* __restrict__ wo, char* __restrict__ ws) {
  const int tid = threadIdx.x, lane = tid & 63, wv = blockIdx.x * 4 + (tid >> 6);
  const int l32 = lane & 31, row = wv * 2 + (lane >> 5);
  const u32x4* xr = (const u32x4*)(x + (size_t)row * DM);
  u32x4 c[3];
  c[0] = xr[l32]; c[1] = xr[l32 + 32];
  c[2] = (l32 < 8) ? xr[l32 + 64] : (u32x4){0, 0, 0, 0};
  u16 wv8[8];
  const bool dow = wv < 648;
  if (dow) {
    const int ct = wv / 18, s = wv % 18, g = lane >> 4, cc = lane & 15;
#pragma unroll
    for (int j = 0; j < 8; j++) wv8[j] = wo[(32 * s + 16 * (j >> 2) + 4 * g + (j & 3)) * DM + 16 * ct + cc];
  }
  float v[24]; float sum = 0.f;
#pragma unroll
  for (int i = 0; i < 3; i++)
#pragma unroll
    for (int j = 0; j < 4; j++) { v[i * 8 + 2 * j] = bflo(c[i][j]); v[i * 8 + 2 * j + 1] = bfhi(c[i][j]); sum += v[i * 8 + 2 * j] + v[i * 8 + 2 * j + 1]; }
  const float mean = red32(sum) * (1.f / DM);
  float sq = 0.f;
#pragma unroll
  for (int i = 0; i < 16; i++) { v[i] -= mean; sq += v[i] * v[i]; }
  float sq2 = 0.f;
#pragma unroll
  for (int i = 16; i < 24; i++) { v[i] -= mean; sq2 += v[i] * v[i]; }
  if (l32 < 8) sq += sq2;
  const float rstd = __builtin_amdgcn_rsqf(red32(sq) * (1.f / DM) + 1e-5f);
  const int cq = l32 & 3; const bool b1 = (cq & 2) != 0;
  const int g = (cq & 1) * 2 + (cq >> 1);
  char* dst = ws + XN_OFF + (size_t)(row >> 4) * 18 * 1024 + ((row & 15) + 16 * g) * 16;
#pragma unroll
  for (int i = 0; i < 3; i++) {
    unsigned d[4];
#pragma unroll
    for (int j = 0; j < 4; j++) d[j] = cvtpk(v[i * 8 + 2 * j] * rstd, v[i * 8 + 2 * j + 1] * rstd);
    const unsigned s0 = b1 ? d[0] : d[2], s1 = b1 ? d[1] : d[3];
    const unsigned r0 = __builtin_amdgcn_update_dpp(0, (int)s0, 0x4E, 0xF, 0xF, true), r1 = __builtin_amdgcn_update_dpp(0, (int)s1, 0x4E, 0xF, 0xF, true);
    u32x4 o = b1 ? (u32x4){r0, r1, d[2], d[3]} : (u32x4){d[0], d[1], r0, r1};
    const int s = (l32 >> 2) + 8 * i;
    if (i < 2 || l32 < 8) *(u32x4*)(dst + s * 1024) = o;
  }
  if (dow) {
    u32x4 o;
#pragma unroll
    for (int j = 0; j < 4; j++) o[j] = (unsigned)wv8[2 * j] | ((unsigned)wv8[2 * j + 1] << 16);
    *(u32x4*)(ws + WOT_OFF + (size_t)wv * 1024 + lane * 16) = o;
  }
}

constexpr int p2_first(int j) { return j < 2 ? 0 : (j <= 7 ? 2 * j : j + 8); }
constexpr int p2_count(int j) { return j < 2 ? 0 : (j <= 7 ? 2 : (j + 8 <= 26 ? 1 : 0)); }
constexpr int vm_allowed(int T, int need) {
  int after = 0;
  auto op = [&](int tag, int n) { if (tag == need) after = 0; else after += n; };
  op(0, 4); op(1, 4); op(2, 4);
  if (T < 0) return after;
  for (int t = 0; t < 18; t++) {
    if (t <= 14) op(t + 3, 4);
    else if (t == 15) { op(100, 2); op(101, 2); }
    else if (t == 16) { op(102, 2); op(103, 2); }
    if (t == T) return after;
  }
  if (T == 18) return after;
  for (int j = 0; j < 27; j++) {
    if (j >= 3 && j % 3 == 0) op(-1, 2);
    for (int c = 0; c < p2_count(j); c++) op(100 + p2_first(j) + c, 2);
    if (19 + j == T) return after;
  }
  return 0;
}

DI void ffn_path(const u16* w1, const u16* w2, char* ws, int rt, int sp) {
  const int tid = threadIdx.x, lane = tid & 63;
  const int w = __builtin_amdgcn_readfirstlane(tid >> 6), tg = w >> 2, cg = w & 3;
  const int g = lane >> 4, q = (lane & 15) >> 2, p = lane & 3;
  const u32x4 srd1 = make_srd(w1, DM * DFF * 2), srd2 = make_srd(w2, DM * DFF * 2), srdx = make_srd(ws + XN_OFF, XN_BYTES);
  const unsigned swz = 16u * ((lane & 7) ^ (2 * ((lane >> 4) & 3)));
  const unsigned vo1 = (lane >> 3) * (DFF * 2) + swz, vo2 = (lane >> 3) * (DM * 2) + swz, vox = lane * 16;
  unsigned so1[3];
#pragma unroll
  for (int i = 0; i < 3; i++) { const int id = 3 * w + i; so1[i] = (unsigned)((8 * (id & 3)) * (DFF * 2) + (sp * 384 + (id >> 2) * 64) * 2); }
  const unsigned sox = (unsigned)((rt * 8 + w) * 18 * 1024);
  const unsigned so2 = (unsigned)((sp * 384 + 16 * w) * (DM * 2));
  const unsigned m1 = (unsigned)(3 * w * 1024), mx = (unsigned)(24576 + w * 1024), m2 = (unsigned)(2 * w * 1024);
  auto load1 = [&](auto Sc, auto Ic) {
    constexpr int S = decltype(Sc)::value, I = decltype(Ic)::value; constexpr unsigned slot = (S % 5) * 32768u;
    if constexpr (I < 3) dma(slot + m1 + I * 1024, vo1, srd1, so1[I] + (unsigned)(S * 32 * DFF * 2));
    else dma(slot + mx, vox, srdx, sox + S * 1024);
  };
  auto load2 = [&](auto Jc, auto Ic) {
    constexpr int J = decltype(Jc)::value, I = decltype(Ic)::value; constexpr unsigned slot = ((J + 6) % 10) * 16384u;
    dma(slot + m2 + I * 1024, vo2, srd2, so2 + (unsigned)(((J % 3) * 128 + 8 * I) * (DM * 2) + (J / 3) * 128));
  };
  sfor<3>([&](auto s) { sfor<4>([&](auto i) { load1(s, i); }); });

  const int row = 4 * g + q;
  const unsigned wb = row * 128 + 32 * (cg ^ ((row >> 1) & 3)) + 8 * p;
  const unsigned wbh = wb + 65536, wbh2 = wb + 131072;
  const unsigned xb = lane * 16 + tg * 4096, xbh = xb + 65536, xbh2 = xb + 131072;
  auto waddr = [&](auto Oc) { constexpr unsigned O = decltype(Oc)::value; return O < 65536 ? wb + O : (O < 131072 ? wbh + (O - 65536) : wbh2 + (O - 131072)); };
  auto xaddr = [&](auto Oc) { constexpr unsigned O = decltype(Oc)::value; return O < 65536 ? xb + O : (O < 131072 ? xbh + (O - 65536) : xbh2 + (O - 131072)); };

  f32x4 acc[6][4];
  bf16x8 xf[2][4];
  bf16x4 wlo[3], whi[3];
  auto rdw1 = [&](auto Sc, auto SBc, auto Hc) {
    constexpr int S = decltype(Sc)::value, SB = decltype(SBc)::value, H = decltype(Hc)::value;
    constexpr int O = (S % 5) * 32768 + SB * 4096 + H * 2048;
    if constexpr (H == 0) wlo[SB % 3] = lds_tr(waddr(IC<O>{})); else whi[SB % 3] = lds_tr(waddr(IC<O>{}));
  };
  auto rdx = [&](auto Sc, auto Tc) {
    constexpr int S = decltype(Sc)::value, T = decltype(Tc)::value;
    xf[S & 1][T] = lds_rd128(xaddr(IC<(S % 5) * 32768 + 24576 + T * 1024>{}));
  };
  waitv<vm_allowed(-1, 0)>(); bar();
  sfor<4>([&](auto t) { rdx(IC<0>{}, t); });
  rdw1(IC<0>{}, IC<0>{}, IC<0>{}); rdw1(IC<0>{}, IC<0>{}, IC<1>{}); rdw1(IC<0>{}, IC<1>{}, IC<0>{}); rdw1(IC<0>{}, IC<1>{}, IC<1>{});
  const unsigned hw = lane * 16 + (tg * 16 + cg) * 1024;
  auto hpack = [&](auto Uc, auto Tc) {
    constexpr int U = decltype(Uc)::value, T = decltype(Tc)::value;
    u32x4 o;
    o[0] = relupk(cvtpk(acc[2 * U][T][0], acc[2 * U][T][1])); o[1] = relupk(cvtpk(acc[2 * U][T][2], acc[2 * U][T][3]));
    o[2] = relupk(cvtpk(acc[2 * U + 1][T][0], acc[2 * U + 1][T][1])); o[3] = relupk(cvtpk(acc[2 * U + 1][T][2], acc[2 * U + 1][T][3]));
    lds_wr128((U == 2 ? hw + 65536 : hw + U * 32768) + T * 4096, o);
  };
  GAP();
  sfor<18>([&](auto Sc) {
    constexpr int S = decltype(Sc)::value;
    sfor<6>([&](auto SBc) {
      constexpr int SB = decltype(SBc)::value;
      const bf16x8 a = cat(wlo[SB % 3], whi[SB % 3]);
      sfor<4>([&](auto Tc) {
        constexpr int T = decltype(Tc)::value;
        if constexpr (S == 0) acc[SB][T] = mfma(a, xf[0][T], (f32x4){0.f, 0.f, 0.f, 0.f});
        else acc[SB][T] = mfma(a, xf[S & 1][T], acc[SB][T]);
        GAP();
        if constexpr (SB == 4 && T == 0) {
          if constexpr (S < 17) { waitv<vm_allowed(S, S + 1)>(); bar(); }
          else { waitl0(); bar(); }
        }
        if constexpr (SB < 4) {
          if constexpr (T < 2) rdw1(Sc, IC<SB + 2>{}, Tc);
          if constexpr (T == 2) {
            if constexpr (S <= 14) load1(IC<S + 3>{}, SBc);
            else if constexpr (S <= 16) load2(IC<(S - 15) * 2 + (SB >> 1)>{}, IC<(SB & 1)>{});
          }
        } else if constexpr (S < 17) {
          if constexpr (T < 2) rdw1(IC<S + 1>{}, IC<SB - 4>{}, Tc);
          else rdx(IC<S + 1>{}, IC<(SB - 4) * 2 + (T - 2)>{});
        } else {
          hpack(IC<SB - 4>{}, Tc);
        }
        GAP();
      });
    });
  });
  sfor<4>([&](auto t) { hpack(IC<2>{}, t); });
  waitv<vm_allowed(18, 100)>(); waitl0(); bar();

  bf16x8 hf[4][12];
  f32x4 oc[4];
  const unsigned hb = lane * 16 + tg * 16384, hbh = hb + 65536;
  auto rdh = [&](auto Ic, auto Tc) {
    constexpr int I = decltype(Ic)::value, T = decltype(Tc)::value; constexpr int U = I >> 2;
    hf[T][I] = lds_rd128((U == 2 ? hbh : hb + U * 32768) + T * 4096 + (I & 3) * 1024);
  };
  auto rdw2 = [&](auto GIc, auto Hc) {
    constexpr int GI = decltype(GIc)::value, H = decltype(Hc)::value;
    constexpr int O = (((GI >> 2) + 6) % 10) * 16384 + (GI & 3) * 2048 + H * 8192;
    if constexpr (H == 0) wlo[GI % 3] = lds_tr(waddr(IC<O>{})); else whi[GI % 3] = lds_tr(waddr(IC<O>{}));
  };
  rdw2(IC<0>{}, IC<0>{}); rdw2(IC<0>{}, IC<1>{});
  sfor<4>([&](auto t) { rdh(IC<0>{}, t); });
  rdw2(IC<1>{}, IC<0>{}); rdw2(IC<1>{}, IC<1>{});
  sfor<4>([&](auto t) { rdh(IC<1>{}, t); });
  const __amdgpu_buffer_rsrc_t prs = __builtin_amdgcn_make_buffer_rsrc(ws + PART_OFF, 0, PART_BYTES, 0x00020000);
  const unsigned pso = (unsigned)(((rt * 9) * 12 + sp) * 16384 + w * 2048);
  unsigned pk[8];
  GAP();
  sfor<108>([&](auto GIc) {
    constexpr int GI = decltype(GIc)::value, J = GI >> 2, C = GI & 3, I = GI % 12, N = GI / 12;
    const bf16x8 a = cat(wlo[GI % 3], whi[GI % 3]);
    sfor<4>([&](auto Tc) {
      constexpr int T = decltype(Tc)::value;
      if constexpr (I == 0 && N > 0) { pk[2 * T] = cvtpk(oc[T][0], oc[T][1]); pk[2 * T + 1] = cvtpk(oc[T][2], oc[T][3]); GAP(); }
      if constexpr (I == 0) oc[T] = mfma(a, hf[T][I], (f32x4){0.f, 0.f, 0.f, 0.f});
      else oc[T] = mfma(a, hf[T][I], oc[T]);
      GAP();
      if constexpr (C == 2 && T == 0 && J < 26) { waitv<vm_allowed(19 + J, 100 + J + 1)>(); bar(); }
      if constexpr (GI + 2 < 108 && T < 2) rdw2(IC<GI + 2>{}, Tc);
      if constexpr (GI + 2 < 12) rdh(IC<GI + 2>{}, Tc);
      if constexpr (I == 0 && N > 0 && (T & 1)) {
        __builtin_amdgcn_raw_buffer_store_b128((u32x4){pk[2 * T - 2], pk[2 * T - 1], pk[2 * T], pk[2 * T + 1]}, prs, lane * 16 + (T >> 1) * 1024, pso + (N - 1) * 12 * 16384, 0);
      }
      if constexpr (C < 2) {
        constexpr int cnt = p2_count(J);
        if constexpr (I == 0 && N > 0) {
        }
        if constexpr (cnt == 1) { if constexpr (C == 1 && T >= 2) load2(IC<p2_first(J)>{}, IC<T - 2>{}); }
        if constexpr (cnt == 2) { if constexpr (C == 1) load2(IC<p2_first(J) + (T >> 1)>{}, IC<(T & 1)>{}); }
      }
      GAP();
    });
  });
  sfor<2>([&](auto hc) {
    constexpr int H = decltype(hc)::value;
    u32x4 o = {cvtpk(oc[2 * H][0], oc[2 * H][1]), cvtpk(oc[2 * H][2], oc[2 * H][3]), cvtpk(oc[2 * H + 1][0], oc[2 * H + 1][1]), cvtpk(oc[2 * H + 1][2], oc[2 * H + 1][3])};
    __builtin_amdgcn_raw_buffer_store_b128(o, prs, lane * 16 + H * 1024, pso + 8 * 12 * 16384, 0);
  });
}

DI void att_path(const u16* wqkv, char* ws, int rt, int hg) {
  const int tid = threadIdx.x, lane = tid & 63;
  const int w = __builtin_amdgcn_readfirstlane(tid >> 6), tgp = w >> 2, cgp = w & 3;
  const int g = lane >> 4, q = (lane & 15) >> 2, p = lane & 3;
  const u32x4 srdw = make_srd(wqkv, DM * 3 * DM * 2), srdx = make_srd(ws + XN_OFF, XN_BYTES);
  constexpr unsigned SLOT = 36864, XO = 27648;
  unsigned vw[4];
#pragma unroll
  for (int i = 0; i < 4; i++) {
    const int n = (w + 8 * i) * 64 + lane, r = n / 54, c = n % 54;
    vw[i] = r * 3456 + (c / 18) * 1152 + hg * 288 + (c % 18) * 16;
  }
  const unsigned vox = lane * 16;
  const int tile0 = rt * 8 - 1;
  const unsigned sox = (unsigned)(max(tile0 + w, 0) * 18 * 1024), sox8 = (unsigned)((tile0 + 8) * 18 * 1024);
  auto load = [&](int s, unsigned slot, int i) {
    if (i < 3) dma(slot + (w + 8 * i) * 1024, vw[i], srdw, (unsigned)(s * 32 * 3456));
    else if (i == 3) dma(slot + XO + w * 1024, vox, srdx, sox + s * 1024);
    else if (w < 3) dma(slot + (w + 24) * 1024, vw[3], srdw, (unsigned)(s * 32 * 3456));
    else if (w == 3) dma(slot + XO + 8192, vox, srdx, sox8 + s * 1024);
  };
#pragma unroll
  for (int s = 0; s < 2; s++)
#pragma unroll
    for (int i = 0; i < 5; i++) load(s, s * SLOT, i);
  const unsigned wb = (4 * g + q) * 864 + 32 * 7 * cgp + 8 * p;
  const unsigned xb = XO + lane * 16 + tgp * 5 * 1024;
  f32x4 acc[7][5];
#pragma unroll
  for (int c = 0; c < 7; c++)
#pragma unroll
    for (int t = 0; t < 5; t++) acc[c][t] = (f32x4){0.f, 0.f, 0.f, 0.f};
  bf16x8 xf[5];
  bf16x4 wlo[7], whi[7];
  for (int s = 0; s < 18; s++) {
    const unsigned slot = (unsigned)(s & 3) * SLOT;
    if (s < 17) { if (w < 4) waitv<5>(); else waitv<4>(); } else waitv<0>();
    bar();
#pragma unroll
    for (int t = 0; t < 4; t++) xf[t] = lds_rd128(xb + slot + t * 1024);
    if (tgp == 0) xf[4] = lds_rd128(xb + slot + 4096);
#pragma unroll
    for (int c = 0; c < 7; c++) { wlo[c] = lds_tr(wb + slot + 32 * c); whi[c] = lds_tr(wb + slot + 32 * c + 16 * 864); }
    if (s + 2 < 18) {
      const unsigned nslot = (unsigned)((s + 2) & 3) * SLOT;
#pragma unroll
      for (int i = 0; i < 5; i++) load(s + 2, nslot, i);
    }
#pragma unroll
    for (int c = 0; c < 7; c++) {
      const bf16x8 a = cat(wlo[c], whi[c]);
#pragma unroll
      for (int t = 0; t < 4; t++) acc[c][t] = mfma(a, xf[t], acc[c][t]);
    }
    if (tgp == 0) {
#pragma unroll
      for (int c = 0; c < 7; c++) acc[c][4] = mfma(cat(wlo[c], whi[c]), xf[4], acc[c][4]);
    }
  }
  waitl0(); bar();
  {
    const unsigned ib = (tgp * 80 + (lane & 15)) * 872 + 32 * 7 * cgp + 8 * g;
#pragma unroll
    for (int c = 0; c < 7; c++) {
      if (c < 6 || cgp < 3) {
#pragma unroll
        for (int t = 0; t < 5; t++)
          if (t < 4 || tgp == 0) lds_wr64(ib + t * 16 * 872 + 32 * c, (u32x2){cvtpk(acc[c][t][0], acc[c][t][1]), cvtpk(acc[c][t][2], acc[c][t][3])});
      }
    }
  }
  waitl0(); bar();
  const int tl = tid & 127, hp = w >> 1;
  const unsigned rb = (tl + 16) * 872 + 72 * hp;
  const int pos = rt * 128 + tl;
  unsigned qv[18];
#pragma unroll
  for (int i = 0; i < 9; i++) { u32x2 t = lds_rd64(rb + 8 * i); qv[2 * i] = t[0]; qv[2 * i + 1] = t[1]; }
  float lg[2][8];
#pragma unroll
  for (int j = 0; j < 8; j++) {
    unsigned kv[18];
#pragma unroll
    for (int i = 0; i < 9; i++) { u32x2 t = lds_rd64(rb - j * 2 * 872 + 288 + 8 * i); kv[2 * i] = t[0]; kv[2 * i + 1] = t[1]; }
    float a0 = 0.f, a1 = 0.f;
#pragma unroll
    for (int i = 0; i < 9; i++) { a0 = dot2(qv[i], kv[i], a0); a1 = dot2(qv[9 + i], kv[9 + i], a1); }
    const bool ok = pos - 2 * j >= 0;
    constexpr float SC = 0.23570226039551584f * 1.4426950408889634f;
    lg[0][j] = ok ? a0 * SC : -1e9f; lg[1][j] = ok ? a1 * SC : -1e9f;
  }
  unsigned pp[2][4];
#pragma unroll
  for (int h = 0; h < 2; h++) {
    float mx = lg[h][0];
#pragma unroll
    for (int j = 1; j < 8; j++) mx = fmaxf(mx, lg[h][j]);
    float sum = 0.f;
#pragma unroll
    for (int j = 0; j < 8; j++) { lg[h][j] = __builtin_amdgcn_exp2f(lg[h][j] - mx); sum += lg[h][j]; }
    const float inv = __builtin_amdgcn_rcpf(sum);
#pragma unroll
    for (int j = 0; j < 4; j++) pp[h][j] = cvtpk(lg[h][2 * j] * inv, lg[h][2 * j + 1] * inv);
  }
  float ov[36];
#pragma unroll
  for (int i = 0; i < 36; i++) ov[i] = 0.f;
#pragma unroll
  for (int j = 0; j < 4; j++) {
#pragma unroll
    for (int i = 0; i < 9; i++) {
      const u32x2 va = lds_rd64(rb - (2 * j) * 2 * 872 + 576 + 8 * i), vb = lds_rd64(rb - (2 * j + 1) * 2 * 872 + 576 + 8 * i);
#pragma unroll
      for (int e = 0; e < 2; e++) {
        const int d2 = 2 * i + e; const unsigned pw = pp[d2 / 9][j];
        ov[2 * d2] = dot2(__builtin_amdgcn_perm(vb[e], va[e], 0x05040100u), pw, ov[2 * d2]);
        ov[2 * d2 + 1] = dot2(__builtin_amdgcn_perm(vb[e], va[e], 0x07060302u), pw, ov[2 * d2 + 1]);
      }
    }
  }
  char* ob = ws + OT_OFF + (size_t)(pos >> 4) * 18 * 1024 + (pos & 15) * 16;
#pragma unroll
  for (int i = 0; i < 9; i++) {
    const int col = hg * 144 + 36 * hp + 4 * i, kl = col & 31;
    *(u32x2*)(ob + (col >> 5) * 1024 + ((kl & 15) >> 2) * 256 + (kl >> 4) * 8) = (u32x2){cvtpk(ov[4 * i], ov[4 * i + 1]), cvtpk(ov[4 * i + 2], ov[4 * i + 3])};
  }
}

__global__ __launch_bounds__(512, 2) void k1_main(const u16* __restrict__ wqkv, const u16* __restrict__ w1, const u16* __restrict__ w2, char* __restrict__ ws) {
  extern __shared__ __attribute__((aligned(16))) char smem[];
  const int b = blockIdx.x, xc = b & 7, i = b >> 3;
  if (i < 24) {
    const int j = xc >> 1;
    int sp, rt;
    if (i < 16) { sp = 3 * j + 2 * (xc & 1); rt = i; } else { sp = 3 * j + 1; rt = 8 * (xc & 1) + (i - 16); }
    ffn_path(w1, w2, ws, rt, sp);
  } else {
    att_path(wqkv, ws, 8 * (xc & 1) + (i - 24), xc >> 1);
  }
}

__global__ __launch_bounds__(256) void k2_fin(const u16* __restrict__ x, const char* __restrict__ ws, u16* __restrict__ out) {
  const int tid = threadIdx.x, lane = tid & 63, cg = tid >> 6;
  const int b = blockIdx.x, n = b % 9, r2 = b / 9, rt = r2 >> 1, tg = r2 & 1;
  const char* pp = ws + PART_OFF + (size_t)((rt * 9 + n) * 12) * 16384 + (tg * 4 + cg) * 2048 + lane * 16;
  u32x4 pv[12][2];
#pragma unroll
  for (int s = 0; s < 12; s++) { pv[s][0] = *(const u32x4*)(pp + s * 16384); pv[s][1] = *(const u32x4*)(pp + s * 16384 + 1024); }
  const int g = lane >> 4, tok0 = r2 * 64 + (lane & 15), col = n * 64 + cg * 16 + 4 * g;
  u32x2 xv[4];
#pragma unroll
  for (int t = 0; t < 4; t++) xv[t] = *(const u32x2*)(x + (size_t)(tok0 + 16 * t) * DM + col);
  const char* ap = ws + WOT_OFF + (size_t)(4 * n + cg) * 18 * 1024 + lane * 16;
  const char* bp = ws + OT_OFF + (size_t)(r2 * 4) * 18 * 1024 + lane * 16;
  f32x4 sc[4] = {};
#pragma unroll
  for (int s = 0; s < 18; s++) {
    const bf16x8 a = *(const bf16x8*)(ap + s * 1024);
#pragma unroll
    for (int t = 0; t < 4; t++) sc[t] = mfma(a, *(const bf16x8*)(bp + (t * 18 + s) * 1024), sc[t]);
  }
  float f[16];
#pragma unroll
  for (int i = 0; i < 16; i++) f[i] = 0.f;
#pragma unroll
  for (int s = 0; s < 12; s++)
#pragma unroll
    for (int h = 0; h < 2; h++)
#pragma unroll
      for (int j = 0; j < 4; j++) { f[h * 8 + 2 * j] += bflo(pv[s][h][j]); f[h * 8 + 2 * j + 1] += bfhi(pv[s][h][j]); }
#pragma unroll
  for (int t = 0; t < 4; t++) {
    float o[4];
#pragma unroll
    for (int j = 0; j < 4; j++) {
      const float fv = f[4 * t + j];
      const float gt = fv * __builtin_amdgcn_rcpf(1.f + __builtin_amdgcn_exp2f(-1.4426950408889634f * fv));
      const float xx = (j & 1) ? bfhi(xv[t][j >> 1]) : bflo(xv[t][j >> 1]);
      o[j] = xx + sc[t][j] * gt;
    }
    *(u32x2*)(out + (size_t)(tok0 + 16 * t) * DM + col) = (u32x2){cvtpk(o[0], o[1]), cvtpk(o[2], o[3])};
  }
}

extern "C" void kernel_launch(void* const* d_in, const int* in_sizes, int n_in, void* d_out, int out_size, void* d_ws, size_t ws_size, hipStream_t stream) {
  const u16* x = (const u16*)d_in[0]; const u16* wqkv = (const u16*)d_in[1]; const u16* wo = (const u16*)d_in[2];
  const u16* w1 = (const u16*)d_in[4]; const u16* w2 = (const u16*)d_in[5];
  char* ws = (char*)d_ws;
  (void)hipFuncSetAttribute((const void*)k1_main, hipFuncAttributeMaxDynamicSharedMemorySize, 163840);
  k0_ln<<<256, 256, 0, stream>>>(x, wo, ws);
  k1_main<<<256, 512, 163840, stream>>>(wqkv, w1, w2, ws);
  k2_fin<<<288, 256, 0, stream>>>(x, ws, (u16*)d_out);
}
```

```cpp
#include <hip/hip_runtime.h>
#include <utility>

typedef unsigned short u16;
typedef __bf16 bf16x2 __attribute__((ext_vector_type(2)));
typedef __bf16 bf16x4 __attribute__((ext_vector_type(4)));
typedef __bf16 bf16x8 __attribute__((ext_vector_type(8)));
typedef short s16x2 __attribute__((ext_vector_type(2)));
typedef float f32x2 __attribute__((ext_vector_type(2)));
typedef float f32x4 __attribute__((ext_vector_type(4)));
typedef unsigned u32x2 __attribute__((ext_vector_type(2)));
typedef unsigned u32x4 __attribute__((ext_vector_type(4)));
#define LDSP(T) __attribute__((address_space(3))) T*
#define DI __device__ __forceinline__
#define GAP() __builtin_amdgcn_sched_barrier(0)
template <int V> using IC = std::integral_constant<int, V>;
template <class F, int... Is> DI void sfor_(F&& f, std::integer_sequence<int, Is...>) { (f(IC<Is>{}), ...); }
template <int N, class F> DI void sfor(F&& f) { sfor_(f, std::make_integer_sequence<int, N>{}); }

constexpr int SEQ = 2048, DM = 576, DFF = 4608;
constexpr unsigned XN_BYTES = SEQ * DM * 2;
constexpr size_t PART_OFF = 0;
constexpr unsigned PART_BYTES = 16u * XN_BYTES;
constexpr size_t FLAG_OFF = PART_OFF + PART_BYTES;
constexpr size_t EPOCH_OFF = FLAG_OFF + 16 * 9 * 16 * 8;
typedef unsigned long long u64;
typedef __attribute__((address_space(1))) u64 gu64;
#define RLX_AGENT __ATOMIC_RELAXED, __HIP_MEMORY_SCOPE_AGENT
#define EXP_W 0
#define ST_AUX 16

DI unsigned cvtpk(float lo, float hi) { f32x2 v = {lo, hi}; return __builtin_bit_cast(unsigned, __builtin_convertvector(v, bf16x2)); }
DI unsigned relupk(unsigned p) { s16x2 z = {0, 0}; return __builtin_bit_cast(unsigned, __builtin_elementwise_max(__builtin_bit_cast(s16x2, p), z)); }
DI float bflo(unsigned p) { return __builtin_bit_cast(float, p << 16); }
DI float bfhi(unsigned p) { return __builtin_bit_cast(float, p & 0xffff0000u); }
DI float dot2(unsigned a, unsigned b, float c) { return __builtin_amdgcn_fdot2_f32_bf16(__builtin_bit_cast(bf16x2, a), __builtin_bit_cast(bf16x2, b), c, false); }
DI bf16x4 lds_tr(unsigned addr) { return __builtin_amdgcn_ds_read_tr16_b64_v4bf16((LDSP(bf16x4))(size_t)addr); }
DI bf16x8 lds_rd128(unsigned addr) { return *(LDSP(bf16x8))(size_t)addr; }
DI u32x2 lds_rd64(unsigned addr) { return *(LDSP(u32x2))(size_t)addr; }
DI void lds_wr128(unsigned addr, u32x4 v) { *(LDSP(u32x4))(size_t)addr = v; }
DI void lds_wr64(unsigned addr, u32x2 v) { *(LDSP(u32x2))(size_t)addr = v; }
DI bf16x8 cat(bf16x4 a, bf16x4 b) { return __builtin_shufflevector(a, b, 0, 1, 2, 3, 4, 5, 6, 7); }
DI f32x4 mfma(bf16x8 a, bf16x8 b, f32x4 c) { return __builtin_amdgcn_mfma_f32_16x16x32_bf16(a, b, c, 0, 0, 0); }
DI void mma(f32x4& c, bf16x8 a, bf16x8 b) { asm volatile("v_mfma_f32_16x16x32_bf16 %0, %1, %2, %0" : "+v"(c) : "v"(a), "v"(b)); }
DI void mma0(f32x4& c, bf16x8 a, bf16x8 b) { asm volatile("v_mfma_f32_16x16x32_bf16 %0, %1, %2, 0" : "=&v"(c) : "v"(a), "v"(b)); }
DI void mma16(f32x4& c, bf16x4 a, u32x2 b) { asm volatile("v_mfma_f32_16x16x16_bf16 %0, %1, %2, %0" : "+v"(c) : "v"(a), "v"(b)); }
template <int N> DI void prio() {}
template <int N> DI void nop() { asm volatile("s_nop %0" ::"i"(N)); }
template <int N> DI void waitv() { asm volatile("s_waitcnt vmcnt(%0)" ::"i"(N) : "memory"); }
DI void waitl0() { asm volatile("s_waitcnt lgkmcnt(0)" ::: "memory"); }
DI void bar() { asm volatile("s_barrier" ::: "memory"); }
DI u32x4 make_srd(const void* p, unsigned bytes) {
  size_t a = (size_t)p; u32x4 s;
  s[0] = __builtin_amdgcn_readfirstlane((unsigned)a); s[1] = __builtin_amdgcn_readfirstlane((unsigned)(a >> 32));
  s[2] = bytes; s[3] = 0x00020000u; return s;
}
DI void dma(unsigned lds_dst, unsigned voff, u32x4 srd, unsigned soff) {
  asm volatile("s_mov_b32 m0, %0\n\ts_nop 0\n\tbuffer_load_dwordx4 %1, %2, %3 offen lds" ::"s"(lds_dst), "v"(voff), "s"(srd), "s"(soff) : "memory");
}
template <int CTRL> DI float dppadd(float v) { return v + __builtin_bit_cast(float, __builtin_amdgcn_update_dpp(0, __builtin_bit_cast(int, v), CTRL, 0xF, 0xF, true)); }
DI float red8(float v) { v = dppadd<0xB1>(v); v = dppadd<0x4E>(v); return dppadd<0x141>(v); }
DI float fmx(float a, float b) { return __builtin_elementwise_maximum(a, b); }
DI void swap16(float& a, float& b) {
  auto r = __builtin_amdgcn_permlane16_swap(__builtin_bit_cast(unsigned, a), __builtin_bit_cast(unsigned, b), false, false);
  const unsigned r0 = r[0], r1 = r[1]; a = __builtin_bit_cast(float, r0); b = __builtin_bit_cast(float, r1);
}
DI void swap32(float& a, float& b) {
  auto r = __builtin_amdgcn_permlane32_swap(__builtin_bit_cast(unsigned, a), __builtin_bit_cast(unsigned, b), false, false);
  const unsigned r0 = r[0], r1 = r[1]; a = __builtin_bit_cast(float, r0); b = __builtin_bit_cast(float, r1);
}
template <class T> DI void pin(T& v) { asm volatile("" : "+v"(v)); }
DI float pack_max(float a, float b, float c, float d) { swap16(a, b); a = fmx(a, b); swap16(c, d); c = fmx(c, d); swap32(a, c); return fmx(a, c); }
DI float pack_sum(float a, float b, float c, float d) { swap16(a, b); a = a + b; swap16(c, d); c = c + d; swap32(a, c); return a + c; }
DI void unpack4(float x, float& a, float& b, float& c, float& d) { a = x; c = x; swap32(a, c); b = a; swap16(a, b); d = c; swap16(c, d); }
template <int OFF> DI u32x4 gload(unsigned voff, u32x4 srd, unsigned soff) {
  u32x4 r; asm volatile("buffer_load_dwordx4 %0, %1, %2, %3 offen offset:%4" : "=v"(r) : "v"(voff), "s"(srd), "s"(soff), "i"(OFF)); return r;
}
template <int N> DI void waitv_tie(u32x4& r) { asm volatile("s_waitcnt vmcnt(%1)" : "+v"(r) : "i"(N)); }
DI u64 next_tag(u64 e) { return (e ^ 0x9E3779B97F4A7C15ull) * 0xD1342543DE82EF95ull + 0x632BE59BD9B4E019ull; }
DI u64 rd_epoch(char* ws, int rt) {
  u64 e; asm volatile("global_load_dwordx2 %0, %1, off sc1" : "=v"(e) : "v"((gu64*)(ws + EPOCH_OFF) + rt * 16) : "memory"); return e;
}
DI u64 epoch_uniform(u64 e) {
  asm volatile("" : "+v"(e));
  return ((u64)(unsigned)__builtin_amdgcn_readfirstlane((unsigned)(e >> 32)) << 32) | (unsigned)__builtin_amdgcn_readfirstlane((unsigned)e);
}
DI void set_flag(char* ws, int rt, int n, int src, u64 tag) {
  __hip_atomic_store((gu64*)(ws + FLAG_OFF) + (rt * 9 + n) * 16 + src, tag, RLX_AGENT);
}
typedef short s16x4 __attribute__((ext_vector_type(4)));
struct Stat { f32x4 s1, s2; };
DI void stat_acc(const u32x4& c, Stat& st) {
  const s16x4 ones = {0x3f80, 0x3f80, 0x3f80, 0x3f80};
  const s16x4 a = __builtin_bit_cast(s16x4, (u32x2){c[0], c[1]}), b = __builtin_bit_cast(s16x4, (u32x2){c[2], c[3]});
  st.s1 = __builtin_amdgcn_mfma_f32_4x4x4bf16_1k(a, ones, st.s1, 0, 0, 0);
  st.s2 = __builtin_amdgcn_mfma_f32_4x4x4bf16_1k(a, a, st.s2, 0, 0, 0);
  st.s1 = __builtin_amdgcn_mfma_f32_4x4x4bf16_1k(b, ones, st.s1, 0, 0, 0);
  st.s2 = __builtin_amdgcn_mfma_f32_4x4x4bf16_1k(b, b, st.s2, 0, 0, 0);
}
DI void stat_fin(const Stat& st, int lane, float& r, float& nmr) {
  const float s1 = dppadd<0x141>((st.s1[0] + st.s1[1]) + (st.s1[2] + st.s1[3]));
  const float d01 = (lane & 1) ? st.s2[1] : st.s2[0], d23 = (lane & 1) ? st.s2[3] : st.s2[2];
  const float s2 = red8((lane & 2) ? d23 : d01);
  const float m = s1 * (1.f / DM);
  const float var = fmaxf(s2 * (1.f / DM) - m * m, 0.f);
  r = __builtin_amdgcn_rsqf(var + 1e-5f); nmr = -m * r;
}
struct NormSt { float lo, hi; u32x4 o; };
template <int O> DI void norm_op(NormSt& st, const u32x4& c0, const u32x4& c1, const float (&rs)[2], const float (&nm)[2], unsigned addr) {
  constexpr int R = O / 21, Q = O % 21, J = Q / 5, K = Q % 5;
  const u32x4& c = R ? c1 : c0;
  if constexpr (Q == 20) lds_wr128(addr + R * 128, st.o);
  else if constexpr (K == 0) { st.lo = bflo(c[J]); pin(st.lo); }
  else if constexpr (K == 1) { st.hi = bfhi(c[J]); pin(st.hi); }
  else if constexpr (K == 2) { st.lo = __builtin_fmaf(st.lo, rs[R], nm[R]); pin(st.lo); }
  else if constexpr (K == 3) { st.hi = __builtin_fmaf(st.hi, rs[R], nm[R]); pin(st.hi); }
  else { unsigned t = cvtpk(st.lo, st.hi); pin(t); st.o[J] = t; }
}
DI unsigned norm2(unsigned p, float r, float nmr) { return cvtpk(__builtin_fmaf(bflo(p), r, nmr), __builtin_fmaf(bfhi(p), r, nmr)); }

#define STAMP_DECL do {} while (0)
#define STAMP(idx) do {} while (0)
#define STAMP_FLUSH(kid) do {} while (0)

constexpr int p2_first(int j) { return j < 2 ? 0 : (j <= 7 ? 2 * j : j + 8); }
constexpr int p2_count(int j) { return j < 2 ? 0 : (j <= 7 ? 2 : (j + 8 <= 26 ? 1 : 0)); }
constexpr int vm_allowed(int T, int need) {
  int after = 0;
  auto op = [&](int tag, int n) { if (tag == need) after = 0; else after += n; };
  op(0, 3); op(1, 3); op(2, 3);
  if (T < 0) return after;
  for (int t = 0; t < 18; t++) {
    if (t <= 14) op(t + 3, 3);
    else if (t == 15) { op(100, 2); op(101, 2); }
    else if (t == 16) op(102, 2);
    else op(103, 2);
    if (t == T) return after;
  }
  if (T == 18) return after;
  for (int j = 0; j < 27; j++) {
    for (int c = 0; c < p2_count(j); c++) op(100 + p2_first(j) + c, 2);
    if (j >= 3 && j % 3 == 0) op(200 + j / 3 - 1, 2);
    if (19 + j == T) return after;
  }
  return 0;
}
constexpr int imin(int a, int b) { return a < b ? a : b; }
constexpr int p2_wait(int j) {
  int a = j < 26 ? vm_allowed(19 + j, 100 + j + 1) : 63;
  if (j >= 5 && j % 3 == 2) a = imin(a, vm_allowed(19 + j, 200 + j / 3 - 1));
  return a;
}
constexpr unsigned w1_slot(int s) { constexpr int pi[5] = {2, 0, 1, 4, 3}; return 32768u + 24576u * pi[s % 5]; }
constexpr unsigned w2_slot(int j) { constexpr int pi[10] = {8, 9, 7, 6, 0, 1, 2, 3, 4, 5}; return 16384u * pi[j % 10]; }

DI void ffn_path(const u16* x, const u16* w1, const u16* w2, char* ws, u16* out, int rt, int sp) {
  const int tid = threadIdx.x, lane = tid & 63;
  const int w = __builtin_amdgcn_readfirstlane(tid >> 6), tg = w >> 2, cg = w & 3;
  const int g = lane >> 4, q = (lane & 15) >> 2, p = lane & 3;
  STAMP_DECL; STAMP(0);
  const u32x4 srdx = make_srd(x, XN_BYTES), srd1 = make_srd(w1, DM * DFF * 2), srd2 = make_srd(w2, DM * DFF * 2);
  const int i8 = lane >> 3, c8 = lane & 7;
  const unsigned vox = i8 * (DM * 2) + c8 * 16;
  const unsigned sox = (unsigned)((rt * 128 + 16 * w) * (DM * 2));
  u64 epoch = rd_epoch(ws, rt);
  u32x4 xr[2][9];
  sfor<9>([&](auto tc) { constexpr int t = decltype(tc)::value;
    xr[0][t] = gload<t * 128>(vox, srdx, sox); xr[1][t] = gload<t * 128>(vox, srdx, sox + 8 * DM * 2); });
  const unsigned swz = 16u * (c8 ^ (2 * ((lane >> 4) & 3)));
  const unsigned vo1 = (8 * (i8 >> 2) + (i8 & 3)) * (DFF * 2) + swz, vo2 = i8 * (DM * 2) + swz;
  unsigned so1[3];
#pragma unroll
  for (int i = 0; i < 3; i++) { const int id = 3 * w + i, rg = id & 3; so1[i] = (unsigned)((16 * (rg & 1) + 4 * (rg >> 1)) * (DFF * 2) + (sp * 384 + (id >> 2) * 64) * 2); }
  const unsigned so2 = (unsigned)((sp * 384 + 16 * w) * (DM * 2));
  const int grp = sp >> 2, rr = sp & 3;
  int chunk[9];
#pragma unroll
  for (int pp = 0; pp < 9; pp++)
    chunk[pp] = pp < 6 ? 3 * ((grp + 1 + pp / 3) % 3) + (pp + rr) % 3 : 3 * grp + ((0x918486u >> (6 * rr + 2 * (pp - 6))) & 3);
  const unsigned m1 = (unsigned)(3 * w * 1024), m2 = (unsigned)(2 * w * 1024);
  auto load1 = [&](auto Sc, auto Ic) {
    constexpr int S = decltype(Sc)::value, I = decltype(Ic)::value;
    dma(w1_slot(S) + m1 + I * 1024, vo1, srd1, so1[I] + (unsigned)(S * 32 * DFF * 2));
  };
  auto load2 = [&](auto Jc, auto Ic) {
    constexpr int J = decltype(Jc)::value, I = decltype(Ic)::value;
    dma(w2_slot(J) + m2 + I * 1024, vo2, srd2, so2 + (unsigned)(((J % 3) * 128 + 8 * I) * (DM * 2)) + (unsigned)chunk[J / 3] * 128);
  };
  sfor<3>([&](auto s) { sfor<3>([&](auto i) { load1(s, i); }); });
  float rs[2], nm[2];
  {
    Stat st[2] = {};
    sfor<18>([&](auto ic) { constexpr int i = decltype(ic)::value;
      waitv_tie<9 + 17 - i>(xr[i & 1][i >> 1]); stat_acc(xr[i & 1][i >> 1], st[i & 1]); });
    stat_fin(st[0], lane, rs[0], nm[0]); stat_fin(st[1], lane, rs[1], nm[1]);
    epoch = epoch_uniform(epoch);
  }
  const unsigned xw = (c8 >> 2) * 8192 + w * 1024 + (16 * (c8 & 3) + (i8 ^ c8)) * 16;
  NormSt ns;
  auto xop = [&](auto Tc, auto Oc) {
    constexpr int T = decltype(Tc)::value, O = decltype(Oc)::value;
    if constexpr (T < 9 && O < 42) norm_op<O>(ns, xr[0][T < 9 ? T : 0], xr[1][T < 9 ? T : 0], rs, nm, xw + (T & 1) * 16384);
  };
  sfor<2>([&](auto r) { constexpr int R = decltype(r)::value;
    lds_wr128(xw + R * 128, (u32x4){norm2(xr[R][0][0], rs[R], nm[R]), norm2(xr[R][0][1], rs[R], nm[R]), norm2(xr[R][0][2], rs[R], nm[R]), norm2(xr[R][0][3], rs[R], nm[R])}); });
  sfor<12>([&](auto o) { xop(IC<1>{}, o); });

  const int row = 4 * g + q;
  const unsigned wb = row * 128 + 32 * (cg ^ ((row >> 1) & 3)) + 8 * p;
  unsigned wbh = wb + 65536, wbh2 = wb + 131072;
  pin(wbh); pin(wbh2);
  auto waddr = [&](auto Oc) { constexpr unsigned O = decltype(Oc)::value; return O < 65536 ? wb + O : (O < 131072 ? wbh + (O - 65536) : wbh2 + (O - 131072)); };
  const unsigned xb0 = tg * 4096 + (16 * g + ((lane & 15) ^ g)) * 16, xb1 = xb0 ^ 64;

  f32x4 acc[6][4];
  bf16x8 xf[2][4];
  bf16x4 wlo[4], whi[4];
  auto rdw1 = [&](auto GIc, auto Hc) {
    constexpr int GI = decltype(GIc)::value, H = decltype(Hc)::value;
    constexpr int O = w1_slot(GI / 6) + (GI % 6) * 4096 + H * 2048;
    if constexpr (H == 0) wlo[GI & 3] = lds_tr(waddr(IC<O>{})); else whi[GI & 3] = lds_tr(waddr(IC<O>{}));
  };
  auto rdx = [&](auto Sc, auto Tc) {
    constexpr int S = decltype(Sc)::value, T = decltype(Tc)::value;
    xf[S & 1][T] = lds_rd128(((S & 1) ? xb1 : xb0) + (S & 3) * 8192 + T * 1024);
  };
  waitv<vm_allowed(-1, 0)>(); waitl0(); bar();
  STAMP(1);
  rdw1(IC<0>{}, IC<0>{}); rdw1(IC<0>{}, IC<1>{});
  sfor<4>([&](auto t) { rdx(IC<0>{}, t); });
  rdw1(IC<1>{}, IC<0>{}); rdw1(IC<1>{}, IC<1>{}); rdw1(IC<2>{}, IC<0>{}); rdw1(IC<2>{}, IC<1>{});
  const unsigned hw = lane * 16 + (tg * 16 + cg) * 1024;
  unsigned hwh = hw + 65536; pin(hwh);
  auto hpack = [&](auto Uc, auto Tc) {
    constexpr int U = decltype(Uc)::value, T = decltype(Tc)::value;
    u32x4 o;
    o[0] = relupk(cvtpk(acc[2 * U][T][0], acc[2 * U][T][1])); o[1] = relupk(cvtpk(acc[2 * U][T][2], acc[2 * U][T][3]));
    o[2] = relupk(cvtpk(acc[2 * U + 1][T][0], acc[2 * U + 1][T][1])); o[3] = relupk(cvtpk(acc[2 * U + 1][T][2], acc[2 * U + 1][T][3]));
    lds_wr128((U == 2 ? hwh : hw + U * 32768) + T * 4096, o);
  };
  GAP();
  sfor<108>([&](auto GIc) {
    constexpr int GI = decltype(GIc)::value, S = GI / 6, SB = GI % 6;
    const bf16x8 a = cat(wlo[GI & 3], whi[GI & 3]);
    sfor<4>([&](auto Tc) {
      constexpr int T = decltype(Tc)::value;
      if constexpr (S == 0) mma0(acc[SB][T], a, xf[0][T]); else mma(acc[SB][T], a, xf[S & 1][T]);
      GAP();
      if constexpr (SB == 3 && T == 0) {
        if constexpr (S < 17) { waitv<vm_allowed(S, S + 1)>(); if constexpr ((S & 1) == 1) asm volatile("s_waitcnt lgkmcnt(4)" ::: "memory"); bar(); }
        else { waitl0(); bar(); }
      }
      {
        constexpr int GP = SB * 4 + T;
        if constexpr ((S & 1) == 1 && SB >= 3) xop(IC<(S + 3) / 2>{}, IC<GP - 12>{});
        else if constexpr ((S & 1) == 0) { xop(IC<S / 2 + 1>{}, IC<GP + 12 + (GP > 8) + (GP > 16)>{}); if constexpr (GP == 8 || GP == 16) xop(IC<S / 2 + 1>{}, IC<GP + 13 + (GP > 8)>{}); }
        else if constexpr (GP < 4) xop(IC<(S + 1) / 2>{}, IC<GP + 38>{});
      }
      if constexpr (T < 2 && GI + 3 < 108) rdw1(IC<GI + 3>{}, Tc);
      if constexpr (T == 2 && SB < 3) {
        if constexpr (S <= 14) load1(IC<S + 3>{}, IC<SB % 3>{});
        else if constexpr (S == 15) { load2(IC<(SB >> 1)>{}, IC<(SB & 1)>{}); if constexpr (SB == 2) load2(IC<1>{}, IC<1>{}); }
        else if constexpr (SB < 2) load2(IC<S - 14>{}, IC<SB>{});
      }
      if constexpr (S < 17 && T >= 2 && (SB == 3 || SB == 4)) rdx(IC<S + 1>{}, IC<(SB - 3) * 2 + (T - 2)>{});
      if constexpr (S == 17 && (SB == 3 || SB == 5)) hpack(IC<(SB - 3) / 2>{}, Tc);
      GAP();
    });
  });
  STAMP(2);
  nop<7>(); GAP();
  sfor<4>([&](auto t) { hpack(IC<2>{}, t); });
  const u64 tag = next_tag(epoch);
  waitv<vm_allowed(18, 100)>(); waitl0(); bar();
  STAMP(3);

  bf16x8 hf[4][12];
  f32x4 oc[4];
  const unsigned hb = lane * 16 + tg * 16384; unsigned hbh = hb + 65536; pin(hbh);
  auto rdh = [&](auto Ic, auto Tc) {
    constexpr int I = decltype(Ic)::value, T = decltype(Tc)::value; constexpr int U = I >> 2;
    hf[T][I] = lds_rd128((U == 2 ? hbh : hb + U * 32768) + T * 4096 + (I & 3) * 1024);
  };
  auto rdw2 = [&](auto GIc, auto Hc) {
    constexpr int GI = decltype(GIc)::value, H = decltype(Hc)::value;
    constexpr int O = w2_slot(GI >> 2) + (GI & 3) * 2048 + H * 8192;
    if constexpr (H == 0) wlo[GI & 3] = lds_tr(waddr(IC<O>{})); else whi[GI & 3] = lds_tr(waddr(IC<O>{}));
  };
  rdw2(IC<0>{}, IC<0>{}); rdw2(IC<0>{}, IC<1>{});
  sfor<4>([&](auto t) { rdh(IC<0>{}, t); });
  rdw2(IC<1>{}, IC<0>{}); rdw2(IC<1>{}, IC<1>{});
  sfor<4>([&](auto t) { rdh(IC<1>{}, t); });
  rdw2(IC<2>{}, IC<0>{}); rdw2(IC<2>{}, IC<1>{});
  const __amdgpu_buffer_rsrc_t prs = __builtin_amdgcn_make_buffer_rsrc(ws + PART_OFF, 0, PART_BYTES, 0x00020000);
  const unsigned pso = (unsigned)(((rt * 9) * 16 + sp) * 16384 + w * 2048);
  unsigned pk[8];
  GAP();
  sfor<108>([&](auto GIc) {
    constexpr int GI = decltype(GIc)::value, J = GI >> 2, C = GI & 3, I = GI % 12, N = GI / 12;
    if constexpr (GI == 12) STAMP(4);
    if constexpr (GI == 36) STAMP(5);
    const bf16x8 a = cat(wlo[GI & 3], whi[GI & 3]);
    sfor<4>([&](auto Tc) {
      constexpr int T = decltype(Tc)::value;
      if constexpr (I == 0 && N > 0) { if constexpr (T == 0) { nop<2>(); GAP(); } pk[2 * T] = cvtpk(oc[T][0], oc[T][1]); pk[2 * T + 1] = cvtpk(oc[T][2], oc[T][3]); GAP(); }
      if constexpr (I == 0) mma0(oc[T], a, hf[T][I]); else mma(oc[T], a, hf[T][I]);
      GAP();
      if constexpr (C == 1 && T == 0) {
        waitv<p2_wait(J)>(); bar();
        if constexpr (J >= 5 && J % 3 == 2) { if (tid == 0) set_flag(ws, rt, chunk[J / 3 - 1], sp, tag); }
      }
      if constexpr (GI + 3 < 108 && T < 2) rdw2(IC<GI + 3>{}, Tc);
      if constexpr (GI + 2 < 12) rdh(IC<GI + 2>{}, Tc);
      if constexpr (I == 0 && N > 0 && (T & 1)) {
        __builtin_amdgcn_raw_buffer_store_b128((u32x4){pk[2 * T - 2], pk[2 * T - 1], pk[2 * T], pk[2 * T + 1]}, prs, lane * 16 + (T >> 1) * 1024, pso + (unsigned)chunk[N > 0 ? N - 1 : 0] * (16 * 16384), ST_AUX);
      }
      if constexpr (C >= 2 && J < 26) {
        constexpr int cnt = p2_count(J + 1);
        if constexpr (cnt == 1) { if constexpr (T == 2) load2(IC<p2_first(J + 1)>{}, IC<C - 2>{}); }
        if constexpr (cnt == 2) { if constexpr (T >= 2) load2(IC<p2_first(J + 1) + C - 2>{}, IC<T - 2>{}); }
      }
      GAP();
    });
  });
  nop<7>(); GAP();
  sfor<2>([&](auto hc) {
    constexpr int H = decltype(hc)::value;
    u32x4 o = {cvtpk(oc[2 * H][0], oc[2 * H][1]), cvtpk(oc[2 * H][2], oc[2 * H][3]), cvtpk(oc[2 * H + 1][0], oc[2 * H + 1][1]), cvtpk(oc[2 * H + 1][2], oc[2 * H + 1][3])};
    __builtin_amdgcn_raw_buffer_store_b128(o, prs, lane * 16 + H * 1024, pso + (unsigned)chunk[8] * (16 * 16384), ST_AUX);
  });
  STAMP(6);
  const int c16 = lane & 15;
  unsigned poff[3], ooff[3]; u32x2 xv[3];
#pragma unroll
  for (int i = 0; i < 3; i++) {
    const int sg = 6 * w + 2 * i, pc = 3 * sp + (sg >> 4), n = pc >> 2;
    poff[i] = (unsigned)((rt * 9 + n) * (16 * 16384) + ((pc >> 1) & 1) * 8192 + ((sg >> 2) & 3) * 2048 + (pc & 1) * 1024 + (sg & 3) * 256);
    ooff[i] = (unsigned)((rt * 128 + (pc & 3) * 32 + (g & 1) * 16 + c16) * DM + n * 64 + (sg & 15) * 4 + (g >> 1) * 4);
    xv[i] = *(const u32x2*)(x + ooff[i]);
  }
  waitv<0>(); bar();
  STAMP(7);
  if (tid == 0) set_flag(ws, rt, chunk[8], sp, tag);
  {
    const int nA = (3 * sp) >> 2, nB = (3 * sp + 2) >> 2;
    const gu64* fp = (const gu64*)(ws + FLAG_OFF) + (rt * 9 + ((lane & 16) ? nB : nA)) * 16 + c16;
    for (int spin = 0; spin < 200000; spin++) {
      const u64 f = __hip_atomic_load(fp, RLX_AGENT);
      if (__all(f == tag || c16 == sp)) break;
      __builtin_amdgcn_s_sleep(2);
    }
    __builtin_amdgcn_fence(__ATOMIC_ACQUIRE, "wavefront");
  }
  STAMP(8);
  u32x4 pv[6][4];
  const unsigned pvo = g * 16384 + c16 * 16;
#pragma unroll
  for (int t = 0; t < 4; t++)
#pragma unroll
    for (int i = 0; i < 6; i++) pv[i][t] = __builtin_amdgcn_raw_buffer_load_b128(prs, pvo, poff[i >> 1] + (i & 1) * 256 + t * 65536, 16);
  u32x4 sel;
#pragma unroll
  for (int j = 0; j < 4; j++) sel[j] = ((c16 & 7) >> 1) == j ? ((c16 & 1) ? 0x3f800000u : 0x00003f80u) : 0u;
  const u32x4 z4 = {0u, 0u, 0u, 0u};
  const bf16x8 af = __builtin_bit_cast(bf16x8, c16 < 8 ? sel : z4), as = __builtin_bit_cast(bf16x8, c16 < 8 ? z4 : sel);
  f32x4 dd[6];
#pragma unroll
  for (int i = 0; i < 6; i++) {
    dd[i] = mfma(af, __builtin_bit_cast(bf16x8, pv[i][0]), (f32x4){0.f, 0.f, 0.f, 0.f});
    dd[i] = mfma(af, __builtin_bit_cast(bf16x8, pv[i][1]), dd[i]);
    dd[i] = mfma(af, __builtin_bit_cast(bf16x8, pv[i][2]), dd[i]);
    dd[i] = mfma(as, __builtin_bit_cast(bf16x8, pv[i][3]), dd[i]);
  }
  STAMP(9);
#pragma unroll
  for (int i = 0; i < 3; i++) {
    float o[4];
#pragma unroll
    for (int j = 0; j < 4; j++) {
      float fv = dd[2 * i][j], sv = dd[2 * i + 1][j];
      swap32(fv, sv);
      const float gt = fv * __builtin_amdgcn_rcpf(1.f + __builtin_amdgcn_exp2f(-1.4426950408889634f * fv));
      o[j] = ((j & 1) ? bfhi(xv[i][j >> 1]) : bflo(xv[i][j >> 1])) + sv * gt;
    }
    *(u32x2*)(out + ooff[i]) = (u32x2){cvtpk(o[0], o[1]), cvtpk(o[2], o[3])};
  }
  if (sp == 0 && tid == 0) __hip_atomic_store((gu64*)(ws + EPOCH_OFF) + rt * 16, tag, RLX_AGENT);
  STAMP_FLUSH(1);
}

constexpr unsigned A_SLOT = 27648, A_XR = 110592, A_HALO = 143360, A_WOH = 125952, A_IMG = 872, A_OB = 125568, A_OS = 296;
constexpr unsigned wo_slot(int n) { return n < 6 ? 18432u * n : (n < 8 ? A_WOH + 18432u * (n - 6) : 0u); }

DI void att_path(const u16* x, const u16* wqkv, const u16* wo, char* ws, int rt, int hg) {
  const int tid = threadIdx.x, lane = tid & 63;
  const int w = __builtin_amdgcn_readfirstlane(tid >> 6), tg = w >> 2, cg = w & 3;
  const int g = lane >> 4, q = (lane & 15) >> 2, p = lane & 3, c16 = lane & 15;
  STAMP_DECL; STAMP(0);
  const u32x4 srdx = make_srd(x, XN_BYTES), srdw = make_srd(wqkv, DM * 3 * DM * 2), srdo = make_srd(wo, DM * DM * 2);
  const int i8 = lane >> 3, c8 = lane & 7;
  const unsigned vox = i8 * (DM * 2) + c8 * 16;
  const unsigned sox = (unsigned)((rt * 128 + 16 * w) * (DM * 2));
  u64 epoch = rd_epoch(ws, rt);
  const bool hal = (w & 6) == 4;
  u32x4 xr[2][9], xh[9];
  if (hal) {
    const unsigned soh = (unsigned)((rt > 0 ? rt * 128 - 16 + 8 * (w & 1) : 0) * (DM * 2));
    sfor<9>([&](auto tc) { constexpr int t = decltype(tc)::value; xh[t] = gload<t * 128>(vox, srdx, soh); });
  }
  sfor<9>([&](auto tc) { constexpr int t = decltype(tc)::value;
    xr[0][t] = gload<t * 128>(vox, srdx, sox); xr[1][t] = gload<t * 128>(vox, srdx, sox + 8 * DM * 2); });
  unsigned vw[4];
#pragma unroll
  for (int i = 0; i < 4; i++) {
    const int n = (w + 8 * i) * 64 + lane, r = n / 54, c = n % 54;
    const int k = 8 * ((r >> 2) & 3) + 4 * (r >> 4) + (r & 3);
    vw[i] = k * 3456 + (c / 18) * 1152 + hg * 288 + (c % 18) * 16;
  }
  auto loadw = [&](auto Sc, auto Ic) {
    constexpr int S = decltype(Sc)::value, I = decltype(Ic)::value;
    if (I < 3 || w < 3) dma((S & 3) * A_SLOT + (w + 8 * I) * 1024, vw[I], srdw, (unsigned)(S * 32 * 3456));
  };
  sfor<2>([&](auto s) { sfor<4>([&](auto i) { loadw(s, i); }); });
  const unsigned xw0 = (c8 >> 2) * 8192 + (16 * (c8 & 3) + (i8 ^ c8)) * 16;
  if (hal) {
    Stat st = {}; float r, nmr;
    sfor<9>([&](auto ic) { constexpr int i = decltype(ic)::value; waitv_tie<6 + 18 + 8 - i>(xh[i]); stat_acc(xh[i], st); });
    stat_fin(st, lane, r, nmr);
    const unsigned hwa = A_HALO + (c8 >> 2) * 1024 + (16 * (c8 & 3) + (i8 ^ c8)) * 16 + (w & 1) * 128;
    sfor<9>([&](auto ic) { constexpr int i = decltype(ic)::value;
      lds_wr128(hwa + i * 2048, (u32x4){norm2(xh[i][0], r, nmr), norm2(xh[i][1], r, nmr), norm2(xh[i][2], r, nmr), norm2(xh[i][3], r, nmr)}); });
  }
  float rs[2], nm[2];
  {
    Stat st[2] = {};
    sfor<18>([&](auto ic) { constexpr int i = decltype(ic)::value;
      waitv_tie<6 + 17 - i>(xr[i & 1][i >> 1]); stat_acc(xr[i & 1][i >> 1], st[i & 1]); });
    stat_fin(st[0], lane, rs[0], nm[0]); stat_fin(st[1], lane, rs[1], nm[1]);
    epoch = epoch_uniform(epoch);
  }
  unsigned xw = A_XR + xw0 + w * 1024; pin(xw);
  NormSt ns;
  auto xop = [&](auto Tc, auto Oc) {
    constexpr int T = decltype(Tc)::value, O = decltype(Oc)::value;
    if constexpr (T < 9 && O < 42) norm_op<O>(ns, xr[0][T < 9 ? T : 0], xr[1][T < 9 ? T : 0], rs, nm, xw + (T & 1) * 16384);
  };
  sfor<2>([&](auto r) { constexpr int R = decltype(r)::value;
    lds_wr128(xw + R * 128, (u32x4){norm2(xr[R][0][0], rs[R], nm[R]), norm2(xr[R][0][1], rs[R], nm[R]), norm2(xr[R][0][2], rs[R], nm[R]), norm2(xr[R][0][3], rs[R], nm[R])}); });

  const unsigned wb = (4 * g + q) * 864 + 32 * (7 * cg + tg) + 8 * p;
  const unsigned wrap6 = tg * 224;
  unsigned wbh = wb + 2 * A_SLOT, wb6 = wb - wrap6, wbh6 = wbh - wrap6; pin(wbh); pin(wb6); pin(wbh6);
  const unsigned xs = (16 * g + (c16 ^ g)) * 16;
  unsigned xb0 = A_XR + tg * 4096 + xs, xb1 = xb0 ^ 64, hb0 = A_HALO + xs, hb1 = hb0 ^ 64; pin(xb0); pin(xb1); pin(hb0); pin(hb1);
  f32x4 acc[7][4], hac[4];
  bf16x8 xf[4], xhf;
  bf16x4 wlo[4], whi[4];
  auto rdw = [&](auto GIc, auto Hc) {
    constexpr int GI = decltype(GIc)::value, H = decltype(Hc)::value, S = GI / 7, C = GI % 7;
    if constexpr (GI < 126) {
      const unsigned a = (C == 6 ? ((S & 2) ? wbh6 : wb6) : ((S & 2) ? wbh : wb)) + (S & 1) * A_SLOT + 32 * C + H * 16 * 864;
      if constexpr (H == 0) wlo[GI & 3] = lds_tr(a); else whi[GI & 3] = lds_tr(a);
    }
  };
  auto rdx = [&](auto Sc, auto Tc) {
    constexpr int S = decltype(Sc)::value, T = decltype(Tc)::value;
    xf[T] = lds_rd128(((S & 1) ? xb1 : xb0) + (S & 3) * 8192 + T * 1024);
  };
  auto rdxh = [&](auto Sc) { constexpr int S = decltype(Sc)::value; xhf = lds_rd128(((S & 1) ? hb1 : hb0) + S * 1024); };
  waitv<3>(); waitl0(); bar();
  STAMP(1);
  rdw(IC<0>{}, IC<0>{}); rdw(IC<0>{}, IC<1>{}); rdw(IC<1>{}, IC<0>{}); rdw(IC<1>{}, IC<1>{});
  sfor<4>([&](auto t) { rdx(IC<0>{}, t); });
  rdxh(IC<0>{});
  const f32x4 zero4 = {0.f, 0.f, 0.f, 0.f};
  GAP();
  sfor<18>([&](auto Sc) {
    constexpr int S = decltype(Sc)::value, G0 = 7 * S;
    auto fill = [&](auto GPc) {
      constexpr int GP = decltype(GPc)::value;
      GAP();
      if constexpr (GP == 23) {
        if constexpr (S < 16) waitv<3>(); else if constexpr (S == 16) waitv<0>(); else waitl0();
        bar();
      }
      if constexpr ((S & 1) == 0) xop(IC<S / 2 + 1>{}, GPc); else xop(IC<(S + 1) / 2>{}, IC<32 + GP>{});
      if constexpr (GP < 4) rdw(IC<G0 + 2 + (GP >> 1)>{}, IC<(GP & 1)>{});
      if constexpr (GP == 9 || GP == 10) rdw(IC<G0 + 4>{}, IC<GP - 9>{});
      if constexpr (GP == 11 || GP == 12) rdw(IC<G0 + 5>{}, IC<GP - 11>{});
      if constexpr (GP == 14 || GP == 15) rdw(IC<G0 + 6>{}, IC<GP - 14>{});
      if constexpr (GP == 23 || GP == 24) rdw(IC<G0 + 7>{}, IC<GP - 23>{});
      if constexpr (GP == 26 || GP == 28) rdw(IC<G0 + 8>{}, IC<(GP - 26) / 2>{});
      if constexpr (S < 17) {
        if constexpr (GP >= 25 && (GP & 1) == 1) rdx(IC<S + 1>{}, IC<(GP - 25) / 2>{});
        if constexpr (GP == 30) rdxh(IC<S + 1>{});
      }
      if constexpr (S + 2 < 18 && (GP == 5 || GP == 7 || GP == 16 || GP == 20)) loadw(IC<S + 2>{}, IC<(GP == 5 ? 0 : (GP == 7 ? 1 : (GP == 16 ? 2 : 3)))>{});
      GAP();
    };
    auto mm = [&](f32x4& c, const bf16x8& a, const bf16x8& b) { if constexpr (S == 0) mma0(c, a, b); else mma(c, a, b); };
    const bf16x8 a0 = cat(wlo[G0 & 3], whi[G0 & 3]), a1 = cat(wlo[(G0 + 1) & 3], whi[(G0 + 1) & 3]);
    sfor<4>([&](auto Tc) { constexpr int T = decltype(Tc)::value;
      mm(acc[0][T], a0, xf[T]); fill(IC<2 * T>{}); mm(acc[1][T], a1, xf[T]); fill(IC<2 * T + 1>{}); });
    mm(hac[0], a0, xhf); fill(IC<8>{});
    const bf16x8 a2 = cat(wlo[(G0 + 2) & 3], whi[(G0 + 2) & 3]);
    sfor<4>([&](auto Tc) { constexpr int T = decltype(Tc)::value; mm(acc[2][T], a2, xf[T]); fill(IC<9 + T>{}); });
    mm(hac[1], a2, xhf); fill(IC<13>{});
    const bf16x8 a3 = cat(wlo[(G0 + 3) & 3], whi[(G0 + 3) & 3]);
    sfor<4>([&](auto Tc) { constexpr int T = decltype(Tc)::value; mm(acc[3][T], a3, xf[T]); fill(IC<14 + T>{}); });
    const bf16x8 a4 = cat(wlo[(G0 + 4) & 3], whi[(G0 + 4) & 3]);
    sfor<4>([&](auto Tc) { constexpr int T = decltype(Tc)::value; mm(acc[4][T], a4, xf[T]); fill(IC<18 + T>{}); });
    mm(hac[2], a4, xhf); fill(IC<22>{});
    const bf16x8 a5 = cat(wlo[(G0 + 5) & 3], whi[(G0 + 5) & 3]), a6 = cat(wlo[(G0 + 6) & 3], whi[(G0 + 6) & 3]);
    mm(hac[3], a6, xhf); fill(IC<23>{});
    sfor<4>([&](auto Tc) { constexpr int T = decltype(Tc)::value;
      mm(acc[5][T], a5, xf[T]); fill(IC<24 + 2 * T>{}); mm(acc[6][T], a6, xf[T]); fill(IC<25 + 2 * T>{}); });
  });
  STAMP(2);
  const u64 tag = next_tag(epoch);
  const unsigned vo2 = i8 * (DM * 2) + 16u * (c8 ^ (2 * ((lane >> 4) & 3)));
  const unsigned soo = (unsigned)((hg * 144 + 16 * w) * (DM * 2));
  auto loado = [&](auto Nc, auto Ic) {
    constexpr int N = decltype(Nc)::value, I = decltype(Ic)::value;
    if constexpr (I < 2) dma(wo_slot(N) + (2 * w + I) * 1024, vo2, srdo, soo + I * 8 * DM * 2 + N * 128);
    else if (w < 2) dma(wo_slot(N) + (16 + w) * 1024, vo2, srdo, (unsigned)((hg * 144 + 128 + 8 * w) * (DM * 2)) + N * 128);
  };
  nop<3>(); GAP();
  {
    const unsigned ih = c16 * A_IMG + 32 * (7 * cg + tg) + 8 * g, ib = ih + (16 + tg * 64) * A_IMG;
    sfor<7>([&](auto Cc) {
      constexpr int C = decltype(Cc)::value;
      if (C < 5 || cg < 3 || C + tg != 6) {
        const unsigned wr = 32 * C - (C == 6 ? wrap6 : 0u);
        sfor<4>([&](auto Tc) { constexpr int T = decltype(Tc)::value;
          lds_wr64(ib + wr + T * 16 * A_IMG, (u32x2){cvtpk(acc[C][T][0], acc[C][T][1]), cvtpk(acc[C][T][2], acc[C][T][3])}); });
        if constexpr ((C & 1) == 0) lds_wr64(ih + wr, (u32x2){cvtpk(hac[C >> 1][0], hac[C >> 1][1]), cvtpk(hac[C >> 1][2], hac[C >> 1][3])});
      }
    });
  }
  GAP();
  {
    const int hp = w >> 1, par = w & 1;
    const unsigned r0 = (2 * c16 + par + 16) * A_IMG + 72 * hp, rm = (unsigned)max(2 * c16 + par - 16, 0) * A_IMG + 72 * hp + 288;
    unsigned rq[3] = {r0 + 8 * g, r0 + 8 * g + 40, r0 + 32};
    unsigned rk[4] = {rq[0] + 288, rq[1] + 288, rq[2] + 288, rq[2] + 288};
    unsigned rkm[4] = {rm + 8 * g, rm + 8 * g + 40, rm + 32, rm + 32};
    const int kr = 4 * g + q;
    unsigned rv = (2 * kr + par + 16) * A_IMG + 576 + 72 * hp + 8 * p, rvp = rv - 32 * A_IMG, rv3 = rv + 96 * A_IMG;
    unsigned rvm = (unsigned)max(2 * kr + par - 16, 0) * A_IMG + 576 + 72 * hp + 8 * p;
    unsigned rq3[3], rk3[4];
#pragma unroll
    for (int i = 0; i < 4; i++) { if (i < 3) { rq3[i] = rq[i] + 96 * A_IMG; pin(rq[i]); pin(rq3[i]); } rk3[i] = rk[i] + 96 * A_IMG; pin(rk[i]); pin(rk3[i]); pin(rkm[i]); }
    pin(rv); pin(rvp); pin(rv3); pin(rvm);
    f32x4 biasA, biasA0, biasB; unsigned mA[2], mA0[2], mB[2];
#pragma unroll
    for (int j = 0; j < 4; j++) {
      const int kk = 4 * g + j; const bool va = kk >= c16 + 9, vb = (kk <= c16) && (kk + 7 >= c16);
      biasA[j] = va ? 0.f : -1e30f; biasB[j] = vb ? 0.f : -1e30f; biasA0[j] = (va && rt > 0) ? 0.f : -1e30f;
      const unsigned bm = (j & 1) ? 0xffff0000u : 0x0000ffffu;
      if ((j & 1) == 0) { mA[j >> 1] = 0; mB[j >> 1] = 0; }
      mA[j >> 1] |= va ? bm : 0u; mB[j >> 1] |= vb ? bm : 0u;
    }
    mA0[0] = rt > 0 ? mA[0] : 0u; mA0[1] = rt > 0 ? mA[1] : 0u;
    pin(biasA); pin(biasA0); pin(biasB); pin(mA[0]); pin(mA[1]); pin(mA0[0]); pin(mA0[1]); pin(mB[0]); pin(mB[1]);
    unsigned qmask = g == 0 ? 0xffffffffu : 0u; pin(qmask);
    const unsigned ow0 = A_OB + (2 * c16 + par) * A_OS + 72 * hp;
    waitl0(); bar();
    STAMP(3);
    auto asb = [](u32x2 a, u32x2 b) { return __builtin_bit_cast(bf16x8, (u32x4){a[0], a[1], b[0], b[1]}); };
    bf16x8 kf[5][2], vf[4][3]; u32x2 qm[4][2], q4[4];
    sfor<2>([&](auto Hc) { constexpr int H = decltype(Hc)::value; kf[0][H] = asb(lds_rd64(rkm[H]), lds_rd64(rkm[2 + H])); });
    sfor<4>([&](auto Uc) { constexpr int U = decltype(Uc)::value; constexpr unsigned ro = U < 3 ? U * 32 * A_IMG : 0;
      q4[U] = lds_rd64((U < 3 ? rq[2] : rq3[2]) + ro);
      sfor<2>([&](auto Hc) { constexpr int H = decltype(Hc)::value;
        qm[U][H] = lds_rd64((U < 3 ? rq[H] : rq3[H]) + ro);
        kf[U + 1][H] = asb(lds_rd64((U < 3 ? rk[H] : rk3[H]) + ro), lds_rd64((U < 3 ? rk[2 + H] : rk3[2 + H]) + ro)); }); });
    GAP();
    sfor<4>([&](auto Uc) { constexpr int U = decltype(Uc)::value;
      sfor<3>([&](auto Dc) { constexpr int D = decltype(Dc)::value; constexpr unsigned co = D == 0 ? 0 : (D == 1 ? 40 : 32);
        vf[U][D] = cat(U == 0 ? lds_tr(rvm + co) : lds_tr(rvp + U * 32 * A_IMG - (U == 3 ? 0 : 0) + co), U < 3 ? lds_tr(rv + U * 32 * A_IMG + co) : lds_tr(rv3 + co)); }); });
    GAP();
    f32x4 sa[8], sb[8];
    sfor<8>([&](auto Ic) { constexpr int I = decltype(Ic)::value, U = I >> 1, H = I & 1;
      const bf16x8 qf = asb(qm[U][H], H ? (u32x2){0u, q4[U][1] & qmask} : (u32x2){q4[U][0] & qmask, 0u});
      sa[I] = mfma(kf[U][H], qf, U == 0 ? biasA0 : biasA); sb[I] = mfma(kf[U + 1][H], qf, biasB); });
    GAP();
    waitl0(); bar();
    STAMP(4);
    GAP();
    float v[8][4], mx[8];
    sfor<8>([&](auto Ic) { constexpr int I = decltype(Ic)::value;
#pragma unroll
      for (int j = 0; j < 4; j++) v[I][j] = fmx(sa[I][j], sb[I][j]);
      mx[I] = fmx(fmx(fmx(v[I][0], v[I][1]), v[I][2]), v[I][3]);
      if constexpr (I < 6) { GAP(); loado(IC<I / 3>{}, IC<I % 3>{}); GAP(); } });
    constexpr float SC = 0.23570226039551584f * 1.4426950408889634f;
    sfor<2>([&](auto hc) { constexpr int B = 4 * decltype(hc)::value;
      unpack4(-SC * pack_max(mx[B], mx[B + 1], mx[B + 2], mx[B + 3]), mx[B], mx[B + 1], mx[B + 2], mx[B + 3]); });
    GAP();
    float sm[8];
    sfor<8>([&](auto Ic) { constexpr int I = decltype(Ic)::value;
#pragma unroll
      for (int j = 0; j < 4; j++) v[I][j] = __builtin_amdgcn_exp2f(__builtin_fmaf(v[I][j], SC, mx[I]));
      sm[I] = (v[I][0] + v[I][1]) + (v[I][2] + v[I][3]);
      if constexpr (I < 6) { GAP(); loado(IC<2 + I / 3>{}, IC<I % 3>{}); GAP(); } });
    sfor<2>([&](auto hc) { constexpr int B = 4 * decltype(hc)::value;
      unpack4(__builtin_amdgcn_rcpf(pack_sum(sm[B], sm[B + 1], sm[B + 2], sm[B + 3])), sm[B], sm[B + 1], sm[B + 2], sm[B + 3]); });
    GAP();
    STAMP(5);
    f32x4 o0[8], o1[8];
    sfor<8>([&](auto Ic) { constexpr int I = decltype(Ic)::value, U = I >> 1, H = I & 1;
      const unsigned p0 = cvtpk(v[I][0] * sm[I], v[I][1] * sm[I]), p1 = cvtpk(v[I][2] * sm[I], v[I][3] * sm[I]);
      const bf16x8 pf = __builtin_bit_cast(bf16x8, (u32x4){p0 & (U == 0 ? mA0[0] : mA[0]), p1 & (U == 0 ? mA0[1] : mA[1]), p0 & mB[0], p1 & mB[1]});
      o0[I] = mfma(vf[U][H], pf, zero4);
      o1[I] = mfma(vf[U][2], pf, zero4);
      if constexpr (I < 6) { GAP(); loado(IC<4 + I / 3>{}, IC<I % 3>{}); GAP(); } });
    GAP();
    sfor<8>([&](auto Ic) { constexpr int I = decltype(Ic)::value, U = I >> 1, H = I & 1;
      lds_wr64(ow0 + 8 * g + H * 40 + U * 32 * A_OS, (u32x2){cvtpk(o0[I][0], o0[I][1]), cvtpk(o0[I][2], o0[I][3])}); });
    if (g == 0) {
      sfor<4>([&](auto Uc) { constexpr int U = decltype(Uc)::value;
        lds_wr64(ow0 + 32 + U * 32 * A_OS, (u32x2){cvtpk(o1[2 * U][0], o1[2 * U][1]), cvtpk(o1[2 * U + 1][2], o1[2 * U + 1][3])}); });
    }
  }
  waitv<10>(); waitl0(); bar();
  STAMP(6);
  const int row = 4 * g + q;
  const unsigned ow = row * 128 + 32 * (cg ^ ((row >> 1) & 3)) + 8 * p; unsigned owh = ow + 65536, owh2 = ow + A_WOH; pin(owh); pin(owh2);
  auto rdo = [&](auto GIc, auto Hc) {
    constexpr int GI = decltype(GIc)::value, H = decltype(Hc)::value, N = GI / 5, S = GI % 5;
    constexpr unsigned O = wo_slot(N) + S * 4096 + H * 2048;
    const unsigned a = O >= A_WOH ? owh2 + (O - A_WOH) : (O >= 65536 ? owh + (O - 65536) : ow + O);
    if constexpr (H == 0) wlo[GI & 3] = lds_tr(a); else if constexpr (S < 4) whi[GI & 3] = lds_tr(a);
  };
  bf16x8 of[4][4]; u32x2 of4[4];
  {
    unsigned ob = A_OB + (tg * 64 + c16) * A_OS + 8 * g, ob2 = ob + 32; pin(ob); pin(ob2);
    sfor<5>([&](auto Sc) { constexpr int S = decltype(Sc)::value;
      if constexpr (S < 3) { rdo(Sc, IC<0>{}); rdo(Sc, IC<1>{}); }
      sfor<4>([&](auto Tc) { constexpr int T = decltype(Tc)::value;
        const u32x2 lo = lds_rd64(ob + T * 16 * A_OS + 64 * S);
        if constexpr (S < 4) {
          const u32x2 hi = lds_rd64(ob2 + T * 16 * A_OS + 64 * S);
          of[T][S & 3] = __builtin_bit_cast(bf16x8, (u32x4){lo[0], lo[1], hi[0], hi[1]});
        } else of4[T] = lo;
      }); });
  }
  const __amdgpu_buffer_rsrc_t prs = __builtin_amdgcn_make_buffer_rsrc(ws + PART_OFF, 0, PART_BYTES, 0x00020000);
  const unsigned pso = (unsigned)(((rt * 9) * 16 + 12 + hg) * 16384 + w * 2048);
  f32x4 oc[4]; unsigned pk[8];
  GAP();
  sfor<45>([&](auto GIc) {
    constexpr int GI = decltype(GIc)::value, N = GI / 5, S = GI % 5;
    if constexpr (GI == 5) STAMP(7);
    if constexpr (GI == 15) STAMP(8);
    if constexpr (GI == 30) STAMP(9);
    const bf16x4 alo = wlo[GI & 3];
    bf16x8 a; if constexpr (S < 4) a = cat(alo, whi[GI & 3]);
    sfor<4>([&](auto Tc) {
      constexpr int T = decltype(Tc)::value;
      if constexpr (S == 0 && N > 0) { if constexpr (T == 0) { nop<2>(); GAP(); } pk[2 * T] = cvtpk(oc[T][0], oc[T][1]); pk[2 * T + 1] = cvtpk(oc[T][2], oc[T][3]); GAP(); }
      if constexpr (S == 0) mma0(oc[T], a, of[T][0]); else if constexpr (S < 4) mma(oc[T], a, of[T][S & 3]); else mma16(oc[T], alo, of4[T]);
      GAP();
      if constexpr (S == 2 && T == 0) {
        constexpr int need = N + 1;
        constexpr int a_ld = N == 8 ? 63 : (need <= 5 ? 2 * (5 - need) + 2 * N + (N >= 1 ? 4 : 0) + (N >= 2 ? 2 : 0)
                                                      : (need == 6 ? 2 + 2 + 2 * (N - 1) : (need == 7 ? 2 + 2 * (N - 1) : 2 * (N - 2))));
        constexpr int a_st = N >= 3 ? 2 : 63;
        if constexpr (N == 0) waitl0();
        waitv<imin(a_ld, a_st)>(); bar();
        if constexpr (N >= 3) { if (tid == 0) { set_flag(ws, rt, N - 2, 12 + hg, tag); if constexpr (N == 3) set_flag(ws, rt, 0, 12 + hg, tag); } }
      }
      if constexpr (GI + 3 < 45 && T < 2) rdo(IC<GI + 3>{}, Tc);
      if constexpr (S == 0 && N > 0 && (T & 1))
        __builtin_amdgcn_raw_buffer_store_b128((u32x4){pk[2 * T - 2], pk[2 * T - 1], pk[2 * T], pk[2 * T + 1]}, prs, lane * 16 + (T >> 1) * 1024, pso + (N - 1) * 16 * 16384, ST_AUX);
      if constexpr (N == 1 && S == 1 && T < 3) loado(IC<6>{}, Tc);
      if constexpr (N == 1 && S == 1 && T == 3) { loado(IC<7>{}, IC<0>{}); loado(IC<7>{}, IC<1>{}); loado(IC<7>{}, IC<2>{}); }
      if constexpr (N == 2 && S == 1 && T < 3) loado(IC<8>{}, Tc);
      GAP();
    });
  });
  nop<7>(); GAP();
  sfor<2>([&](auto hc) {
    constexpr int H = decltype(hc)::value;
    u32x4 o = {cvtpk(oc[2 * H][0], oc[2 * H][1]), cvtpk(oc[2 * H][2], oc[2 * H][3]), cvtpk(oc[2 * H + 1][0], oc[2 * H + 1][1]), cvtpk(oc[2 * H + 1][2], oc[2 * H + 1][3])};
    __builtin_amdgcn_raw_buffer_store_b128(o, prs, lane * 16 + H * 1024, pso + 8 * 16 * 16384, ST_AUX);
  });
  STAMP(10);
  waitv<0>(); bar();
  if (tid == 0) { set_flag(ws, rt, 7, 12 + hg, tag); set_flag(ws, rt, 8, 12 + hg, tag); }
  STAMP(11); STAMP_FLUSH(1);
}

__global__ __launch_bounds__(512, 2) void k1_main(const u16* __restrict__ x, const u16* __restrict__ wqkv, const u16* __restrict__ wo,
                                                  const u16* __restrict__ w1, const u16* __restrict__ w2, char* __restrict__ ws, u16* __restrict__ out) {
  const int b = blockIdx.x, xc = b & 7, i = b >> 3;
  const int rt = 4 * (xc >> 1) + (i & 3), j = i >> 2;
  if (j < 6) ffn_path(x, w1, w2, ws, out, rt, 6 * (xc & 1) + j);
  else att_path(x, wqkv, wo, ws, rt, 2 * (xc & 1) + (j - 6));
}

extern "C" void kernel_launch(void* const* d_in, const int* in_sizes, int n_in, void* d_out, int out_size, void* d_ws, size_t ws_size, hipStream_t stream) {
  const u16* x = (const u16*)d_in[0]; const u16* wqkv = (const u16*)d_in[1]; const u16* wo = (const u16*)d_in[2];
  const u16* w1 = (const u16*)d_in[4]; const u16* w2 = (const u16*)d_in[5];
  (void)hipFuncSetAttribute((const void*)k1_main, hipFuncAttributeMaxDynamicSharedMemorySize, 163840);
  k1_main<<<256, 512, 163840, stream>>>(x, wqkv, wo, w1, w2, (char*)d_ws, (u16*)d_out);
}
```

```cpp
#include <hip/hip_runtime.h>
#include <utility>

typedef unsigned short u16;
typedef __bf16 bf16x2 __attribute__((ext_vector_type(2)));
typedef __bf16 bf16x4 __attribute__((ext_vector_type(4)));
typedef __bf16 bf16x8 __attribute__((ext_vector_type(8)));
typedef short s16x2 __attribute__((ext_vector_type(2)));
typedef float f32x2 __attribute__((ext_vector_type(2)));
typedef float f32x4 __attribute__((ext_vector_type(4)));
typedef unsigned u32x2 __attribute__((ext_vector_type(2)));
typedef unsigned u32x4 __attribute__((ext_vector_type(4)));
#define LDSP(T) __attribute__((address_space(3))) T*
#define DI __device__ __forceinline__
#define GAP() __builtin_amdgcn_sched_barrier(0)
template <int V> using IC = std::integral_constant<int, V>;
template <class F, int... Is> DI void sfor_(F&& f, std::integer_sequence<int, Is...>) { (f(IC<Is>{}), ...); }
template <int N, class F> DI void sfor(F&& f) { sfor_(f, std::make_integer_sequence<int, N>{}); }

constexpr int SEQ = 2048, DM = 576, DFF = 4608;
constexpr unsigned XN_BYTES = SEQ * DM * 2;
constexpr size_t PART_OFF = 0;
constexpr unsigned PART_BYTES = 16u * XN_BYTES;
constexpr size_t FLAG_OFF = PART_OFF + PART_BYTES;
constexpr size_t EPOCH_OFF = FLAG_OFF + 16 * 9 * 16 * 8;
typedef unsigned long long u64;
typedef __attribute__((address_space(1))) u64 gu64;
#define RLX_AGENT __ATOMIC_RELAXED, __HIP_MEMORY_SCOPE_AGENT
#define TAIL_SLEEP 16
#define ST_AUX 16

DI unsigned cvtpk(float lo, float hi) { f32x2 v = {lo, hi}; return __builtin_bit_cast(unsigned, __builtin_convertvector(v, bf16x2)); }
DI unsigned relupk(unsigned p) { s16x2 z = {0, 0}; return __builtin_bit_cast(unsigned, __builtin_elementwise_max(__builtin_bit_cast(s16x2, p), z)); }
DI float bflo(unsigned p) { return __builtin_bit_cast(float, p << 16); }
DI float bfhi(unsigned p) { return __builtin_bit_cast(float, p & 0xffff0000u); }
DI bf16x4 lds_tr(unsigned addr) { return __builtin_amdgcn_ds_read_tr16_b64_v4bf16((LDSP(bf16x4))(size_t)addr); }
DI bf16x8 lds_rd128(unsigned addr) { return *(LDSP(bf16x8))(size_t)addr; }
typedef bf16x8 bf16x8a8 __attribute__((aligned(8)));
DI bf16x8 lds_rd128a8(unsigned addr) { return *(LDSP(bf16x8a8))(size_t)addr; }
DI u32x2 lds_rd64(unsigned addr) { return *(LDSP(u32x2))(size_t)addr; }
DI void lds_wr128(unsigned addr, u32x4 v) { *(LDSP(u32x4))(size_t)addr = v; }
DI void lds_wr64(unsigned addr, u32x2 v) { *(LDSP(u32x2))(size_t)addr = v; }
DI bf16x8 cat(bf16x4 a, bf16x4 b) { return __builtin_shufflevector(a, b, 0, 1, 2, 3, 4, 5, 6, 7); }
DI f32x4 mfma(bf16x8 a, bf16x8 b, f32x4 c) { return __builtin_amdgcn_mfma_f32_16x16x32_bf16(a, b, c, 0, 0, 0); }
DI void mma(f32x4& c, bf16x8 a, bf16x8 b) { asm volatile("v_mfma_f32_16x16x32_bf16 %0, %1, %2, %0" : "+v"(c) : "v"(a), "v"(b)); }
DI void mma0(f32x4& c, bf16x8 a, bf16x8 b) { asm volatile("v_mfma_f32_16x16x32_bf16 %0, %1, %2, 0" : "=&v"(c) : "v"(a), "v"(b)); }
DI void mma16(f32x4& c, bf16x4 a, u32x2 b) { asm volatile("v_mfma_f32_16x16x16_bf16 %0, %1, %2, %0" : "+v"(c) : "v"(a), "v"(b)); }
DI void mma_not3(f32x4& c, bf16x8 a, bf16x8 b, int cg) {
  asm volatile("s_cmp_eq_u32 %3, 3\n\ts_cbranch_scc1 .Le%=\n\tv_mfma_f32_16x16x32_bf16 %0, %1, %2, %0\n.Le%=:" : "+v"(c) : "v"(a), "v"(b), "s"(cg) : "scc");
}
DI void mma_alt3(f32x4& c, bf16x8 a, bf16x8 b, f32x4& h, bf16x8 ah, bf16x8 bh, int cg) {
  asm volatile("s_cmp_eq_u32 %6, 3\n\ts_cbranch_scc1 .Lh%=\n\tv_mfma_f32_16x16x32_bf16 %0, %2, %3, %0\n\ts_branch .Le%=\n"
               ".Lh%=:\n\tv_mfma_f32_16x16x32_bf16 %1, %4, %5, %1\n.Le%=:" : "+v"(c), "+v"(h) : "v"(a), "v"(b), "v"(ah), "v"(bh), "s"(cg) : "scc");
}
template <int N> DI void nop() { asm volatile("s_nop %0" ::"i"(N)); }
template <int N> DI void waitv() { asm volatile("s_waitcnt vmcnt(%0)" ::"i"(N) : "memory"); }
DI void waitl0() { asm volatile("s_waitcnt lgkmcnt(0)" ::: "memory"); }
DI void bar() { asm volatile("s_barrier" ::: "memory"); }
DI u32x4 make_srd(const void* p, unsigned bytes) {
  size_t a = (size_t)p; u32x4 s;
  s[0] = __builtin_amdgcn_readfirstlane((unsigned)a); s[1] = __builtin_amdgcn_readfirstlane((unsigned)(a >> 32));
  s[2] = bytes; s[3] = 0x00020000u; return s;
}
DI void dma(unsigned lds_dst, unsigned voff, u32x4 srd, unsigned soff) {
  asm volatile("s_mov_b32 m0, %0\n\ts_nop 0\n\tbuffer_load_dwordx4 %1, %2, %3 offen lds" ::"s"(lds_dst), "v"(voff), "s"(srd), "s"(soff) : "memory");
}
DI void dma_sc1(unsigned lds_dst, unsigned voff, u32x4 srd, unsigned soff) {
  asm volatile("s_mov_b32 m0, %0\n\ts_nop 0\n\tbuffer_load_dwordx4 %1, %2, %3 offen sc1 lds" ::"s"(lds_dst), "v"(voff), "s"(srd), "s"(soff) : "memory");
}
template <int CTRL> DI float dppadd(float v) { return v + __builtin_bit_cast(float, __builtin_amdgcn_update_dpp(0, __builtin_bit_cast(int, v), CTRL, 0xF, 0xF, true)); }
DI float red8(float v) { v = dppadd<0xB1>(v); v = dppadd<0x4E>(v); return dppadd<0x141>(v); }
DI float fmx(float a, float b) { return __builtin_elementwise_maximum(a, b); }
DI void swap16(float& a, float& b) {
  auto r = __builtin_amdgcn_permlane16_swap(__builtin_bit_cast(unsigned, a), __builtin_bit_cast(unsigned, b), false, false);
  const unsigned r0 = r[0], r1 = r[1]; a = __builtin_bit_cast(float, r0); b = __builtin_bit_cast(float, r1);
}
DI void swap32(float& a, float& b) {
  auto r = __builtin_amdgcn_permlane32_swap(__builtin_bit_cast(unsigned, a), __builtin_bit_cast(unsigned, b), false, false);
  const unsigned r0 = r[0], r1 = r[1]; a = __builtin_bit_cast(float, r0); b = __builtin_bit_cast(float, r1);
}
template <class T> DI void pin(T& v) { asm volatile("" : "+v"(v)); }
DI float pack_max(float a, float b, float c, float d) { swap16(a, b); a = fmx(a, b); swap16(c, d); c = fmx(c, d); swap32(a, c); return fmx(a, c); }
DI float pack_sum(float a, float b, float c, float d) { swap16(a, b); a = a + b; swap16(c, d); c = c + d; swap32(a, c); return a + c; }
DI void unpack4(float x, float& a, float& b, float& c, float& d) { a = x; c = x; swap32(a, c); b = a; swap16(a, b); d = c; swap16(c, d); }
template <int OFF> DI u32x4 gload(unsigned voff, u32x4 srd, unsigned soff) {
  u32x4 r; asm volatile("buffer_load_dwordx4 %0, %1, %2, %3 offen offset:%4" : "=v"(r) : "v"(voff), "s"(srd), "s"(soff), "i"(OFF)); return r;
}
DI void touch(unsigned lds_dst, unsigned voff, u32x4 srd, unsigned soff) {
  asm volatile("s_mov_b32 m0, %0\n\ts_nop 0\n\tbuffer_load_dword %1, %2, %3 offen lds" ::"s"(lds_dst), "v"(voff), "s"(srd), "s"(soff) : "memory");
}
constexpr unsigned OOB = 0x7fffffffu;
DI u64 poll_load(const gu64* p) { u64 r; asm volatile("global_load_dwordx2 %0, %1, off sc1" : "=v"(r) : "v"(p) : "memory"); return r; }
template <int N> DI void waitv_tie(u64& r) { asm volatile("s_waitcnt vmcnt(%1)" : "+v"(r) : "i"(N)); }
template <int N> DI void waitv_tie(u32x4& r) { asm volatile("s_waitcnt vmcnt(%1)" : "+v"(r) : "i"(N)); }
constexpr unsigned POISON = 0xffffffffu;
DI unsigned nopoison(unsigned v) { return v < POISON - 1 ? v : POISON - 1; }
DI bool clean6(const u32x4 (&v)[6]) {
  unsigned m = 0;
#pragma unroll
  for (int i = 0; i < 6; i++) m = max(max(m, max(v[i][0], v[i][1])), max(v[i][2], v[i][3]));
  return __all(m != POISON) != 0;
}
DI u64 next_tag(u64 e) { return (e ^ 0x9E3779B97F4A7C15ull) * 0xD1342543DE82EF95ull + 0x632BE59BD9B4E019ull; }
DI u64 rd_epoch(char* ws, int rt) {
  return poll_load((const gu64*)(ws + EPOCH_OFF) + rt * 16);
}
DI u64 epoch_uniform(u64 e) {
  asm volatile("" : "+v"(e));
  return ((u64)(unsigned)__builtin_amdgcn_readfirstlane((unsigned)(e >> 32)) << 32) | (unsigned)__builtin_amdgcn_readfirstlane((unsigned)e);
}
DI void set_flag(char* ws, int rt, int n, int src, u64 tag) {
  __hip_atomic_store((gu64*)(ws + FLAG_OFF) + (rt * 9 + n) * 16 + src, tag, RLX_AGENT);
}
typedef short s16x4 __attribute__((ext_vector_type(4)));
struct Stat { f32x4 s1, s2; };
DI void stat_acc(const u32x4& c, Stat& st) {
  const s16x4 ones = {0x3f80, 0x3f80, 0x3f80, 0x3f80};
  const s16x4 a = __builtin_bit_cast(s16x4, (u32x2){c[0], c[1]}), b = __builtin_bit_cast(s16x4, (u32x2){c[2], c[3]});
  st.s1 = __builtin_amdgcn_mfma_f32_4x4x4bf16_1k(a, ones, st.s1, 0, 0, 0);
  st.s2 = __builtin_amdgcn_mfma_f32_4x4x4bf16_1k(a, a, st.s2, 0, 0, 0);
  st.s1 = __builtin_amdgcn_mfma_f32_4x4x4bf16_1k(b, ones, st.s1, 0, 0, 0);
  st.s2 = __builtin_amdgcn_mfma_f32_4x4x4bf16_1k(b, b, st.s2, 0, 0, 0);
}
template <bool WIDE> DI void stat_fin(const Stat& st, int lane, float& r, float& nmr) {
  float s1 = dppadd<0x141>((st.s1[0] + st.s1[1]) + (st.s1[2] + st.s1[3]));
  const float d01 = (lane & 1) ? st.s2[1] : st.s2[0], d23 = (lane & 1) ? st.s2[3] : st.s2[2];
  float s2 = red8((lane & 2) ? d23 : d01);
  if constexpr (WIDE) {
    s1 = dppadd<0x140>(s1); s2 = dppadd<0x140>(s2);
    float t1 = s1, t2 = s2; swap16(s1, t1); swap16(s2, t2); s1 += t1; s2 += t2;
  }
  const float m = s1 * (1.f / DM);
  const float var = fmaxf(s2 * (1.f / DM) - m * m, 0.f);
  r = __builtin_amdgcn_rsqf(var + 1e-5f); nmr = -m * r;
}
struct NormSt { float lo, hi; u32x4 o; };
template <int O> DI void norm_op(NormSt& st, const u32x4& c0, const u32x4& c1, const float (&rs)[2], const float (&nm)[2], unsigned addr) {
  constexpr int R = O / 21, Q = O % 21, J = Q / 5, K = Q % 5;
  const u32x4& c = R ? c1 : c0;
  if constexpr (Q == 20) lds_wr128(addr + R * 128, st.o);
  else if constexpr (K == 0) { st.lo = bflo(c[J]); pin(st.lo); }
  else if constexpr (K == 1) { st.hi = bfhi(c[J]); pin(st.hi); }
  else if constexpr (K == 2) { st.lo = __builtin_fmaf(st.lo, rs[R], nm[R]); pin(st.lo); }
  else if constexpr (K == 3) { st.hi = __builtin_fmaf(st.hi, rs[R], nm[R]); pin(st.hi); }
  else { unsigned t = cvtpk(st.lo, st.hi); pin(t); st.o[J] = t; }
}
DI unsigned norm2(unsigned p, float r, float nmr) { return cvtpk(__builtin_fmaf(bflo(p), r, nmr), __builtin_fmaf(bfhi(p), r, nmr)); }
DI u32x4 norm8(const u32x4& c, float r, float nmr) { return (u32x4){norm2(c[0], r, nmr), norm2(c[1], r, nmr), norm2(c[2], r, nmr), norm2(c[3], r, nmr)}; }

#define STAMP_DECL do {} while (0)
#define STAMP(idx) do {} while (0)
#define STAMP_FLUSH(kid) do {} while (0)

constexpr int p2_first(int j) { return j < 2 ? 0 : (j <= 7 ? 2 * j : j + 8); }
constexpr int p2_count(int j) { return j < 2 ? 0 : (j <= 7 ? 2 : (j + 8 <= 26 ? 1 : 0)); }
constexpr int POLL1 = 16, USE1 = 19, POLL2 = 25, LASTFLAG = 22;
constexpr int inbox_count(int j) { return (j >= USE1 + 1 && j <= USE1 + 3) ? 2 : 0; }
constexpr int f_touch(int t) { return t == 0 || t == 5 ? 1 : (t == 2 || t == 7 || t == 10 ? 2 : 0); }
constexpr int vm_allowed(int T, int need) {
  int after = 0;
  auto op = [&](int tag, int n) { if (tag == need) after = 0; else after += n; };
  op(0, 3); op(1, 3); op(2, 3); op(298, 1);
  if (T < 0) return after;
  for (int t = 0; t < 18; t++) {
    if (t <= 14) op(t + 3, 3);
    if (f_touch(t)) op(298, 1);
    if (t >= 8 && t < 14) op(297, 1);
    else if (t == 15) { op(100, 2); op(101, 2); }
    else if (t == 16) op(102, 2);
    else op(103, 2);
    if (t == T) return after;
  }
  if (T == 18) return after;
  for (int j = 0; j < 27; j++) {
    for (int c = 0; c < p2_count(j); c++) op(100 + p2_first(j) + c, 2);
    op(300, inbox_count(j));
    if (j >= 3 && j % 3 == 0) op(200 + j / 3 - 1, 2);
    if (j == POLL1) op(301, 1);
    if (j == POLL2) op(302, 1);
    if (19 + j == T) return after;
  }
  return 0;
}
constexpr int imin(int a, int b) { return a < b ? a : b; }
constexpr int p2_wait(int j) {
  int a = j < 26 ? vm_allowed(19 + j, 100 + j + 1) : 63;
  if (j >= 7 && j % 3 == 1 && j <= LASTFLAG) a = imin(a, vm_allowed(19 + j, 200 + j / 3 - 2));
  return a;
}
constexpr unsigned w1_slot(int s) { constexpr int pi[5] = {2, 0, 1, 4, 3}; return 32768u + 24576u * pi[s % 5]; }
constexpr unsigned w2_slot(int j) { constexpr int pi[10] = {8, 9, 7, 6, 0, 1, 2, 3, 4, 5}; return 16384u * pi[j % 10]; }
constexpr unsigned inbox_at(int a, int i) { return w2_slot(17 + 3 * a + i / 2) + (i & 1) * 1024u; }
constexpr unsigned F_OWN = w2_slot(23);
constexpr unsigned F_DUMMY = 159744;

DI void ffn_path(const u16* x, const u16* w1, const u16* w2, char* ws, u16* out, int rt, int sp) {
  const int tid = threadIdx.x, lane = tid & 63;
  const int w = __builtin_amdgcn_readfirstlane(tid >> 6), tg = w >> 2, cg = w & 3;
  const int g = lane >> 4, q = (lane & 15) >> 2, p = lane & 3;
  STAMP_DECL; STAMP(0);
  const u32x4 srdx = make_srd(x, XN_BYTES), srd1 = make_srd(w1, DM * DFF * 2), srd2 = make_srd(w2, DM * DFF * 2);
  const int i8 = lane >> 3, c8 = lane & 7;
  const unsigned vox = i8 * (DM * 2) + c8 * 16;
  const unsigned sox = (unsigned)((rt * 128 + 16 * w) * (DM * 2));
  u64 epoch = rd_epoch(ws, rt);
  u32x4 xr[2][9];
  sfor<9>([&](auto tc) { constexpr int t = decltype(tc)::value;
    xr[0][t] = gload<t * 128>(vox, srdx, sox); xr[1][t] = gload<t * 128>(vox, srdx, sox + 8 * DM * 2); });
  const unsigned swz = 16u * (c8 ^ (2 * ((lane >> 4) & 3)));
  const unsigned vo1 = (8 * (i8 >> 2) + (i8 & 3)) * (DFF * 2) + swz, vo2 = i8 * (DM * 2) + swz;
  unsigned so1[3];
#pragma unroll
  for (int i = 0; i < 3; i++) { const int id = 3 * w + i, rg = id & 3; so1[i] = (unsigned)((16 * (rg & 1) + 4 * (rg >> 1)) * (DFF * 2) + (sp * 384 + (id >> 2) * 64) * 2); }
  const unsigned so2 = (unsigned)((sp * 384 + 16 * w) * (DM * 2));
  const int grp = sp >> 2, rr = sp & 3;
  int chunk[9];
#pragma unroll
  for (int pp = 0; pp < 9; pp++)
    chunk[pp] = pp < 6 ? 3 * ((grp + 1 + pp / 3) % 3) + (pp + rr) % 3 : 3 * grp + ((0x918486u >> (6 * rr + 2 * (pp - 6))) & 3);
  const unsigned m1 = (unsigned)(3 * w * 1024), m2 = (unsigned)(2 * w * 1024);
  auto load1 = [&](auto Sc, auto Ic) {
    constexpr int S = decltype(Sc)::value, I = decltype(Ic)::value;
    dma(w1_slot(S) + m1 + I * 1024, vo1, srd1, so1[I] + (unsigned)(S * 32 * DFF * 2));
  };
  auto load2 = [&](auto Jc, auto Ic) {
    constexpr int J = decltype(Jc)::value, I = decltype(Ic)::value;
    dma(w2_slot(J) + m2 + I * 1024, vo2, srd2, so2 + (unsigned)(((J % 3) * 128 + 8 * I) * (DM * 2)) + (unsigned)chunk[J / 3] * 128);
  };
  sfor<3>([&](auto i) { load1(IC<0>{}, i); });
  const unsigned tv1 = (lane < 16 && c8 < 6 && w < 5) ? (unsigned)((w * 32 + 2 * rt) * (DFF * 2)) + i8 * (DFF * 2) + c8 * 128 : OOB, tv2 = lane < 9 ? lane * 128u : OOB;
  float rs[2], nm[2];
  {
    Stat st[2] = {};
    sfor<18>([&](auto ic) { constexpr int i = decltype(ic)::value;
      waitv_tie<3 + (i > 5 ? 3 : 0) + (i > 11 ? 4 : 0) + 17 - i>(xr[i & 1][i >> 1]); stat_acc(xr[i & 1][i >> 1], st[i & 1]); GAP();
      if constexpr (i == 5) sfor<3>([&](auto k) { load1(IC<1>{}, k); });
      if constexpr (i == 11) { sfor<3>([&](auto k) { load1(IC<2>{}, k); }); touch(F_DUMMY, tv1, srd1, (unsigned)(sp * 768 + 3 * 32 * DFF * 2)); } });
    stat_fin<false>(st[0], lane, rs[0], nm[0]); stat_fin<false>(st[1], lane, rs[1], nm[1]);
    epoch = epoch_uniform(epoch);
  }
  const unsigned xw = (c8 >> 2) * 8192 + w * 1024 + (16 * (c8 & 3) + (i8 ^ c8)) * 16;
  NormSt ns;
  auto xop = [&](auto Tc, auto Oc) {
    constexpr int T = decltype(Tc)::value, O = decltype(Oc)::value;
    if constexpr (T < 9 && O < 42) norm_op<O>(ns, xr[0][T < 9 ? T : 0], xr[1][T < 9 ? T : 0], rs, nm, xw + (T & 1) * 16384);
  };
  sfor<2>([&](auto r) { constexpr int R = decltype(r)::value;
    lds_wr128(xw + R * 128, norm8(xr[R][0], rs[R], nm[R])); });
  sfor<12>([&](auto o) { xop(IC<1>{}, o); });

  const int row = 4 * g + q;
  const unsigned wb = row * 128 + 32 * (cg ^ ((row >> 1) & 3)) + 8 * p;
  unsigned wbh = wb + 65536, wbh2 = wb + 131072;
  pin(wbh); pin(wbh2);
  auto waddr = [&](auto Oc) { constexpr unsigned O = decltype(Oc)::value; return O < 65536 ? wb + O : (O < 131072 ? wbh + (O - 65536) : wbh2 + (O - 131072)); };
  const unsigned xb0 = tg * 4096 + (16 * g + ((lane & 15) ^ g)) * 16, xb1 = xb0 ^ 64;

  const __amdgpu_buffer_rsrc_t prs = __builtin_amdgcn_make_buffer_rsrc(ws + PART_OFF, 0, PART_BYTES, 0x00020000);
  const unsigned pso = (unsigned)(((rt * 9) * 16 + sp) * 16384 + w * 2048);
  const u64 tag = next_tag(epoch);
  unsigned poff[3];
#pragma unroll
  for (int i = 0; i < 3; i++) {
    const int sg = 6 * w + 2 * i, pc = 3 * sp + (sg >> 4);
    poff[i] = (unsigned)((rt * 9 + (pc >> 2)) * (16 * 16384) + ((pc >> 1) & 1) * 8192 + ((sg >> 2) & 3) * 2048 + (pc & 1) * 1024 + (sg & 3) * 256);
    asm volatile("" : "+s"(poff[i]));
  }
  f32x4 acc[6][4];
  bf16x8 xf[2][4];
  bf16x4 wlo[4], whi[4];
  auto rdw1 = [&](auto GIc, auto Hc) {
    constexpr int GI = decltype(GIc)::value, H = decltype(Hc)::value;
    constexpr int O = w1_slot(GI / 6) + (GI % 6) * 4096 + H * 2048;
    if constexpr (H == 0) wlo[GI & 3] = lds_tr(waddr(IC<O>{})); else whi[GI & 3] = lds_tr(waddr(IC<O>{}));
  };
  auto rdx = [&](auto Sc, auto Tc) {
    constexpr int S = decltype(Sc)::value, T = decltype(Tc)::value;
    xf[S & 1][T] = lds_rd128(((S & 1) ? xb1 : xb0) + (S & 3) * 8192 + T * 1024);
  };
  waitv<vm_allowed(-1, 0)>(); waitl0(); bar();
  STAMP(1);
  rdw1(IC<0>{}, IC<0>{}); rdw1(IC<0>{}, IC<1>{});
  sfor<4>([&](auto t) { rdx(IC<0>{}, t); });
  rdw1(IC<1>{}, IC<0>{}); rdw1(IC<1>{}, IC<1>{}); rdw1(IC<2>{}, IC<0>{}); rdw1(IC<2>{}, IC<1>{});
  const unsigned hw = lane * 16 + (tg * 16 + cg) * 1024;
  unsigned hwh = hw + 65536; pin(hwh);
  auto hpack = [&](auto Uc, auto Tc) {
    constexpr int U = decltype(Uc)::value, T = decltype(Tc)::value;
    u32x4 o;
    o[0] = relupk(cvtpk(acc[2 * U][T][0], acc[2 * U][T][1])); o[1] = relupk(cvtpk(acc[2 * U][T][2], acc[2 * U][T][3]));
    o[2] = relupk(cvtpk(acc[2 * U + 1][T][0], acc[2 * U + 1][T][1])); o[3] = relupk(cvtpk(acc[2 * U + 1][T][2], acc[2 * U + 1][T][3]));
    lds_wr128((U == 2 ? hwh : hw + U * 32768) + T * 4096, o);
  };
  GAP();
  sfor<108>([&](auto GIc) {
    constexpr int GI = decltype(GIc)::value, S = GI / 6, SB = GI % 6;
    const bf16x8 a = cat(wlo[GI & 3], whi[GI & 3]);
    sfor<4>([&](auto Tc) {
      constexpr int T = decltype(Tc)::value;
      if constexpr (S == 0) mma0(acc[SB][T], a, xf[0][T]); else mma(acc[SB][T], a, xf[S & 1][T]);
      GAP();
      if constexpr (SB == 3 && T == 0) {
        if constexpr (S < 17) { waitv<vm_allowed(S, S + 1)>(); if constexpr ((S & 1) == 1) asm volatile("s_waitcnt lgkmcnt(4)" ::: "memory");
          bar(); }
        else { waitl0(); bar(); }
      }
      {
        constexpr int GP = SB * 4 + T;
        if constexpr ((S & 1) == 1 && SB >= 3) xop(IC<(S + 3) / 2>{}, IC<GP - 12>{});
        else if constexpr ((S & 1) == 0) { xop(IC<S / 2 + 1>{}, IC<GP + 12 + (GP > 8) + (GP > 16)>{}); if constexpr (GP == 8 || GP == 16) xop(IC<S / 2 + 1>{}, IC<GP + 13 + (GP > 8)>{}); }
        else if constexpr (GP < 4) xop(IC<(S + 1) / 2>{}, IC<GP + 38>{});
      }
      if constexpr (SB == 2 && T == 3 && f_touch(S) == 1) touch(F_DUMMY, tv1, srd1, (unsigned)(sp * 768 + (S + 8) * 32 * DFF * 2));
      if constexpr (SB == 2 && T == 3 && f_touch(S) == 2) touch(F_DUMMY, tv2, srd2, (unsigned)((sp * 384 + rt * 24 + w * 3 + (S == 2 ? 0 : (S == 7 ? 1 : 2))) * (DM * 2)));
      if constexpr (SB == 2 && T == 3 && S >= 8 && S < 14)
        __builtin_amdgcn_raw_buffer_store_b128((u32x4){POISON, POISON, POISON, POISON}, prs, lane * 16 + (S & 1) * 1024, pso + (unsigned)(3 * grp + (S - 8) / 2) * (16 * 16384), ST_AUX);
      if constexpr (T < 2 && GI + 3 < 108) rdw1(IC<GI + 3>{}, Tc);
      if constexpr (T == 2 && SB < 3) {
        if constexpr (S <= 14) load1(IC<S + 3>{}, IC<SB % 3>{});
        else if constexpr (S == 15) { load2(IC<(SB >> 1)>{}, IC<(SB & 1)>{}); if constexpr (SB == 2) load2(IC<1>{}, IC<1>{}); }
        else if constexpr (SB < 2) load2(IC<S - 14>{}, IC<SB>{});
      }
      if constexpr (S < 17 && T >= 2 && (SB == 3 || SB == 4)) rdx(IC<S + 1>{}, IC<(SB - 3) * 2 + (T - 2)>{});
      if constexpr (S == 17 && (SB == 3 || SB == 5)) hpack(IC<(SB - 3) / 2>{}, Tc);
      GAP();
    });
  });
  STAMP(2);
  nop<7>(); GAP();
  sfor<4>([&](auto t) { hpack(IC<2>{}, t); });
  waitv<vm_allowed(18, 100)>(); waitl0(); bar();
  if (tid < 3) set_flag(ws, rt, 3 * grp + tid, sp, tag);
  STAMP(3);

  bf16x8 hf[4][12];
  f32x4 oc[4];
  const unsigned hb = lane * 16 + tg * 16384; unsigned hbh = hb + 65536; pin(hbh);
  auto rdh = [&](auto Ic, auto Tc) {
    constexpr int I = decltype(Ic)::value, T = decltype(Tc)::value; constexpr int U = I >> 2;
    hf[T][I] = lds_rd128((U == 2 ? hbh : hb + U * 32768) + T * 4096 + (I & 3) * 1024);
  };
  auto rdw2 = [&](auto GIc, auto Hc) {
    constexpr int GI = decltype(GIc)::value, H = decltype(Hc)::value;
    constexpr int O = w2_slot(GI >> 2) + (GI & 3) * 2048 + H * 8192;
    if constexpr (H == 0) wlo[GI & 3] = lds_tr(waddr(IC<O>{})); else whi[GI & 3] = lds_tr(waddr(IC<O>{}));
  };
  rdw2(IC<0>{}, IC<0>{}); rdw2(IC<0>{}, IC<1>{});
  sfor<4>([&](auto t) { rdh(IC<0>{}, t); });
  rdw2(IC<1>{}, IC<0>{}); rdw2(IC<1>{}, IC<1>{});
  sfor<4>([&](auto t) { rdh(IC<1>{}, t); });
  rdw2(IC<2>{}, IC<0>{}); rdw2(IC<2>{}, IC<1>{});
  unsigned pk[8];
  const u32x4 srdp = make_srd(ws + PART_OFF, PART_BYTES);
  const int c16 = lane & 15;
  const unsigned pvo = g * 16384 + c16 * 16;
  const gu64* fp = (const gu64*)(ws + FLAG_OFF) + (rt * 9 + ((lane & 16) ? (3 * sp + 2) >> 2 : (3 * sp) >> 2)) * 16 + c16;
  const int gsrc[2] = {(grp + 1) % 3, (grp + 2) % 3};
  u64 fl; bool pre0; unsigned pso2;
  auto inbox = [&](auto Ac, auto Ic, unsigned so) {
    constexpr int A = decltype(Ac)::value, I = decltype(Ic)::value;
    if constexpr (I >= 0 && I < 6) dma_sc1(inbox_at(A, I) + m2, pvo, srdp, so + poff[I / 2] + (I & 1) * 256);
  };
  auto own_idx = [&](int pp, int half) { return (unsigned)(4 * chunk[pp] + 2 * tg + half - 3 * sp); };
  const unsigned own_w = F_OWN + cg * 1024 + lane * 16;
  auto ready = [&](u64 f, int gs) { return __all(f == tag || (c16 >> 2) != gs || c16 == sp) != 0; };
  GAP();
  sfor<108>([&](auto GIc) {
    constexpr int GI = decltype(GIc)::value, J = GI >> 2, C = GI & 3, I = GI % 12, N = GI / 12;
    if constexpr (GI == 12) STAMP(4);
    if constexpr (GI == 36) STAMP(5);
    const bf16x8 a = cat(wlo[GI & 3], whi[GI & 3]);
    sfor<4>([&](auto Tc) {
      constexpr int T = decltype(Tc)::value;
      if constexpr (I == 0 && N > 0) { if constexpr (T == 0) { nop<2>(); GAP(); } pk[2 * T] = cvtpk(oc[T][0], oc[T][1]); pk[2 * T + 1] = cvtpk(oc[T][2], oc[T][3]); GAP(); }
      if constexpr (I == 0) mma0(oc[T], a, hf[T][I]); else mma(oc[T], a, hf[T][I]);
      GAP();
      if constexpr (I == 0 && N > 6) { pk[2 * T] = nopoison(pk[2 * T]); pk[2 * T + 1] = nopoison(pk[2 * T + 1]); }
      if constexpr (C == 1 && T == 0 && J < 26) {
        waitv<p2_wait(J)>();
        bar();
        if constexpr (J >= 7 && J % 3 == 1 && J <= LASTFLAG) { if (tid == 0) set_flag(ws, rt, chunk[J >= 6 ? J / 3 - 2 : 0], sp, tag); }
      }
      if constexpr (C == 1 && T == 1 && J == USE1) {
        waitv_tie<vm_allowed(19 + J, 301)>(fl);
        pre0 = ready(fl, gsrc[0]);
        pso2 = pre0 ? (unsigned)gsrc[0] * 65536u : 0x80000000u;
      }
      if constexpr (C == 0 && T == 3 && (J == POLL1 || J == POLL2)) fl = poll_load(fp);
      if constexpr (GI + 3 < 108 && T < 2) rdw2(IC<GI + 3>{}, Tc);
      if constexpr (GI + 2 < 12) rdh(IC<GI + 2>{}, Tc);
      if constexpr (I == 0 && N > 0 && (T & 1)) {
        unsigned so = pso + (unsigned)chunk[N > 0 ? N - 1 : 0] * (16 * 16384);
        if constexpr (N == 8) so = own_idx(7, T >> 1) < 3u ? 0x80000000u : so;
        __builtin_amdgcn_raw_buffer_store_b128((u32x4){pk[2 * T - 2], pk[2 * T - 1], pk[2 * T], pk[2 * T + 1]}, prs, lane * 16 + (T >> 1) * 1024, so, ST_AUX);
      }
      if constexpr (I == 1 && N == 8 && T >= 2) {
        const unsigned oi = own_idx(7, T - 2);
        if (oi < 3u) lds_wr128(own_w + oi * 4096, (u32x4){pk[4 * T - 8], pk[4 * T - 7], pk[4 * T - 6], pk[4 * T - 5]});
      }
      if constexpr (C >= 2 && J < 26) {
        constexpr int cnt = p2_count(J + 1);
        if constexpr (cnt == 1) { if constexpr (T == 2) load2(IC<p2_first(J + 1)>{}, IC<C - 2>{}); }
        if constexpr (cnt == 2) { if constexpr (T >= 2) load2(IC<p2_first(J + 1) + C - 2>{}, IC<T - 2>{}); }
      }
      if constexpr (C >= 2 && J >= USE1 && J < USE1 + 3 && T == 2) inbox(IC<0>{}, IC<2 * (J - USE1) + C - 2>{}, pso2);
      GAP();
    });
  });
  nop<7>(); GAP();
  sfor<2>([&](auto hc) {
    constexpr int H = decltype(hc)::value;
    u32x4 o = {nopoison(cvtpk(oc[2 * H][0], oc[2 * H][1])), nopoison(cvtpk(oc[2 * H][2], oc[2 * H][3])), nopoison(cvtpk(oc[2 * H + 1][0], oc[2 * H + 1][1])), nopoison(cvtpk(oc[2 * H + 1][2], oc[2 * H + 1][3]))};
    const unsigned oi = own_idx(8, H);
    __builtin_amdgcn_raw_buffer_store_b128(o, prs, lane * 16 + H * 1024, oi < 3u ? 0x80000000u : pso + (unsigned)chunk[8] * (16 * 16384), ST_AUX);
    if (oi < 3u) lds_wr128(own_w + oi * 4096, o);
  });
  STAMP(6);
  waitv_tie<2>(fl);
  bool pre1 = ready(fl, gsrc[1]);
  sfor<6>([&](auto i) { inbox(IC<1>{}, i, pre1 ? (unsigned)gsrc[1] * 65536u : 0x80000000u); });
  unsigned ooff[3]; u32x2 xv[3];
#pragma unroll
  for (int i = 0; i < 3; i++) {
    const int sg = 6 * w + 2 * i, pc = 3 * sp + (sg >> 4);
    ooff[i] = (unsigned)((rt * 128 + (pc & 3) * 32 + (g & 1) * 16 + c16) * DM + (pc >> 2) * 64 + (sg & 15) * 4 + (g >> 1) * 4);
    xv[i] = *(const u32x2*)(x + ooff[i]);
  }
  u32x4 sel;
#pragma unroll
  for (int j = 0; j < 4; j++) sel[j] = ((c16 & 7) >> 1) == j ? ((c16 & 1) ? 0x3f800000u : 0x00003f80u) : 0u;
  const u32x4 z4 = {0u, 0u, 0u, 0u};
  const bf16x8 af = __builtin_bit_cast(bf16x8, c16 < 8 ? sel : z4), as = __builtin_bit_cast(bf16x8, c16 < 8 ? z4 : sel);
  f32x4 dd[6];
  unsigned ib = lane * 16 + w * 2048, ibh = ib + 65536, ibh2 = ib + 131072; pin(ibh); pin(ibh2);
  auto rdin = [&](auto Ac, auto Ic) { constexpr unsigned O = inbox_at(decltype(Ac)::value, decltype(Ic)::value);
    return lds_rd128(O < 65536 ? ib + O : (O < 131072 ? ibh + (O - 65536) : ibh2 + (O - 131072))); };
  sfor<6>([&](auto ic) { dd[decltype(ic)::value] = mfma(af, rdin(IC<0>{}, ic), (f32x4){0.f, 0.f, 0.f, 0.f}); });
  waitl0(); bar();
  {
    const bf16x8 af0 = __builtin_bit_cast(bf16x8, lane < 8 ? sel : z4);
    const unsigned orb = F_OWN + c16 * 16 + w * 1536;
    sfor<6>([&](auto ic) { constexpr int i = decltype(ic)::value; dd[i] = mfma(af0, lds_rd128(orb + i * 256), dd[i]); });
  }
  int spin = 0;
  if (!__all(fl == tag || c16 == sp || c16 >= 12)) {
    for (; spin < 200000; spin++) {
      __builtin_amdgcn_s_sleep(1);
      fl = poll_load(fp); waitv_tie<0>(fl);
      if (__all(fl == tag || c16 == sp || c16 >= 12)) break;
    }
  }
  STAMP(7);
  auto ld6 = [&](u32x4 (&v)[6], unsigned vo, int gs) {
#pragma unroll
    for (int i = 0; i < 6; i++) v[i] = __builtin_amdgcn_raw_buffer_load_b128(prs, vo, poff[i >> 1] + (i & 1) * 256 + gs * 65536, 16);
  };
  u32x4 po[6], pa[6];
#pragma unroll
  for (int k = 0; k < 2; k++) {
    if (!(k ? pre1 : pre0)) {
      ld6(po, pvo, gsrc[k]);
#pragma unroll
      for (int i = 0; i < 6; i++) dd[i] = mfma(af, __builtin_bit_cast(bf16x8, po[i]), dd[i]);
    }
  }
  const unsigned pvo_o = g == rr ? OOB : pvo;
  bool oka = false, oko = false;
  for (; spin < 200000; spin++) {
    asm volatile("" ::: "memory");
    const bool ra = !oka && ready(fl, 3);
    if (!oko) ld6(po, pvo_o, grp);
    if (ra) { ld6(pa, pvo, 3); oka = true; }
    if (!oka) fl = __hip_atomic_load(fp, RLX_AGENT);
    oko = clean6(po);
    if (oka && oko) break;
    if (oko) __builtin_amdgcn_s_sleep(1);
  }
  STAMP(8);
  waitv<0>();
  sfor<6>([&](auto ic) { constexpr int i = decltype(ic)::value;
    dd[i] = mfma(af, rdin(IC<1>{}, ic), dd[i]); dd[i] = mfma(af, __builtin_bit_cast(bf16x8, po[i]), dd[i]); dd[i] = mfma(as, __builtin_bit_cast(bf16x8, pa[i]), dd[i]); });
  STAMP(9);
#pragma unroll
  for (int i = 0; i < 3; i++) {
    float o[4];
#pragma unroll
    for (int j = 0; j < 4; j++) {
      float fv = dd[2 * i][j], sv = dd[2 * i + 1][j];
      swap32(fv, sv);
      const float gt = fv * __builtin_amdgcn_rcpf(1.f + __builtin_amdgcn_exp2f(-1.4426950408889634f * fv));
      o[j] = ((j & 1) ? bfhi(xv[i][j >> 1]) : bflo(xv[i][j >> 1])) + sv * gt;
    }
    *(u32x2*)(out + ooff[i]) = (u32x2){cvtpk(o[0], o[1]), cvtpk(o[2], o[3])};
  }
  if (sp == 0 && tid == 0) __hip_atomic_store((gu64*)(ws + EPOCH_OFF) + rt * 16, tag, RLX_AGENT);
  STAMP_FLUSH(1);
}

constexpr unsigned A_SLOT = 27648, A_XR = 110592, A_HALO = 143360, A_DUMMY = 161792, A_WOH = 125952, A_IMG = 872, A_OB = 125568, A_OS = 296;
constexpr unsigned wo_slot(int n) { return n < 6 ? 18432u * n : (n < 8 ? A_WOH + 18432u * (n - 6) : 0u); }
constexpr int a_touch(int s) { return ((s == -1 || s == 1 || s == 3 || s == 5 || s == 9) ? 1 : 0); }
constexpr int wo_allowed(int N, int need) {
  int after = 0;
  auto op = [&](int tag) { if (tag == need) after = 0; else after += 2; };
  for (int n = 0; n < 6; n++) op(n);
  for (int n = 0; n <= N; n++) {
    if (n >= 2) op(20 + n - 1);
    if (n == N) break;
    if (n == 0) { op(6); op(7); }
    if (n == 1) { op(8); op(20); }
  }
  return after;
}
constexpr int item_pos(int cg, int tg, int C) {
  if (cg == 3) { const int i = (C < 3 ? C : C - 1) + 3 * tg, j = i >= 6 ? i - 6 : i; return j == 2 ? 25 : (j == 5 ? 26 : 20 + j - (j > 2)); }
  if (C < 4) return 8 + 4 * cg + (C ^ (2 * tg));
  return (cg == 2 && C == 6) ? 24 : 3 * cg + C - 4;
}

constexpr u64 pos_table(int C) { u64 t = 0; for (int w = 0; w < 8; w++) t |= (u64)item_pos(w & 3, w >> 2, C) << (5 * w); return t; }
template <int C> DI unsigned pos_of(int w) { return (unsigned)(pos_table(C) >> (5 * w)) & 31u; }

DI void att_tail(const u16* wo, char* ws, int rt, int hg, int w, u64 tag
) {
  int tid = threadIdx.x; pin(tid);
  const int lane = tid & 63, tg = w >> 2, cg = w & 3;
  const int g = lane >> 4, q = (lane & 15) >> 2, p = lane & 3, c16 = lane & 15, i8 = lane >> 3, c8 = lane & 7;
  const u32x4 srdo = make_srd(wo, DM * DM * 2);
  const f32x4 zero4 = {0.f, 0.f, 0.f, 0.f};
  bf16x4 wlo[4], whi[4];
  auto wimg = [&](unsigned a, const f32x4& v) { lds_wr64(a, (u32x2){cvtpk(v[0], v[1]), cvtpk(v[2], v[3])}); };
  const unsigned swz = 16u * (c8 ^ (2 * ((lane >> 4) & 3)));
  const unsigned vo1 = (8 * (i8 >> 2) + (i8 & 3)) * (DM * 2) + swz, vo2 = i8 * (DM * 2) + swz;
  const unsigned soo = (unsigned)((hg * 144 + 32 * (w >> 1) + 4 * (w & 1)) * (DM * 2));
  auto loado = [&](auto Nc, auto Ic) {
    constexpr int N = decltype(Nc)::value, I = decltype(Ic)::value;
    if constexpr (I < 2) dma(wo_slot(N) + (2 * w + I) * 1024, vo1, srdo, soo + I * 16 * DM * 2 + N * 128);
    else if (w < 2) dma(wo_slot(N) + (16 + w) * 1024, vo2, srdo, (unsigned)((hg * 144 + 128 + 8 * w) * (DM * 2)) + N * 128);
  };
  {
    const int hp = w >> 1, par = w & 1;
    const unsigned r0 = (2 * c16 + par + 16) * A_IMG + 72 * hp, rm = (unsigned)max(2 * c16 + par - 16, 0) * A_IMG + 72 * hp + 288;
    unsigned rq[3] = {r0 + 8 * g, r0 + 8 * g + 40, r0 + 32};
    unsigned rk[4] = {rq[0] + 288, rq[1] + 288, rq[2] + 288, rq[2] + 288};
    unsigned rkm[4] = {rm + 8 * g, rm + 8 * g + 40, rm + 32, rm + 32};
    const int kr = 4 * g + q;
    unsigned rv = (2 * kr + par + 16) * A_IMG + 576 + 72 * hp + 8 * p, rvp = rv, rv3 = rv + 96 * A_IMG;
    unsigned rvm = (unsigned)max(2 * kr + par - 16, 0) * A_IMG + 576 + 72 * hp + 8 * p;
    unsigned rq3[3], rk3[4];
#pragma unroll
    for (int i = 0; i < 4; i++) { if (i < 3) { rq3[i] = rq[i] + 96 * A_IMG; pin(rq[i]); pin(rq3[i]); } rk3[i] = rk[i] + 96 * A_IMG; pin(rk[i]); pin(rk3[i]); pin(rkm[i]); }
    pin(rv); pin(rvp); pin(rv3); pin(rvm);
    f32x4 biasA, biasA0, biasB; unsigned mA[2], mA0[2], mB[2];
#pragma unroll
    for (int j = 0; j < 4; j++) {
      const int kk = 4 * g + j; const bool va = kk >= c16 + 9, vb = (kk <= c16) && (kk + 7 >= c16);
      biasA[j] = va ? 0.f : -1e30f; biasB[j] = vb ? 0.f : -1e30f; biasA0[j] = (va && rt > 0) ? 0.f : -1e30f;
      const unsigned bm = (j & 1) ? 0xffff0000u : 0x0000ffffu;
      if ((j & 1) == 0) { mA[j >> 1] = 0; mB[j >> 1] = 0; }
      mA[j >> 1] |= va ? bm : 0u; mB[j >> 1] |= vb ? bm : 0u;
    }
    mA0[0] = rt > 0 ? mA[0] : 0u; mA0[1] = rt > 0 ? mA[1] : 0u;
    pin(biasA); pin(biasA0); pin(biasB); pin(mA[0]); pin(mA[1]); pin(mA0[0]); pin(mA0[1]); pin(mB[0]); pin(mB[1]);
    unsigned qmask = g == 0 ? 0xffffffffu : 0u; pin(qmask);
    unsigned ow0 = A_OB + (2 * c16 + par) * A_OS + 72 * hp, owa = ow0 + 8 * g, owb = owa + 40; pin(ow0); pin(owa); pin(owb);
    waitl0(); bar();
    STAMP(3);
    auto asb = [](u32x2 a, u32x2 b) { return __builtin_bit_cast(bf16x8, (u32x4){a[0], a[1], b[0], b[1]}); };
    bf16x8 kf[5][2], vf[4][3]; u32x2 qm[4][2], q4[4];
    sfor<2>([&](auto Hc) { constexpr int H = decltype(Hc)::value; kf[0][H] = asb(lds_rd64(rkm[H]), lds_rd64(rkm[2 + H])); });
    sfor<4>([&](auto Uc) { constexpr int U = decltype(Uc)::value; constexpr unsigned ro = U < 3 ? U * 32 * A_IMG : 0;
      q4[U] = lds_rd64((U < 3 ? rq[2] : rq3[2]) + ro);
      sfor<2>([&](auto Hc) { constexpr int H = decltype(Hc)::value;
        qm[U][H] = lds_rd64((U < 3 ? rq[H] : rq3[H]) + ro);
        kf[U + 1][H] = asb(lds_rd64((U < 3 ? rk[H] : rk3[H]) + ro), lds_rd64((U < 3 ? rk[2 + H] : rk3[2 + H]) + ro)); });
      GAP(); });
    sfor<4>([&](auto Uc) { constexpr int U = decltype(Uc)::value;
      sfor<3>([&](auto Dc) { constexpr int D = decltype(Dc)::value; constexpr unsigned co = D == 0 ? 0 : (D == 1 ? 40 : 32);
        vf[U][D] = cat(U == 0 ? lds_tr(rvm + co) : lds_tr(rvp + (U > 0 ? U - 1 : 0) * 32 * A_IMG + co), U < 3 ? lds_tr(rv + U * 32 * A_IMG + co) : lds_tr(rv3 + co)); }); });
    GAP();
    f32x4 sa[8], sb[8];
    sfor<8>([&](auto Ic) { constexpr int I = decltype(Ic)::value, U = I >> 1, H = I & 1;
      const bf16x8 qf = asb(qm[U][H], H ? (u32x2){0u, q4[U][1] & qmask} : (u32x2){q4[U][0] & qmask, 0u});
      sa[I] = mfma(kf[U][H], qf, U == 0 ? biasA0 : biasA); sb[I] = mfma(kf[U + 1][H], qf, biasB); });
    GAP();
    waitl0(); bar();
    STAMP(4);
    sfor<3>([&](auto i) { loado(IC<0>{}, i); }); sfor<3>([&](auto i) { loado(IC<1>{}, i); });
    GAP();
    float v[8][4], mx[8];
    sfor<8>([&](auto Ic) { constexpr int I = decltype(Ic)::value;
#pragma unroll
      for (int j = 0; j < 4; j++) v[I][j] = fmx(sa[I][j], sb[I][j]);
      mx[I] = fmx(fmx(fmx(v[I][0], v[I][1]), v[I][2]), v[I][3]); });
    constexpr float SC = 0.23570226039551584f * 1.4426950408889634f;
    sfor<2>([&](auto hc) { constexpr int B = 4 * decltype(hc)::value;
      unpack4(-SC * pack_max(mx[B], mx[B + 1], mx[B + 2], mx[B + 3]), mx[B], mx[B + 1], mx[B + 2], mx[B + 3]); });
    GAP();
    sfor<3>([&](auto i) { loado(IC<2>{}, i); }); sfor<3>([&](auto i) { loado(IC<3>{}, i); });
    GAP();
    float sm[8];
    sfor<8>([&](auto Ic) { constexpr int I = decltype(Ic)::value;
#pragma unroll
      for (int j = 0; j < 4; j++) v[I][j] = __builtin_amdgcn_exp2f(__builtin_fmaf(v[I][j], SC, mx[I]));
      sm[I] = (v[I][0] + v[I][1]) + (v[I][2] + v[I][3]); });
    sfor<2>([&](auto hc) { constexpr int B = 4 * decltype(hc)::value;
      unpack4(__builtin_amdgcn_rcpf(pack_sum(sm[B], sm[B + 1], sm[B + 2], sm[B + 3])), sm[B], sm[B + 1], sm[B + 2], sm[B + 3]); });
    GAP();
    STAMP(5);
    sfor<3>([&](auto i) { loado(IC<4>{}, i); }); sfor<3>([&](auto i) { loado(IC<5>{}, i); });
    GAP();
    bf16x8 pf[8];
    sfor<8>([&](auto Ic) { constexpr int I = decltype(Ic)::value, U = I >> 1;
      const unsigned p0 = cvtpk(v[I][0] * sm[I], v[I][1] * sm[I]), p1 = cvtpk(v[I][2] * sm[I], v[I][3] * sm[I]);
      pf[I] = __builtin_bit_cast(bf16x8, (u32x4){p0 & (U == 0 ? mA0[0] : mA[0]), p1 & (U == 0 ? mA0[1] : mA[1]), p0 & mB[0], p1 & mB[1]}); });
    GAP();
    f32x4 o0[8], o1[8];
    sfor<8>([&](auto Ic) { constexpr int I = decltype(Ic)::value; o0[I] = mfma(vf[I >> 1][I & 1], pf[I], zero4); });
    sfor<8>([&](auto Ic) { constexpr int I = decltype(Ic)::value; o1[I] = mfma(vf[I >> 1][2], pf[I], zero4); });
    GAP();
    sfor<8>([&](auto Ic) { constexpr int I = decltype(Ic)::value; wimg(((I & 1) ? owb : owa) + (I >> 1) * 32 * A_OS, o0[I]); });
    if (g == 0) {
      sfor<4>([&](auto Uc) { constexpr int U = decltype(Uc)::value;
        lds_wr64(ow0 + 32 + U * 32 * A_OS, (u32x2){cvtpk(o1[2 * U][0], o1[2 * U][1]), cvtpk(o1[2 * U + 1][2], o1[2 * U + 1][3])}); });
    }
  }
  const int row = 4 * g + q;
  const unsigned ow = row * 128 + 32 * (cg ^ ((row >> 1) & 3)) + 8 * p; unsigned owh = ow + 65536, owh2 = ow + A_WOH; pin(owh); pin(owh2);
  unsigned ob = A_OB + (tg * 64 + c16) * A_OS + 16 * g, ob4 = A_OB + (tg * 64 + c16) * A_OS + 256 + 8 * g; pin(ob); pin(ob4);
  waitv<10>(); waitl0(); bar();
  STAMP(6);
  auto rdo = [&](auto GIc, auto Hc) {
    constexpr int GI = decltype(GIc)::value, H = decltype(Hc)::value, N = GI / 5, S = GI % 5;
    constexpr unsigned O = wo_slot(N) + S * 4096 + H * 2048;
    const unsigned a = O >= A_WOH ? owh2 + (O - A_WOH) : (O >= 65536 ? owh + (O - 65536) : ow + O);
    if constexpr (H == 0) wlo[GI & 3] = lds_tr(a); else if constexpr (S < 4) whi[GI & 3] = lds_tr(a);
  };
  bf16x8 of[4][4]; u32x2 of4[4];
  sfor<5>([&](auto Sc) { constexpr int S = decltype(Sc)::value;
    if constexpr (S < 3) { rdo(Sc, IC<0>{}); rdo(Sc, IC<1>{}); }
    sfor<4>([&](auto Tc) { constexpr int T = decltype(Tc)::value;
      if constexpr (S < 4) of[T][S & 3] = lds_rd128a8(ob + T * 16 * A_OS + 64 * S); else of4[T] = lds_rd64(ob4 + T * 16 * A_OS); });
    GAP(); });
  const __amdgpu_buffer_rsrc_t prs = __builtin_amdgcn_make_buffer_rsrc(ws + PART_OFF, 0, PART_BYTES, 0x00020000);
  const unsigned pso = (unsigned)(((rt * 9) * 16 + 12 + hg) * 16384 + w * 2048);
  f32x4 oc[4]; unsigned pk[8];
  GAP();
  sfor<45>([&](auto GIc) {
    constexpr int GI = decltype(GIc)::value, N = GI / 5, S = GI % 5;
    if constexpr (GI == 5) STAMP(7);
    if constexpr (GI == 15) STAMP(8);
    if constexpr (GI == 30) STAMP(9);
    const bf16x4 alo = wlo[GI & 3];
    bf16x8 a; if constexpr (S < 4) a = cat(alo, whi[GI & 3]);
    sfor<4>([&](auto Tc) {
      constexpr int T = decltype(Tc)::value;
      if constexpr (S == 0 && N > 0) { if constexpr (T == 0) { nop<2>(); GAP(); } pk[2 * T] = cvtpk(oc[T][0], oc[T][1]); pk[2 * T + 1] = cvtpk(oc[T][2], oc[T][3]); GAP(); }
      if constexpr (S == 0) mma0(oc[T], a, of[T][0]); else if constexpr (S < 4) mma(oc[T], a, of[T][S & 3]); else mma16(oc[T], alo, of4[T]);
      GAP();
      if constexpr (S == 2 && T == 0 && (N == 0 || N == 1 || N == 4)) {
        if constexpr (N == 0) waitl0();
        waitv<wo_allowed(N, N == 0 ? 1 : (N == 1 ? 5 : 8))>(); bar();
      }
      if constexpr (GI + 3 < 45 && T < 2) rdo(IC<GI + 3>{}, Tc);
      if constexpr (N == 0 && S >= 3 && T > 0) loado(IC<3 + S>{}, IC<T - 1>{});
      if constexpr (N == 1 && S == 2 && T > 0) loado(IC<8>{}, IC<T - 1>{});
      if constexpr (S == (N == 1 ? 3 : 0) && N > 0 && (T & 1))
        __builtin_amdgcn_raw_buffer_store_b128((u32x4){pk[2 * T - 2], pk[2 * T - 1], pk[2 * T], pk[2 * T + 1]}, prs, lane * 16 + (T >> 1) * 1024, pso + (N - 1) * 16 * 16384, ST_AUX);
      GAP();
    });
  });
  nop<7>(); GAP();
  sfor<2>([&](auto hc) {
    constexpr int H = decltype(hc)::value;
    u32x4 o = {nopoison(cvtpk(oc[2 * H][0], oc[2 * H][1])), nopoison(cvtpk(oc[2 * H][2], oc[2 * H][3])), nopoison(cvtpk(oc[2 * H + 1][0], oc[2 * H + 1][1])), nopoison(cvtpk(oc[2 * H + 1][2], oc[2 * H + 1][3]))};
    __builtin_amdgcn_raw_buffer_store_b128(o, prs, lane * 16 + H * 1024, pso + 8 * 16 * 16384, ST_AUX);
  });
  STAMP(10);
  waitv<0>(); bar();
  if (tid < 9) set_flag(ws, rt, tid, 12 + hg, tag);
  STAMP(11); STAMP_FLUSH(1);
}

DI void att_path(const u16* x, const u16* wqkv, const u16* wo, char* ws, int rt, int hg) {
  const int tid = threadIdx.x, lane = tid & 63;
  const int w = __builtin_amdgcn_readfirstlane(tid >> 6), tg = w >> 2, cg = w & 3;
  const int g = lane >> 4, q = (lane & 15) >> 2, p = lane & 3, c16 = lane & 15;
  STAMP_DECL; STAMP(0);
  const u32x4 srdx = make_srd(x, XN_BYTES), srdw = make_srd(wqkv, DM * 3 * DM * 2), srdo = make_srd(wo, DM * DM * 2);
  const int i8 = lane >> 3, c8 = lane & 7, l32 = lane & 31;
  const unsigned vox = i8 * (DM * 2) + c8 * 16;
  const unsigned sox = (unsigned)((rt * 128 + 16 * w) * (DM * 2));
  u64 epoch = rd_epoch(ws, rt);
  u32x4 xr[2][9], xh[3];
  {
    const unsigned soh = (unsigned)((rt > 0 ? rt * 128 - 16 + 2 * w : 0) * (DM * 2)), voh = (lane >> 5) * (DM * 2) + l32 * 16;
    xh[0] = gload<0>(voh, srdx, soh); xh[1] = gload<512>(voh, srdx, soh); xh[2] = gload<1024>(l32 < 8 ? voh : OOB, srdx, soh);
  }
  sfor<9>([&](auto tc) { constexpr int t = decltype(tc)::value;
    xr[0][t] = gload<t * 128>(vox, srdx, sox); xr[1][t] = gload<t * 128>(vox, srdx, sox + 8 * DM * 2); });
  unsigned vw[4];
#pragma unroll
  for (int i = 0; i < 4; i++) {
    const unsigned n = (w + 8 * i) * 64 + lane, r = (n * 9710u) >> 19, c = n - 54 * r;
    const unsigned k = 8 * ((r >> 2) & 3) + 4 * (r >> 4) + (r & 3);
    const unsigned sec = (c * 57u) >> 10;
    vw[i] = k * 3456 + c * 16 + sec * (1152 - 288);
  }
  auto loadw = [&](auto Sc, auto Ic) {
    constexpr int S = decltype(Sc)::value, I = decltype(Ic)::value;
    if constexpr (S < 18) { if (I < 3 || w < 3) dma(
(S & 3) * A_SLOT + (w + 8 * I) * 1024, vw[I], srdw, (unsigned)(S * 32 * 3456 + hg * 288)); }
  };
  sfor<4>([&](auto i) { loadw(IC<0>{}, i); });
  const unsigned tvw = (lane < 12 && p < 3) ? (unsigned)(((w >> 1) * 32 + 2 * rt + (w & 1)) * 3456 + 2 * hg * 128) + q * 1152 + p * 128 : OOB;
  unsigned wbs[7];
  auto img_off = [&](unsigned pos) { const unsigned left = 0u - (unsigned)(pos >= 24), odd = hg & 1;
    return ((288u * (pos >> 3) + 32u * ((pos & 7) + odd)) & ~left) | ((288u * (pos - 24) + 256u - 256u * odd) & left); };
  {
    const unsigned lr = (4 * g + q) * 864 + 8 * p;
    sfor<7>([&](auto Cc) { constexpr int C = decltype(Cc)::value;
      const unsigned pos = pos_of<C>(w);
      wbs[C] = lr + img_off(pos);
      pin(wbs[C]); });
  }
  const unsigned xs = (16 * g + (c16 ^ g)) * 16;
  unsigned xb0 = A_XR + tg * 4096 + xs, xb1 = xb0 ^ 64, hb0 = A_HALO + xs, hb1 = hb0 ^ 64; pin(xb0); pin(xb1); pin(hb0); pin(hb1);
  unsigned xw = A_XR + (c8 >> 2) * 8192 + (16 * (c8 & 3) + (i8 ^ c8)) * 16 + w * 1024; pin(xw);
  GAP();
  STAMP(12);
  {
    Stat st = {}; float r, nmr;
    sfor<3>([&](auto ic) { constexpr int i = decltype(ic)::value; waitv_tie<3 + 18 + 2 - i>(xh[i]); stat_acc(xh[i], st); });
    stat_fin<true>(st, lane, r, nmr);
    const unsigned hwa = A_HALO + (l32 >> 2) * 1024 + (16 * (l32 & 3) + ((2 * w + (lane >> 5)) ^ c8)) * 16;
    lds_wr128(hwa, norm8(xh[0], r, nmr)); lds_wr128(hwa + 8192, norm8(xh[1], r, nmr));
    if (l32 < 8) lds_wr128(hwa + 16384, norm8(xh[2], r, nmr));
  }
  GAP();
  float rs[2], nm[2];
  {
    Stat st[2] = {};
    sfor<18>([&](auto ic) { constexpr int i = decltype(ic)::value;
      waitv_tie<3 + (i > 7 ? 4 : 0) + 17 - i>(xr[i & 1][i >> 1]); stat_acc(xr[i & 1][i >> 1], st[i & 1]); GAP();
      if constexpr (i == 7) { sfor<4>([&](auto k) { loadw(IC<1>{}, k); }); touch(A_DUMMY, tvw, srdw, (unsigned)(2 * 32 * 3456)); } });
    stat_fin<false>(st[0], lane, rs[0], nm[0]); stat_fin<false>(st[1], lane, rs[1], nm[1]);
    epoch = epoch_uniform(epoch);
  }
  STAMP(13);
  NormSt ns;
  auto xop = [&](auto Tc, auto Oc) {
    constexpr int T = decltype(Tc)::value, O = decltype(Oc)::value;
    if constexpr (T < 9 && O < 42) norm_op<O>(ns, xr[0][T < 9 ? T : 0], xr[1][T < 9 ? T : 0], rs, nm, xw + (T & 1) * 16384);
  };
  sfor<2>([&](auto r) { constexpr int R = decltype(r)::value; lds_wr128(xw + R * 128, norm8(xr[R][0], rs[R], nm[R])); });
  lds_wr128(xw + 16384, norm8(xr[0][1], rs[0], nm[0]));

  const f32x4 zero4 = {0.f, 0.f, 0.f, 0.f};
  const u64 tag = next_tag(epoch);
  f32x4 acc[7][4], hac[3];
  sfor<4>([&](auto t) { acc[3][decltype(t)::value] = zero4; }); hac[2] = zero4;
  bf16x8 xf[4], xhf;
  bf16x4 wlo[4], whi[4];
  auto rdw = [&](auto GIc, auto Hc) {
    constexpr int GI = decltype(GIc)::value, H = decltype(Hc)::value, S = GI / 7, C = GI % 7;
    if constexpr (GI < 126) {
      const unsigned a = wbs[C] + (S & 1) * A_SLOT + H * 16 * 864;
      if constexpr (H == 0) wlo[GI & 3] = lds_tr(a); else whi[GI & 3] = lds_tr(a);
      if constexpr (H == 1 && (S & 1) == 1) { wbs[C] += (S & 2) ? -2 * A_SLOT : 2 * A_SLOT; pin(wbs[C]); }
    }
  };
  auto rdx = [&](auto Sc, auto Tc) {
    constexpr int S = decltype(Sc)::value, T = decltype(Tc)::value;
    xf[T] = lds_rd128(((S & 1) ? xb1 : xb0) + (S & 3) * 8192 + T * 1024);
  };
  auto rdxh = [&](auto Sc) { constexpr int S = decltype(Sc)::value; xhf = lds_rd128(((S & 1) ? hb1 : hb0) + S * 1024); };
  unsigned ih, ibase;
  unsigned toff[7];
  auto tileoff = [&](int C) { return toff[C]; };
  auto wimg = [&](unsigned a, const f32x4& v) { lds_wr64(a, (u32x2){cvtpk(v[0], v[1]), cvtpk(v[2], v[3])}); };
  STAMP(14);
  waitv<4>(); waitl0(); bar();
  STAMP(1);
  rdw(IC<0>{}, IC<0>{}); rdw(IC<0>{}, IC<1>{}); rdw(IC<1>{}, IC<0>{}); rdw(IC<1>{}, IC<1>{});
  sfor<4>([&](auto t) { rdx(IC<0>{}, t); });
  rdxh(IC<0>{});
  nop<1>();
  GAP();
  sfor<18>([&](auto Sc) {
    constexpr int S = decltype(Sc)::value, G0 = 7 * S;
    auto fill = [&](auto GPc) {
      constexpr int GP = decltype(GPc)::value;
      GAP();
      if constexpr (GP == 21) {
        if constexpr (S < 16) waitv<3 + a_touch(S) + a_touch(S - 1)>(); else if
 constexpr (S == 16) waitv<0>(); else waitl0();
        bar();
      }
      if constexpr (S == 17 && GP == 12) { int tz = tid; pin(tz); ih = (tz & 15) * A_IMG + 8 * ((tz >> 4) & 3); ibase = ih + (16 + tg * 64) * A_IMG;
        sfor<7>([&](auto Cc) { constexpr int C = decltype(Cc)::value; toff[C] = img_off(pos_of<C>(w)); }); }
      if constexpr (S == 0) xop(IC<1>{}, IC<GP + 21>{});
      else if constexpr ((S & 1) == 0) xop(IC<S / 2 + 1>{}, IC<GP + 8>{});
      else if constexpr (GP >= 22) xop(IC<(S + 3) / 2>{}, IC<GP - 22>{});
      else if constexpr (GP < 4 && S > 1) xop(IC<(S + 1) / 2>{}, IC<38 + GP>{});
      if constexpr (GP < 4) rdw(IC<G0 + 2 + (GP >> 1)>{}, IC<(GP & 1)>{});
      if constexpr (GP >= 8 && GP < 12) rdw(IC<G0 + 4 + ((GP - 8) >> 1)>{}, IC<(GP & 1)>{});
      if constexpr (GP == 15 || GP == 16) rdw(IC<G0 + 6>{}, IC<GP - 15>{});
      if constexpr (GP == 21 || GP == 22) rdw(IC<G0 + 7>{}, IC<GP - 21>{});
      if constexpr (GP == 24 || GP == 26) rdw(IC<G0 + 8>{}, IC<(GP - 24) / 2>{});
      if constexpr (S < 17) {
        if constexpr (GP >= 23 && (GP & 1) == 1) rdx(IC<S + 1>{}, IC<(GP - 23) / 2>{});
        if constexpr (GP == 28) rdxh(IC<S + 1>{});
      } else if constexpr (GP >= 21) {
        if constexpr (GP < 29) wimg(ibase + tileoff((GP - 21) >> 2) + ((GP - 21) & 3) * 16 * A_IMG, acc[(GP - 21) >> 2][(GP - 21) & 3]);
      }
      if constexpr (GP == 4 || GP == 6 || GP == 12 || GP == 17) loadw(IC<S + 2>{}, IC<(GP == 4 ? 0 : (GP == 6 ? 1 : (GP == 12 ? 2 : 3)))>{});
      if constexpr (GP == 18 && (S == 1 || S == 5 || S == 9)) touch(A_DUMMY, tvw, srdw, (unsigned)((S + 5) * 32 * 3456));
      if constexpr (GP == 18 && S == 3) touch(A_DUMMY, lane < 11 && w * 11 + lane < 81 ? lane * 128u : OOB, srdo, (unsigned)((hg * 144 + rt * 9) * (DM * 2) + w * 11 * 128));
      GAP();
    };
    auto mm = [&](f32x4& c, const bf16x8& a, const bf16x8& b) { if constexpr (S == 0) mma0(c, a, b); else mma(c, a, b); };
    const bf16x8 a0 = cat(wlo[G0 & 3], whi[G0 & 3]), a1 = cat(wlo[(G0 + 1) & 3], whi[(G0 + 1) & 3]);
    sfor<4>([&](auto Tc) { constexpr int T = decltype(Tc)::value;
      mm(acc[0][T], a0, xf[T]); fill(IC<2 * T>{}); mm(acc[1][T], a1, xf[T]); fill(IC<2 * T + 1>{}); });
    mm(hac[0], a0, xhf); fill(IC<8>{});
    mm(hac[1], a1, xhf); fill(IC<9>{});
    const bf16x8 a2 = cat(wlo[(G0 + 2) & 3], whi[(G0 + 2) & 3]);
    sfor<4>([&](auto Tc) { constexpr int T = decltype(Tc)::value; mm(acc[2][T], a2, xf[T]); fill(IC<10 + T>{}); });
    const bf16x8 a3 = cat(wlo[(G0 + 3) & 3], whi[(G0 + 3) & 3]);
    mma_alt3(acc[3][0], a3, xf[0], hac[2], a2, xhf, cg); fill(IC<14>{});
    sfor<3>([&](auto Tc) { constexpr int T = decltype(Tc)::value + 1; mma_not3(acc[3][T], a3, xf[T], cg); fill(IC<14 + T>{}); });
    const bf16x8 a4 = cat(wlo[(G0 + 4) & 3], whi[(G0 + 4) & 3]);
    sfor<4>([&](auto Tc) { constexpr int T = decltype(Tc)::value; mm(acc[4][T], a4, xf[T]); fill(IC<18 + T>{}); });
    const bf16x8 a5 = cat(wlo[(G0 + 5) & 3], whi[(G0 + 5) & 3]), a6 = cat(wlo[(G0 + 6) & 3], whi[(G0 + 6) & 3]);
    sfor<4>([&](auto Tc) { constexpr int T = decltype(Tc)::value;
      mm(acc[5][T], a5, xf[T]); fill(IC<22 + 2 * T>{}); mm(acc[6][T], a6, xf[T]); fill(IC<23 + 2 * T>{}); });
  });
  STAMP(2);
  {
    wimg(ih + tileoff(0), hac[0]); wimg(ih + tileoff(1), hac[1]);
    sfor<4>([&](auto Tc) { constexpr int T = decltype(Tc)::value; wimg(ibase + tileoff(2) + T * 16 * A_IMG, acc[2][T]); });
    if (cg < 3) { sfor<4>([&](auto Tc) { constexpr int T = decltype(Tc)::value; wimg(ibase + tileoff(3) + T * 16 * A_IMG, acc[3][T]); }); }
    else wimg(ih + tileoff(2), hac[2]);
    sfor<3>([&](auto Cc) { constexpr int C = 4 + decltype(Cc)::value;
      sfor<4>([&](auto Tc) { constexpr int T = decltype(Tc)::value; wimg(ibase + tileoff(C) + T * 16 * A_IMG, acc[C][T]); }); });
  }
  GAP();
  att_tail(wo, ws, rt, hg, w, tag
  );
}

__global__ __launch_bounds__(512, 2) void k1_main(const u16* __restrict__ x, const u16* __restrict__ wqkv, const u16* __restrict__ wo,
                                                  const u16* __restrict__ w1, const u16* __restrict__ w2, char* __restrict__ ws, u16* __restrict__ out) {
  const int b = blockIdx.x, xc = b & 7, i = b >> 3;
  const int rt = 4 * (xc >> 1) + (i & 3), j = i >> 2;
  if (j < 6) ffn_path(x, w1, w2, ws, out, rt, 6 * (xc & 1) + j);
  else att_path(x, wqkv, wo, ws, rt, 2 * (xc & 1) + (j - 6));
}

extern "C" void kernel_launch(void* const* d_in, const int* in_sizes, int n_in, void* d_out, int out_size, void* d_ws, size_t ws_size, hipStream_t stream) {
  const u16* x = (const u16*)d_in[0]; const u16* wqkv = (const u16*)d_in[1]; const u16* wo = (const u16*)d_in[2];
  const u16* w1 = (const u16*)d_in[4]; const u16* w2 = (const u16*)d_in[5];
  (void)hipFuncSetAttribute((const void*)k1_main, hipFuncAttributeMaxDynamicSharedMemorySize, 163840);
  k1_main<<<256, 512, 163840, stream>>>(x, wqkv, wo, w1, w2, (char*)d_ws, (u16*)d_out);
}
```

```cpp
#include <hip/hip_runtime.h>
#include <utility>

typedef unsigned short u16;
typedef __bf16 bf16x2 __attribute__((ext_vector_type(2)));
typedef __bf16 bf16x4 __attribute__((ext_vector_type(4)));
typedef __bf16 bf16x8 __attribute__((ext_vector_type(8)));
typedef short s16x2 __attribute__((ext_vector_type(2)));
typedef float f32x2 __attribute__((ext_vector_type(2)));
typedef float f32x4 __attribute__((ext_vector_type(4)));
typedef unsigned u32x2 __attribute__((ext_vector_type(2)));
typedef unsigned u32x4 __attribute__((ext_vector_type(4)));
#define LDSP(T) __attribute__((address_space(3))) T*
#define DI __device__ __forceinline__
#define GAP() __builtin_amdgcn_sched_barrier(0)
template <int V> using IC = std::integral_constant<int, V>;
template <class F, int... Is> DI void sfor_(F&& f, std::integer_sequence<int, Is...>) { (f(IC<Is>{}), ...); }
template <int N, class F> DI void sfor(F&& f) { sfor_(f, std::make_integer_sequence<int, N>{}); }

constexpr int SEQ = 2048, DM = 576, DFF = 4608;
constexpr unsigned XN_BYTES = SEQ * DM * 2;
constexpr size_t PART_OFF = 0;
constexpr unsigned PART_BYTES = 16u * XN_BYTES;
constexpr size_t FLAG_OFF = PART_OFF + PART_BYTES;
constexpr size_t EPOCH_OFF = FLAG_OFF + 16 * 9 * 16 * 8;
typedef unsigned long long u64;
typedef __attribute__((address_space(1))) u64 gu64;
#define RLX_AGENT __ATOMIC_RELAXED, __HIP_MEMORY_SCOPE_AGENT
#define TAIL_SLEEP 16
#define ST_AUX 16

DI unsigned cvtpk(float lo, float hi) { f32x2 v = {lo, hi}; return __builtin_bit_cast(unsigned, __builtin_convertvector(v, bf16x2)); }
DI unsigned relupk(unsigned p) { s16x2 z = {0, 0}; return __builtin_bit_cast(unsigned, __builtin_elementwise_max(__builtin_bit_cast(s16x2, p), z)); }
DI float bflo(unsigned p) { return __builtin_bit_cast(float, p << 16); }
DI float bfhi(unsigned p) { return __builtin_bit_cast(float, p & 0xffff0000u); }
DI bf16x4 lds_tr(unsigned addr) { return __builtin_amdgcn_ds_read_tr16_b64_v4bf16((LDSP(bf16x4))(size_t)addr); }
DI bf16x8 lds_rd128(unsigned addr) { return *(LDSP(bf16x8))(size_t)addr; }
typedef bf16x8 bf16x8a8 __attribute__((aligned(8)));
DI bf16x8 lds_rd128a8(unsigned addr) { return *(LDSP(bf16x8a8))(size_t)addr; }
DI u32x2 lds_rd64(unsigned addr) { return *(LDSP(u32x2))(size_t)addr; }
DI void lds_wr128(unsigned addr, u32x4 v) { *(LDSP(u32x4))(size_t)addr = v; }
DI void lds_wr64(unsigned addr, u32x2 v) { *(LDSP(u32x2))(size_t)addr = v; }
DI bf16x8 cat(bf16x4 a, bf16x4 b) { return __builtin_shufflevector(a, b, 0, 1, 2, 3, 4, 5, 6, 7); }
DI f32x4 mfma(bf16x8 a, bf16x8 b, f32x4 c) { return __builtin_amdgcn_mfma_f32_16x16x32_bf16(a, b, c, 0, 0, 0); }
DI void mma(f32x4& c, bf16x8 a, bf16x8 b) { asm volatile("v_mfma_f32_16x16x32_bf16 %0, %1, %2, %0" : "+v"(c) : "v"(a), "v"(b)); }
DI void mma0(f32x4& c, bf16x8 a, bf16x8 b) { asm volatile("v_mfma_f32_16x16x32_bf16 %0, %1, %2, 0" : "=&v"(c) : "v"(a), "v"(b)); }
DI void mma16(f32x4& c, bf16x4 a, u32x2 b) { asm volatile("v_mfma_f32_16x16x16_bf16 %0, %1, %2, %0" : "+v"(c) : "v"(a), "v"(b)); }
DI void mma_not3(f32x4& c, bf16x8 a, bf16x8 b, int cg) {
  asm volatile("s_cmp_eq_u32 %3, 3\n\ts_cbranch_scc1 .Le%=\n\tv_mfma_f32_16x16x32_bf16 %0, %1, %2, %0\n.Le%=:" : "+v"(c) : "v"(a), "v"(b), "s"(cg) : "scc");
}
DI void mma_alt3(f32x4& c, bf16x8 a, bf16x8 b, f32x4& h, bf16x8 ah, bf16x8 bh, int cg) {
  asm volatile("s_cmp_eq_u32 %6, 3\n\ts_cbranch_scc1 .Lh%=\n\tv_mfma_f32_16x16x32_bf16 %0, %2, %3, %0\n\ts_branch .Le%=\n"
               ".Lh%=:\n\tv_mfma_f32_16x16x32_bf16 %1, %4, %5, %1\n.Le%=:" : "+v"(c), "+v"(h) : "v"(a), "v"(b), "v"(ah), "v"(bh), "s"(cg) : "scc");
}
template <int N> DI void nop() { asm volatile("s_nop %0" ::"i"(N)); }
template <int N> DI void waitv() { asm volatile("s_waitcnt vmcnt(%0)" ::"i"(N) : "memory"); }
DI void waitl0() { asm volatile("s_waitcnt lgkmcnt(0)" ::: "memory"); }
DI void bar() { asm volatile("s_barrier" ::: "memory"); }
DI u32x4 make_srd(const void* p, unsigned bytes) {
  size_t a = (size_t)p; u32x4 s;
  s[0] = __builtin_amdgcn_readfirstlane((unsigned)a); s[1] = __builtin_amdgcn_readfirstlane((unsigned)(a >> 32));
  s[2] = bytes; s[3] = 0x00020000u; return s;
}
DI void dma(unsigned lds_dst, unsigned voff, u32x4 srd, unsigned soff) {
  asm volatile("s_mov_b32 m0, %0\n\ts_nop 0\n\tbuffer_load_dwordx4 %1, %2, %3 offen lds" ::"s"(lds_dst), "v"(voff), "s"(srd), "s"(soff) : "memory");
}
DI void dma_sc1(unsigned lds_dst, unsigned voff, u32x4 srd, unsigned soff) {
  asm volatile("s_mov_b32 m0, %0\n\ts_nop 0\n\tbuffer_load_dwordx4 %1, %2, %3 offen sc1 lds" ::"s"(lds_dst), "v"(voff), "s"(srd), "s"(soff) : "memory");
}
template <int CTRL> DI float dppadd(float v) { return v + __builtin_bit_cast(float, __builtin_amdgcn_update_dpp(0, __builtin_bit_cast(int, v), CTRL, 0xF, 0xF, true)); }
DI float red8(float v) { v = dppadd<0xB1>(v); v = dppadd<0x4E>(v); return dppadd<0x141>(v); }
DI float fmx(float a, float b) { return __builtin_elementwise_maximum(a, b); }
DI void swap16(float& a, float& b) {
  auto r = __builtin_amdgcn_permlane16_swap(__builtin_bit_cast(unsigned, a), __builtin_bit_cast(unsigned, b), false, false);
  const unsigned r0 = r[0], r1 = r[1]; a = __builtin_bit_cast(float, r0); b = __builtin_bit_cast(float, r1);
}
DI void swap32(float& a, float& b) {
  auto r = __builtin_amdgcn_permlane32_swap(__builtin_bit_cast(unsigned, a), __builtin_bit_cast(unsigned, b), false, false);
  const unsigned r0 = r[0], r1 = r[1]; a = __builtin_bit_cast(float, r0); b = __builtin_bit_cast(float, r1);
}
template <class T> DI void pin(T& v) { asm volatile("" : "+v"(v)); }
DI float pack_max(float a, float b, float c, float d) { swap16(a, b); a = fmx(a, b); swap16(c, d); c = fmx(c, d); swap32(a, c); return fmx(a, c); }
DI float pack_sum(float a, float b, float c, float d) { swap16(a, b); a = a + b; swap16(c, d); c = c + d; swap32(a, c); return a + c; }
DI void unpack4(float x, float& a, float& b, float& c, float& d) { a = x; c = x; swap32(a, c); b = a; swap16(a, b); d = c; swap16(c, d); }
template <int OFF> DI u32x4 gload(unsigned voff, u32x4 srd, unsigned soff) {
  u32x4 r; asm volatile("buffer_load_dwordx4 %0, %1, %2, %3 offen offset:%4" : "=v"(r) : "v"(voff), "s"(srd), "s"(soff), "i"(OFF)); return r;
}
DI void touch(unsigned lds_dst, unsigned voff, u32x4 srd, unsigned soff) {
  asm volatile("s_mov_b32 m0, %0\n\ts_nop 0\n\tbuffer_load_dword %1, %2, %3 offen lds" ::"s"(lds_dst), "v"(voff), "s"(srd), "s"(soff) : "memory");
}
constexpr unsigned OOB = 0x7fffffffu;
DI u32x4 code_srd() {
  unsigned long long pc; unsigned len;
  asm volatile("s_getpc_b64 %0\n.Lcs%=:\n\ts_mov_b32 %1, .Lkernel_end-.Lcs%=\n\ts_max_i32 %1, %1, 0\n\ts_min_i32 %1, %1, 0x10000" : "=s"(pc), "=s"(len) :: "scc");
  u32x4 r; r[0] = (unsigned)pc; r[1] = (unsigned)(pc >> 32); r[2] = len; r[3] = 0x00020000u; return r;
}
DI u64 poll_load(const gu64* p) { u64 r; asm volatile("global_load_dwordx2 %0, %1, off sc1" : "=v"(r) : "v"(p) : "memory"); return r; }
template <int N> DI void waitv_tie(u64& r) { asm volatile("s_waitcnt vmcnt(%1)" : "+v"(r) : "i"(N)); }
template <int N> DI void waitv_tie(u32x4& r) { asm volatile("s_waitcnt vmcnt(%1)" : "+v"(r) : "i"(N)); }
constexpr unsigned POISON = 0xffffffffu;
DI unsigned nopoison(unsigned v) { return v < POISON - 1 ? v : POISON - 1; }
DI bool clean6(const u32x4 (&v)[6]) {
  unsigned m = 0;
#pragma unroll
  for (int i = 0; i < 6; i++) m = max(max(m, max(v[i][0], v[i][1])), max(v[i][2], v[i][3]));
  return __all(m != POISON) != 0;
}
DI u64 next_tag(u64 e) { return (e ^ 0x9E3779B97F4A7C15ull) * 0xD1342543DE82EF95ull + 0x632BE59BD9B4E019ull; }
DI u64 rd_epoch(char* ws, int rt) {
  return poll_load((const gu64*)(ws + EPOCH_OFF) + rt * 16);
}
DI u64 epoch_uniform(u64 e) {
  asm volatile("" : "+v"(e));
  return ((u64)(unsigned)__builtin_amdgcn_readfirstlane((unsigned)(e >> 32)) << 32) | (unsigned)__builtin_amdgcn_readfirstlane((unsigned)e);
}
DI void set_flag(char* ws, int rt, int n, int src, u64 tag) {
  __hip_atomic_store((gu64*)(ws + FLAG_OFF) + (rt * 9 + n) * 16 + src, tag, RLX_AGENT);
}
typedef short s16x4 __attribute__((ext_vector_type(4)));
struct Stat { f32x4 s1, s2; };
DI void stat_acc(const u32x4& c, Stat& st) {
  const s16x4 ones = {0x3f80, 0x3f80, 0x3f80, 0x3f80};
  const s16x4 a = __builtin_bit_cast(s16x4, (u32x2){c[0], c[1]}), b = __builtin_bit_cast(s16x4, (u32x2){c[2], c[3]});
  st.s1 = __builtin_amdgcn_mfma_f32_4x4x4bf16_1k(a, ones, st.s1, 0, 0, 0);
  st.s2 = __builtin_amdgcn_mfma_f32_4x4x4bf16_1k(a, a, st.s2, 0, 0, 0);
  st.s1 = __builtin_amdgcn_mfma_f32_4x4x4bf16_1k(b, ones, st.s1, 0, 0, 0);
  st.s2 = __builtin_amdgcn_mfma_f32_4x4x4bf16_1k(b, b, st.s2, 0, 0, 0);
}
template <bool WIDE> DI void stat_fin(const Stat& st, int lane, float& r, float& nmr) {
  float s1 = dppadd<0x141>((st.s1[0] + st.s1[1]) + (st.s1[2] + st.s1[3]));
  const float d01 = (lane & 1) ? st.s2[1] : st.s2[0], d23 = (lane & 1) ? st.s2[3] : st.s2[2];
  float s2 = red8((lane & 2) ? d23 : d01);
  if constexpr (WIDE) {
    s1 = dppadd<0x140>(s1); s2 = dppadd<0x140>(s2);
    float t1 = s1, t2 = s2; swap16(s1, t1); swap16(s2, t2); s1 += t1; s2 += t2;
  }
  const float m = s1 * (1.f / DM);
  const float var = fmaxf(s2 * (1.f / DM) - m * m, 0.f);
  r = __builtin_amdgcn_rsqf(var + 1e-5f); nmr = -m * r;
}
struct NormSt { float lo, hi; u32x4 o; };
template <int O> DI void norm_op(NormSt& st, const u32x4& c0, const u32x4& c1, const float (&rs)[2], const float (&nm)[2], unsigned addr) {
  constexpr int R = O / 21, Q = O % 21, J = Q / 5, K = Q % 5;
  const u32x4& c = R ? c1 : c0;
  if constexpr (Q == 20) lds_wr128(addr + R * 128, st.o);
  else if constexpr (K == 0) { st.lo = bflo(c[J]); pin(st.lo); }
  else if constexpr (K == 1) { st.hi = bfhi(c[J]); pin(st.hi); }
  else if constexpr (K == 2) { st.lo = __builtin_fmaf(st.lo, rs[R], nm[R]); pin(st.lo); }
  else if constexpr (K == 3) { st.hi = __builtin_fmaf(st.hi, rs[R], nm[R]); pin(st.hi); }
  else { unsigned t = cvtpk(st.lo, st.hi); pin(t); st.o[J] = t; }
}
DI unsigned norm2(unsigned p, float r, float nmr) { return cvtpk(__builtin_fmaf(bflo(p), r, nmr), __builtin_fmaf(bfhi(p), r, nmr)); }
DI u32x4 norm8(const u32x4& c, float r, float nmr) { return (u32x4){norm2(c[0], r, nmr), norm2(c[1], r, nmr), norm2(c[2], r, nmr), norm2(c[3], r, nmr)}; }

#define STAMP_DECL do {} while (0)
#define STAMP(idx) do {} while (0)
#define STAMP_FLUSH(kid) do {} while (0)

constexpr int p2_first(int j) { return j < 2 ? 0 : (j <= 7 ? 2 * j : j + 8); }
constexpr int p2_count(int j) { return j < 2 ? 0 : (j <= 7 ? 2 : (j + 8 <= 26 ? 1 : 0)); }
constexpr int POLL1 = 16, USE1 = 19, POLL2 = 25, LASTFLAG = 22;
constexpr int inbox_count(int j) { return (j >= USE1 + 1 && j <= USE1 + 3) ? 2 : 0; }
constexpr int f_touch(int t) { return t == 0 || t == 5 ? 1 : (t == 2 || t == 7 || t == 10 ? 2 : 0); }
constexpr int vm_allowed(int T, int need) {
  int after = 0;
  auto op = [&](int tag, int n) { if (tag == need) after = 0; else after += n; };
  op(0, 3); op(1, 3); op(2, 3); op(298, 1);
  if (T < 0) return after;
  for (int t = 0; t < 18; t++) {
    if (t <= 14) op(t + 3, 3);
    if (f_touch(t)) op(298, 1);
    if (t >= 8 && t < 14) op(297, 1);
    else if (t == 15) { op(100, 2); op(101, 2); }
    else if (t == 16) op(102, 2);
    else op(103, 2);
    if (t == T) return after;
  }
  if (T == 18) return after;
  for (int j = 0; j < 27; j++) {
    for (int c = 0; c < p2_count(j); c++) op(100 + p2_first(j) + c, 2);
    op(300, inbox_count(j));
    if (j >= 3 && j % 3 == 0) op(200 + j / 3 - 1, 2);
    if (j == POLL1) op(301, 1);
    if (j == POLL2) op(302, 1);
    if (19 + j == T) return after;
  }
  return 0;
}
constexpr int imin(int a, int b) { return a < b ? a : b; }
constexpr int p2_wait(int j) {
  int a = j < 26 ? vm_allowed(19 + j, 100 + j + 1) : 63;
  if (j >= 7 && j % 3 == 1 && j <= LASTFLAG) a = imin(a, vm_allowed(19 + j, 200 + j / 3 - 2));
  return a;
}
constexpr unsigned w1_slot(int s) { constexpr int pi[5] = {2, 0, 1, 4, 3}; return 32768u + 24576u * pi[s % 5]; }
constexpr unsigned w2_slot(int j) { constexpr int pi[10] = {8, 9, 7, 6, 0, 1, 2, 3, 4, 5}; return 16384u * pi[j % 10]; }
constexpr unsigned inbox_at(int a, int i) { return w2_slot(17 + 3 * a + i / 2) + (i & 1) * 1024u; }
constexpr unsigned F_OWN = w2_slot(23);
constexpr unsigned F_DUMMY = 159744;

DI void ffn_path(const u16* x, const u16* w1, const u16* w2, char* ws, u16* out, int rt, int sp) {
  const int tid = threadIdx.x, lane = tid & 63;
  const int w = __builtin_amdgcn_readfirstlane(tid >> 6), tg = w >> 2, cg = w & 3;
  const int g = lane >> 4, q = (lane & 15) >> 2, p = lane & 3;
  STAMP_DECL; STAMP(0);
  const u32x4 srdx = make_srd(x, XN_BYTES), srd1 = make_srd(w1, DM * DFF * 2), srd2 = make_srd(w2, DM * DFF * 2);
  const int i8 = lane >> 3, c8 = lane & 7;
  const unsigned vox = i8 * (DM * 2) + c8 * 16;
  const unsigned sox = (unsigned)((rt * 128 + 16 * w) * (DM * 2));
  u64 epoch = rd_epoch(ws, rt);
  u32x4 xr[2][9];
  sfor<9>([&](auto tc) { constexpr int t = decltype(tc)::value;
    xr[0][t] = gload<t * 128>(vox, srdx, sox); xr[1][t] = gload<t * 128>(vox, srdx, sox + 8 * DM * 2); });
  const unsigned swz = 16u * (c8 ^ (2 * ((lane >> 4) & 3)));
  const unsigned vo1 = (8 * (i8 >> 2) + (i8 & 3)) * (DFF * 2) + swz, vo2 = i8 * (DM * 2) + swz;
  unsigned so1[3];
#pragma unroll
  for (int i = 0; i < 3; i++) { const int id = 3 * w + i, rg = id & 3; so1[i] = (unsigned)((16 * (rg & 1) + 4 * (rg >> 1)) * (DFF * 2) + (sp * 384 + (id >> 2) * 64) * 2); }
  const unsigned so2 = (unsigned)((sp * 384 + 16 * w) * (DM * 2));
  const int grp = sp >> 2, rr = sp & 3;
  int chunk[9];
#pragma unroll
  for (int pp = 0; pp < 9; pp++)
    chunk[pp] = pp < 6 ? 3 * ((grp + 1 + pp / 3) % 3) + (pp + rr) % 3 : 3 * grp + ((0x918486u >> (6 * rr + 2 * (pp - 6))) & 3);
  const unsigned m1 = (unsigned)(3 * w * 1024), m2 = (unsigned)(2 * w * 1024);
  auto load1 = [&](auto Sc, auto Ic) {
    constexpr int S = decltype(Sc)::value, I = decltype(Ic)::value;
    dma(w1_slot(S) + m1 + I * 1024, vo1, srd1, so1[I] + (unsigned)(S * 32 * DFF * 2));
  };
  auto load2 = [&](auto Jc, auto Ic) {
    constexpr int J = decltype(Jc)::value, I = decltype(Ic)::value;
    dma(w2_slot(J) + m2 + I * 1024, vo2, srd2, so2 + (unsigned)(((J % 3) * 128 + 8 * I) * (DM * 2)) + (unsigned)chunk[J / 3] * 128);
  };
  sfor<3>([&](auto i) { load1(IC<0>{}, i); });
  const unsigned tv1 = (lane < 16 && c8 < 6 && w < 5) ? (unsigned)((w * 32 + 2 * rt) * (DFF * 2)) + i8 * (DFF * 2) + c8 * 128 : OOB, tv2 = lane < 9 ? lane * 128u : OOB;
  float rs[2], nm[2];
  {
    Stat st[2] = {};
    sfor<18>([&](auto ic) { constexpr int i = decltype(ic)::value;
      waitv_tie<3 + (i > 5 ? 3 : 0) + (i > 11 ? 4 : 0) + 17 - i>(xr[i & 1][i >> 1]); stat_acc(xr[i & 1][i >> 1], st[i & 1]); GAP();
      if constexpr (i == 5) sfor<3>([&](auto k) { load1(IC<1>{}, k); });
      if constexpr (i == 11) { sfor<3>([&](auto k) { load1(IC<2>{}, k); }); touch(F_DUMMY, tv1, srd1, (unsigned)(sp * 768 + 3 * 32 * DFF * 2)); } });
    stat_fin<false>(st[0], lane, rs[0], nm[0]); stat_fin<false>(st[1], lane, rs[1], nm[1]);
    epoch = epoch_uniform(epoch);
  }
  const unsigned xw = (c8 >> 2) * 8192 + w * 1024 + (16 * (c8 & 3) + (i8 ^ c8)) * 16;
  NormSt ns;
  auto xop = [&](auto Tc, auto Oc) {
    constexpr int T = decltype(Tc)::value, O = decltype(Oc)::value;
    if constexpr (T < 9 && O < 42) norm_op<O>(ns, xr[0][T < 9 ? T : 0], xr[1][T < 9 ? T : 0], rs, nm, xw + (T & 1) * 16384);
  };
  sfor<2>([&](auto r) { constexpr int R = decltype(r)::value;
    lds_wr128(xw + R * 128, norm8(xr[R][0], rs[R], nm[R])); });
  sfor<12>([&](auto o) { xop(IC<1>{}, o); });

  const int row = 4 * g + q;
  const unsigned wb = row * 128 + 32 * (cg ^ ((row >> 1) & 3)) + 8 * p;
  unsigned wbh = wb + 65536, wbh2 = wb + 131072;
  pin(wbh); pin(wbh2);
  auto waddr = [&](auto Oc) { constexpr unsigned O = decltype(Oc)::value; return O < 65536 ? wb + O : (O < 131072 ? wbh + (O - 65536) : wbh2 + (O - 131072)); };
  const unsigned xb0 = tg * 4096 + (16 * g + ((lane & 15) ^ g)) * 16, xb1 = xb0 ^ 64;

  const __amdgpu_buffer_rsrc_t prs = __builtin_amdgcn_make_buffer_rsrc(ws + PART_OFF, 0, PART_BYTES, 0x00020000);
  const unsigned pso = (unsigned)(((rt * 9) * 16 + sp) * 16384 + w * 2048);
  const u64 tag = next_tag(epoch);
  unsigned poff[3];
#pragma unroll
  for (int i = 0; i < 3; i++) {
    const int sg = 6 * w + 2 * i, pc = 3 * sp + (sg >> 4);
    poff[i] = (unsigned)((rt * 9 + (pc >> 2)) * (16 * 16384) + ((pc >> 1) & 1) * 8192 + ((sg >> 2) & 3) * 2048 + (pc & 1) * 1024 + (sg & 3) * 256);
    asm volatile("" : "+s"(poff[i]));
  }
  f32x4 acc[6][4];
  bf16x8 xf[2][4];
  bf16x4 wlo[4], whi[4];
  auto rdw1 = [&](auto GIc, auto Hc) {
    constexpr int GI = decltype(GIc)::value, H = decltype(Hc)::value;
    constexpr int O = w1_slot(GI / 6) + (GI % 6) * 4096 + H * 2048;
    if constexpr (H == 0) wlo[GI & 3] = lds_tr(waddr(IC<O>{})); else whi[GI & 3] = lds_tr(waddr(IC<O>{}));
  };
  auto rdx = [&](auto Sc, auto Tc) {
    constexpr int S = decltype(Sc)::value, T = decltype(Tc)::value;
    xf[S & 1][T] = lds_rd128(((S & 1) ? xb1 : xb0) + (S & 3) * 8192 + T * 1024);
  };
  waitv<vm_allowed(-1, 0)>(); waitl0(); bar();
  STAMP(1);
  rdw1(IC<0>{}, IC<0>{}); rdw1(IC<0>{}, IC<1>{});
  sfor<4>([&](auto t) { rdx(IC<0>{}, t); });
  rdw1(IC<1>{}, IC<0>{}); rdw1(IC<1>{}, IC<1>{}); rdw1(IC<2>{}, IC<0>{}); rdw1(IC<2>{}, IC<1>{});
  const unsigned hw = lane * 16 + (tg * 16 + cg) * 1024;
  unsigned hwh = hw + 65536; pin(hwh);
  auto hpack = [&](auto Uc, auto Tc) {
    constexpr int U = decltype(Uc)::value, T = decltype(Tc)::value;
    u32x4 o;
    o[0] = relupk(cvtpk(acc[2 * U][T][0], acc[2 * U][T][1])); o[1] = relupk(cvtpk(acc[2 * U][T][2], acc[2 * U][T][3]));
    o[2] = relupk(cvtpk(acc[2 * U + 1][T][0], acc[2 * U + 1][T][1])); o[3] = relupk(cvtpk(acc[2 * U + 1][T][2], acc[2 * U + 1][T][3]));
    lds_wr128((U == 2 ? hwh : hw + U * 32768) + T * 4096, o);
  };
  GAP();
  sfor<108>([&](auto GIc) {
    constexpr int GI = decltype(GIc)::value, S = GI / 6, SB = GI % 6;
    const bf16x8 a = cat(wlo[GI & 3], whi[GI & 3]);
    sfor<4>([&](auto Tc) {
      constexpr int T = decltype(Tc)::value;
      if constexpr (S == 0) mma0(acc[SB][T], a, xf[0][T]); else mma(acc[SB][T], a, xf[S & 1][T]);
      GAP();
      if constexpr (SB == 3 && T == 0) {
        if constexpr (S < 17) { waitv<vm_allowed(S, S + 1)>(); if constexpr ((S & 1) == 1) asm volatile("s_waitcnt lgkmcnt(4)" ::: "memory");
          bar(); }
        else { waitl0(); bar(); }
      }
      {
        constexpr int GP = SB * 4 + T;
        if constexpr ((S & 1) == 1 && SB >= 3) xop(IC<(S + 3) / 2>{}, IC<GP - 12>{});
        else if constexpr ((S & 1) == 0) { xop(IC<S / 2 + 1>{}, IC<GP + 12 + (GP > 8) + (GP > 16)>{}); if constexpr (GP == 8 || GP == 16) xop(IC<S / 2 + 1>{}, IC<GP + 13 + (GP > 8)>{}); }
        else if constexpr (GP < 4) xop(IC<(S + 1) / 2>{}, IC<GP + 38>{});
      }
      if constexpr (SB == 2 && T == 3 && f_touch(S) == 1) touch(F_DUMMY, tv1, srd1, (unsigned)(sp * 768 + (S + 8) * 32 * DFF * 2));
      if constexpr (SB == 2 && T == 3 && f_touch(S) == 2) touch(F_DUMMY, tv2, srd2, (unsigned)((sp * 384 + rt * 24 + w * 3 + (S == 2 ? 0 : (S == 7 ? 1 : 2))) * (DM * 2)));
      if constexpr (SB == 2 && T == 3 && S >= 8 && S < 14)
        __builtin_amdgcn_raw_buffer_store_b128((u32x4){POISON, POISON, POISON, POISON}, prs, lane * 16 + (S & 1) * 1024, pso + (unsigned)(3 * grp + (S - 8) / 2) * (16 * 16384), ST_AUX);
      if constexpr (T < 2 && GI + 3 < 108) rdw1(IC<GI + 3>{}, Tc);
      if constexpr (T == 2 && SB < 3) {
        if constexpr (S <= 14) load1(IC<S + 3>{}, IC<SB % 3>{});
        else if constexpr (S == 15) { load2(IC<(SB >> 1)>{}, IC<(SB & 1)>{}); if constexpr (SB == 2) load2(IC<1>{}, IC<1>{}); }
        else if constexpr (SB < 2) load2(IC<S - 14>{}, IC<SB>{});
      }
      if constexpr (S < 17 && T >= 2 && (SB == 3 || SB == 4)) rdx(IC<S + 1>{}, IC<(SB - 3) * 2 + (T - 2)>{});
      if constexpr (S == 17 && (SB == 3 || SB == 5)) hpack(IC<(SB - 3) / 2>{}, Tc);
      GAP();
    });
  });
  STAMP(2);
  nop<7>(); GAP();
  sfor<4>([&](auto t) { hpack(IC<2>{}, t); });
  waitv<vm_allowed(18, 100)>(); waitl0(); bar();
  if (tid < 3) set_flag(ws, rt, 3 * grp + tid, sp, tag);
  STAMP(3);

  bf16x8 hf[4][12];
  f32x4 oc[4];
  const unsigned hb = lane * 16 + tg * 16384; unsigned hbh = hb + 65536; pin(hbh);
  auto rdh = [&](auto Ic, auto Tc) {
    constexpr int I = decltype(Ic)::value, T = decltype(Tc)::value; constexpr int U = I >> 2;
    hf[T][I] = lds_rd128((U == 2 ? hbh : hb + U * 32768) + T * 4096 + (I & 3) * 1024);
  };
  auto rdw2 = [&](auto GIc, auto Hc) {
    constexpr int GI = decltype(GIc)::value, H = decltype(Hc)::value;
    constexpr int O = w2_slot(GI >> 2) + (GI & 3) * 2048 + H * 8192;
    if constexpr (H == 0) wlo[GI & 3] = lds_tr(waddr(IC<O>{})); else whi[GI & 3] = lds_tr(waddr(IC<O>{}));
  };
  rdw2(IC<0>{}, IC<0>{}); rdw2(IC<0>{}, IC<1>{});
  sfor<4>([&](auto t) { rdh(IC<0>{}, t); });
  rdw2(IC<1>{}, IC<0>{}); rdw2(IC<1>{}, IC<1>{});
  sfor<4>([&](auto t) { rdh(IC<1>{}, t); });
  rdw2(IC<2>{}, IC<0>{}); rdw2(IC<2>{}, IC<1>{});
  unsigned pk[8];
  const u32x4 srdp = make_srd(ws + PART_OFF, PART_BYTES);
  const int c16 = lane & 15;
  const unsigned pvo = g * 16384 + c16 * 16;
  const gu64* fp = (const gu64*)(ws + FLAG_OFF) + (rt * 9 + ((lane & 16) ? (3 * sp + 2) >> 2 : (3 * sp) >> 2)) * 16 + c16;
  const int gsrc[2] = {(grp + 1) % 3, (grp + 2) % 3};
  u64 fl; bool pre0; unsigned pso2;
  auto inbox = [&](auto Ac, auto Ic, unsigned so) {
    constexpr int A = decltype(Ac)::value, I = decltype(Ic)::value;
    if constexpr (I >= 0 && I < 6) dma_sc1(inbox_at(A, I) + m2, pvo, srdp, so + poff[I / 2] + (I & 1) * 256);
  };
  auto own_idx = [&](int pp, int half) { return (unsigned)(4 * chunk[pp] + 2 * tg + half - 3 * sp); };
  const unsigned own_w = F_OWN + cg * 1024 + lane * 16;
  auto ready = [&](u64 f, int gs) { return __all(f == tag || (c16 >> 2) != gs || c16 == sp) != 0; };
  GAP();
  sfor<108>([&](auto GIc) {
    constexpr int GI = decltype(GIc)::value, J = GI >> 2, C = GI & 3, I = GI % 12, N = GI / 12;
    if constexpr (GI == 12) STAMP(4);
    if constexpr (GI == 36) STAMP(5);
    const bf16x8 a = cat(wlo[GI & 3], whi[GI & 3]);
    sfor<4>([&](auto Tc) {
      constexpr int T = decltype(Tc)::value;
      if constexpr (I == 0 && N > 0) { if constexpr (T == 0) { nop<2>(); GAP(); } pk[2 * T] = cvtpk(oc[T][0], oc[T][1]); pk[2 * T + 1] = cvtpk(oc[T][2], oc[T][3]); GAP(); }
      if constexpr (I == 0) mma0(oc[T], a, hf[T][I]); else mma(oc[T], a, hf[T][I]);
      GAP();
      if constexpr (I == 0 && N > 6) { pk[2 * T] = nopoison(pk[2 * T]); pk[2 * T + 1] = nopoison(pk[2 * T + 1]); }
      if constexpr (C == 1 && T == 0 && J < 26) {
        waitv<p2_wait(J)>();
        bar();
        if constexpr (J >= 7 && J % 3 == 1 && J <= LASTFLAG) { if (tid == 0) set_flag(ws, rt, chunk[J >= 6 ? J / 3 - 2 : 0], sp, tag); }
      }
      if constexpr (C == 1 && T == 1 && J == USE1) {
        waitv_tie<vm_allowed(19 + J, 301)>(fl);
        pre0 = ready(fl, gsrc[0]);
        pso2 = pre0 ? (unsigned)gsrc[0] * 65536u : 0x80000000u;
      }
      if constexpr (C == 0 && T == 3 && (J == POLL1 || J == POLL2)) fl = poll_load(fp);
      if constexpr (GI + 3 < 108 && T < 2) rdw2(IC<GI + 3>{}, Tc);
      if constexpr (GI + 2 < 12) rdh(IC<GI + 2>{}, Tc);
      if constexpr (I == 0 && N > 0 && (T & 1)) {
        unsigned so = pso + (unsigned)chunk[N > 0 ? N - 1 : 0] * (16 * 16384);
        if constexpr (N == 8) so = own_idx(7, T >> 1) < 3u ? 0x80000000u : so;
        __builtin_amdgcn_raw_buffer_store_b128((u32x4){pk[2 * T - 2], pk[2 * T - 1], pk[2 * T], pk[2 * T + 1]}, prs, lane * 16 + (T >> 1) * 1024, so, ST_AUX);
      }
      if constexpr (I == 1 && N == 8 && T >= 2) {
        const unsigned oi = own_idx(7, T - 2);
        if (oi < 3u) lds_wr128(own_w + oi * 4096, (u32x4){pk[4 * T - 8], pk[4 * T - 7], pk[4 * T - 6], pk[4 * T - 5]});
      }
      if constexpr (C >= 2 && J < 26) {
        constexpr int cnt = p2_count(J + 1);
        if constexpr (cnt == 1) { if constexpr (T == 2) load2(IC<p2_first(J + 1)>{}, IC<C - 2>{}); }
        if constexpr (cnt == 2) { if constexpr (T >= 2) load2(IC<p2_first(J + 1) + C - 2>{}, IC<T - 2>{}); }
      }
      if constexpr (C >= 2 && J >= USE1 && J < USE1 + 3 && T == 2) inbox(IC<0>{}, IC<2 * (J - USE1) + C - 2>{}, pso2);
      GAP();
    });
  });
  nop<7>(); GAP();
  sfor<2>([&](auto hc) {
    constexpr int H = decltype(hc)::value;
    u32x4 o = {nopoison(cvtpk(oc[2 * H][0], oc[2 * H][1])), nopoison(cvtpk(oc[2 * H][2], oc[2 * H][3])), nopoison(cvtpk(oc[2 * H + 1][0], oc[2 * H + 1][1])), nopoison(cvtpk(oc[2 * H + 1][2], oc[2 * H + 1][3]))};
    const unsigned oi = own_idx(8, H);
    __builtin_amdgcn_raw_buffer_store_b128(o, prs, lane * 16 + H * 1024, oi < 3u ? 0x80000000u : pso + (unsigned)chunk[8] * (16 * 16384), ST_AUX);
    if (oi < 3u) lds_wr128(own_w + oi * 4096, o);
  });
  STAMP(6);
  waitv_tie<2>(fl);
  bool pre1 = ready(fl, gsrc[1]);
  sfor<6>([&](auto i) { inbox(IC<1>{}, i, pre1 ? (unsigned)gsrc[1] * 65536u : 0x80000000u); });
  unsigned ooff[3]; u32x2 xv[3];
#pragma unroll
  for (int i = 0; i < 3; i++) {
    const int sg = 6 * w + 2 * i, pc = 3 * sp + (sg >> 4);
    ooff[i] = (unsigned)((rt * 128 + (pc & 3) * 32 + (g & 1) * 16 + c16) * DM + (pc >> 2) * 64 + (sg & 15) * 4 + (g >> 1) * 4);
    xv[i] = *(const u32x2*)(x + ooff[i]);
  }
  u32x4 sel;
#pragma unroll
  for (int j = 0; j < 4; j++) sel[j] = ((c16 & 7) >> 1) == j ? ((c16 & 1) ? 0x3f800000u : 0x00003f80u) : 0u;
  const u32x4 z4 = {0u, 0u, 0u, 0u};
  const bf16x8 af = __builtin_bit_cast(bf16x8, c16 < 8 ? sel : z4), as = __builtin_bit_cast(bf16x8, c16 < 8 ? z4 : sel);
  f32x4 dd[6];
  unsigned ib = lane * 16 + w * 2048, ibh = ib + 65536, ibh2 = ib + 131072; pin(ibh); pin(ibh2);
  auto rdin = [&](auto Ac, auto Ic) { constexpr unsigned O = inbox_at(decltype(Ac)::value, decltype(Ic)::value);
    return lds_rd128(O < 65536 ? ib + O : (O < 131072 ? ibh + (O - 65536) : ibh2 + (O - 131072))); };
  sfor<6>([&](auto ic) { dd[decltype(ic)::value] = mfma(af, rdin(IC<0>{}, ic), (f32x4){0.f, 0.f, 0.f, 0.f}); });
  waitl0(); bar();
  {
    const bf16x8 af0 = __builtin_bit_cast(bf16x8, lane < 8 ? sel : z4);
    const unsigned orb = F_OWN + c16 * 16 + w * 1536;
    sfor<6>([&](auto ic) { constexpr int i = decltype(ic)::value; dd[i] = mfma(af0, lds_rd128(orb + i * 256), dd[i]); });
  }
  int spin = 0;
  if (!__all(fl == tag || c16 == sp || c16 >= 12)) {
    for (; spin < 200000; spin++) {
      __builtin_amdgcn_s_sleep(1);
      fl = poll_load(fp); waitv_tie<0>(fl);
      if (__all(fl == tag || c16 == sp || c16 >= 12)) break;
    }
  }
  STAMP(7);
  auto ld6 = [&](u32x4 (&v)[6], unsigned vo, int gs) {
#pragma unroll
    for (int i = 0; i < 6; i++) v[i] = __builtin_amdgcn_raw_buffer_load_b128(prs, vo, poff[i >> 1] + (i & 1) * 256 + gs * 65536, 16);
  };
  u32x4 po[6], pa[6];
#pragma unroll
  for (int k = 0; k < 2; k++) {
    if (!(k ? pre1 : pre0)) {
      ld6(po, pvo, gsrc[k]);
#pragma unroll
      for (int i = 0; i < 6; i++) dd[i] = mfma(af, __builtin_bit_cast(bf16x8, po[i]), dd[i]);
    }
  }
  const unsigned pvo_o = g == rr ? OOB : pvo;
  bool oka = false, oko = false;
  for (; spin < 200000; spin++) {
    asm volatile("" ::: "memory");
    const bool ra = !oka && ready(fl, 3);
    if (!oko) ld6(po, pvo_o, grp);
    if (ra) { ld6(pa, pvo, 3); oka = true; }
    if (!oka) fl = __hip_atomic_load(fp, RLX_AGENT);
    oko = clean6(po);
    if (oka && oko) break;
    if (oko) __builtin_amdgcn_s_sleep(1);
  }
  STAMP(8);
  waitv<0>();
  sfor<6>([&](auto ic) { constexpr int i = decltype(ic)::value;
    dd[i] = mfma(af, rdin(IC<1>{}, ic), dd[i]); dd[i] = mfma(af, __builtin_bit_cast(bf16x8, po[i]), dd[i]); dd[i] = mfma(as, __builtin_bit_cast(bf16x8, pa[i]), dd[i]); });
  STAMP(9);
#pragma unroll
  for (int i = 0; i < 3; i++) {
    float o[4];
#pragma unroll
    for (int j = 0; j < 4; j++) {
      float fv = dd[2 * i][j], sv = dd[2 * i + 1][j];
      swap32(fv, sv);
      const float gt = fv * __builtin_amdgcn_rcpf(1.f + __builtin_amdgcn_exp2f(-1.4426950408889634f * fv));
      o[j] = ((j & 1) ? bfhi(xv[i][j >> 1]) : bflo(xv[i][j >> 1])) + sv * gt;
    }
    *(u32x2*)(out + ooff[i]) = (u32x2){cvtpk(o[0], o[1]), cvtpk(o[2], o[3])};
  }
  if (sp == 0 && tid == 0) __hip_atomic_store((gu64*)(ws + EPOCH_OFF) + rt * 16, tag, RLX_AGENT);
  STAMP_FLUSH(1);
  asm volatile(".Lkernel_end:");
}

constexpr unsigned A_SLOT = 27648, A_XR = 110592, A_HALO = 143360, A_DUMMY = 161792, A_WOH = 125952, A_IMG = 872, A_OB = 125568, A_OS = 296;
constexpr unsigned wo_slot(int n) { return n < 6 ? 18432u * n : (n < 8 ? A_WOH + 18432u * (n - 6) : 0u); }
constexpr int a_touch(int s) { return ((s == -1 || s == 1 || s == 3 || s == 5 || s == 9) ? 1 : 0); }
constexpr int wo_allowed(int N, int need) {
  int after = 0;
  auto op = [&](int tag) { if (tag == need) after = 0; else after += 2; };
  for (int n = 0; n < 6; n++) op(n);
  for (int n = 0; n <= N; n++) {
    if (n >= 2) op(20 + n - 1);
    if (n == N) break;
    if (n == 0) { op(6); op(7); }
    if (n == 1) { op(8); op(20); }
  }
  return after;
}
constexpr int item_pos(int cg, int tg, int C) {
  if (cg == 3) { const int i = (C < 3 ? C : C - 1) + 3 * tg, j = i >= 6 ? i - 6 : i; return j == 2 ? 25 : (j == 5 ? 26 : 20 + j - (j > 2)); }
  if (C < 4) return 8 + 4 * cg + (C ^ (2 * tg));
  return (cg == 2 && C == 6) ? 24 : 3 * cg + C - 4;
}

constexpr u64 pos_table(int C) { u64 t = 0; for (int w = 0; w < 8; w++) t |= (u64)item_pos(w & 3, w >> 2, C) << (5 * w); return t; }
template <int C> DI unsigned pos_of(int w) { return (unsigned)(pos_table(C) >> (5 * w)) & 31u; }

DI void att_tail(const u16* wo, char* ws, int rt, int hg, int w, u64 tag
) {
  int tid = threadIdx.x; pin(tid);
  const int lane = tid & 63, tg = w >> 2, cg = w & 3;
  const int g = lane >> 4, q = (lane & 15) >> 2, p = lane & 3, c16 = lane & 15, i8 = lane >> 3, c8 = lane & 7;
  const u32x4 srdo = make_srd(wo, DM * DM * 2);
  const f32x4 zero4 = {0.f, 0.f, 0.f, 0.f};
  bf16x4 wlo[4], whi[4];
  auto wimg = [&](unsigned a, const f32x4& v) { lds_wr64(a, (u32x2){cvtpk(v[0], v[1]), cvtpk(v[2], v[3])}); };
  const unsigned swz = 16u * (c8 ^ (2 * ((lane >> 4) & 3)));
  const unsigned vo1 = (8 * (i8 >> 2) + (i8 & 3)) * (DM * 2) + swz, vo2 = i8 * (DM * 2) + swz;
  const unsigned soo = (unsigned)((hg * 144 + 32 * (w >> 1) + 4 * (w & 1)) * (DM * 2));
  auto loado = [&](auto Nc, auto Ic) {
    constexpr int N = decltype(Nc)::value, I = decltype(Ic)::value;
    if constexpr (I < 2) dma(wo_slot(N) + (2 * w + I) * 1024, vo1, srdo, soo + I * 16 * DM * 2 + N * 128);
    else if (w < 2) dma(wo_slot(N) + (16 + w) * 1024, vo2, srdo, (unsigned)((hg * 144 + 128 + 8 * w) * (DM * 2)) + N * 128);
  };
  {
    const int hp = w >> 1, par = w & 1;
    const unsigned r0 = (2 * c16 + par + 16) * A_IMG + 72 * hp, rm = (unsigned)max(2 * c16 + par - 16, 0) * A_IMG + 72 * hp + 288;
    unsigned rq[3] = {r0 + 8 * g, r0 + 8 * g + 40, r0 + 32};
    unsigned rk[4] = {rq[0] + 288, rq[1] + 288, rq[2] + 288, rq[2] + 288};
    unsigned rkm[4] = {rm + 8 * g, rm + 8 * g + 40, rm + 32, rm + 32};
    const int kr = 4 * g + q;
    unsigned rv = (2 * kr + par + 16) * A_IMG + 576 + 72 * hp + 8 * p, rvp = rv, rv3 = rv + 96 * A_IMG;
    unsigned rvm = (unsigned)max(2 * kr + par - 16, 0) * A_IMG + 576 + 72 * hp + 8 * p;
    unsigned rq3[3], rk3[4];
#pragma unroll
    for (int i = 0; i < 4; i++) { if (i < 3) { rq3[i] = rq[i] + 96 * A_IMG; pin(rq[i]); pin(rq3[i]); } rk3[i] = rk[i] + 96 * A_IMG; pin(rk[i]); pin(rk3[i]); pin(rkm[i]); }
    pin(rv); pin(rvp); pin(rv3); pin(rvm);
    f32x4 biasA, biasA0, biasB; unsigned mA[2], mA0[2], mB[2];
#pragma unroll
    for (int j = 0; j < 4; j++) {
      const int kk = 4 * g + j; const bool va = kk >= c16 + 9, vb = (kk <= c16) && (kk + 7 >= c16);
      biasA[j] = va ? 0.f : -1e30f; biasB[j] = vb ? 0.f : -1e30f; biasA0[j] = (va && rt > 0) ? 0.f : -1e30f;
      const unsigned bm = (j & 1) ? 0xffff0000u : 0x0000ffffu;
      if ((j & 1) == 0) { mA[j >> 1] = 0; mB[j >> 1] = 0; }
      mA[j >> 1] |= va ? bm : 0u; mB[j >> 1] |= vb ? bm : 0u;
    }
    mA0[0] = rt > 0 ? mA[0] : 0u; mA0[1] = rt > 0 ? mA[1] : 0u;
    pin(biasA); pin(biasA0); pin(biasB); pin(mA[0]); pin(mA[1]); pin(mA0[0]); pin(mA0[1]); pin(mB[0]); pin(mB[1]);
    unsigned qmask = g == 0 ? 0xffffffffu : 0u; pin(qmask);
    unsigned ow0 = A_OB + (2 * c16 + par) * A_OS + 72 * hp, owa = ow0 + 8 * g, owb = owa + 40; pin(ow0); pin(owa); pin(owb);
    waitl0(); bar();
    STAMP(3);
    auto asb = [](u32x2 a, u32x2 b) { return __builtin_bit_cast(bf16x8, (u32x4){a[0], a[1], b[0], b[1]}); };
    bf16x8 kf[5][2], vf[4][3]; u32x2 qm[4][2], q4[4];
    sfor<2>([&](auto Hc) { constexpr int H = decltype(Hc)::value; kf[0][H] = asb(lds_rd64(rkm[H]), lds_rd64(rkm[2 + H])); });
    sfor<4>([&](auto Uc) { constexpr int U = decltype(Uc)::value; constexpr unsigned ro = U < 3 ? U * 32 * A_IMG : 0;
      q4[U] = lds_rd64((U < 3 ? rq[2] : rq3[2]) + ro);
      sfor<2>([&](auto Hc) { constexpr int H = decltype(Hc)::value;
        qm[U][H] = lds_rd64((U < 3 ? rq[H] : rq3[H]) + ro);
        kf[U + 1][H] = asb(lds_rd64((U < 3 ? rk[H] : rk3[H]) + ro), lds_rd64((U < 3 ? rk[2 + H] : rk3[2 + H]) + ro)); });
      GAP(); });
    sfor<4>([&](auto Uc) { constexpr int U = decltype(Uc)::value;
      sfor<3>([&](auto Dc) { constexpr int D = decltype(Dc)::value; constexpr unsigned co = D == 0 ? 0 : (D == 1 ? 40 : 32);
        vf[U][D] = cat(U == 0 ? lds_tr(rvm + co) : lds_tr(rvp + (U > 0 ? U - 1 : 0) * 32 * A_IMG + co), U < 3 ? lds_tr(rv + U * 32 * A_IMG + co) : lds_tr(rv3 + co)); }); });
    GAP();
    f32x4 sa[8], sb[8];
    sfor<8>([&](auto Ic) { constexpr int I = decltype(Ic)::value, U = I >> 1, H = I & 1;
      const bf16x8 qf = asb(qm[U][H], H ? (u32x2){0u, q4[U][1] & qmask} : (u32x2){q4[U][0] & qmask, 0u});
      sa[I] = mfma(kf[U][H], qf, U == 0 ? biasA0 : biasA); sb[I] = mfma(kf[U + 1][H], qf, biasB); });
    GAP();
    waitl0(); bar();
    STAMP(4);
    sfor<3>([&](auto i) { loado(IC<0>{}, i); }); sfor<3>([&](auto i) { loado(IC<1>{}, i); });
    GAP();
    float v[8][4], mx[8];
    sfor<8>([&](auto Ic) { constexpr int I = decltype(Ic)::value;
#pragma unroll
      for (int j = 0; j < 4; j++) v[I][j] = fmx(sa[I][j], sb[I][j]);
      mx[I] = fmx(fmx(fmx(v[I][0], v[I][1]), v[I][2]), v[I][3]); });
    constexpr float SC = 0.23570226039551584f * 1.4426950408889634f;
    sfor<2>([&](auto hc) { constexpr int B = 4 * decltype(hc)::value;
      unpack4(-SC * pack_max(mx[B], mx[B + 1], mx[B + 2], mx[B + 3]), mx[B], mx[B + 1], mx[B + 2], mx[B + 3]); });
    GAP();
    sfor<3>([&](auto i) { loado(IC<2>{}, i); }); sfor<3>([&](auto i) { loado(IC<3>{}, i); });
    GAP();
    float sm[8];
    sfor<8>([&](auto Ic) { constexpr int I = decltype(Ic)::value;
#pragma unroll
      for (int j = 0; j < 4; j++) v[I][j] = __builtin_amdgcn_exp2f(__builtin_fmaf(v[I][j], SC, mx[I]));
      sm[I] = (v[I][0] + v[I][1]) + (v[I][2] + v[I][3]); });
    sfor<2>([&](auto hc) { constexpr int B = 4 * decltype(hc)::value;
      unpack4(__builtin_amdgcn_rcpf(pack_sum(sm[B], sm[B + 1], sm[B + 2], sm[B + 3])), sm[B], sm[B + 1], sm[B + 2], sm[B + 3]); });
    GAP();
    STAMP(5);
    sfor<3>([&](auto i) { loado(IC<4>{}, i); }); sfor<3>([&](auto i) { loado(IC<5>{}, i); });
    GAP();
    bf16x8 pf[8];
    sfor<8>([&](auto Ic) { constexpr int I = decltype(Ic)::value, U = I >> 1;
      const unsigned p0 = cvtpk(v[I][0] * sm[I], v[I][1] * sm[I]), p1 = cvtpk(v[I][2] * sm[I], v[I][3] * sm[I]);
      pf[I] = __builtin_bit_cast(bf16x8, (u32x4){p0 & (U == 0 ? mA0[0] : mA[0]), p1 & (U == 0 ? mA0[1] : mA[1]), p0 & mB[0], p1 & mB[1]}); });
    GAP();
    f32x4 o0[8], o1[8];
    sfor<8>([&](auto Ic) { constexpr int I = decltype(Ic)::value; o0[I] = mfma(vf[I >> 1][I & 1], pf[I], zero4); });
    sfor<8>([&](auto Ic) { constexpr int I = decltype(Ic)::value; o1[I] = mfma(vf[I >> 1][2], pf[I], zero4); });
    GAP();
    sfor<8>([&](auto Ic) { constexpr int I = decltype(Ic)::value; wimg(((I & 1) ? owb : owa) + (I >> 1) * 32 * A_OS, o0[I]); });
    if (g == 0) {
      sfor<4>([&](auto Uc) { constexpr int U = decltype(Uc)::value;
        lds_wr64(ow0 + 32 + U * 32 * A_OS, (u32x2){cvtpk(o1[2 * U][0], o1[2 * U][1]), cvtpk(o1[2 * U + 1][2], o1[2 * U + 1][3])}); });
    }
  }
  const int row = 4 * g + q;
  const unsigned ow = row * 128 + 32 * (cg ^ ((row >> 1) & 3)) + 8 * p; unsigned owh = ow + 65536, owh2 = ow + A_WOH; pin(owh); pin(owh2);
  unsigned ob = A_OB + (tg * 64 + c16) * A_OS + 16 * g, ob4 = A_OB + (tg * 64 + c16) * A_OS + 256 + 8 * g; pin(ob); pin(ob4);
  waitv<10>(); waitl0(); bar();
  STAMP(6);
  auto rdo = [&](auto GIc, auto Hc) {
    constexpr int GI = decltype(GIc)::value, H = decltype(Hc)::value, N = GI / 5, S = GI % 5;
    constexpr unsigned O = wo_slot(N) + S * 4096 + H * 2048;
    const unsigned a = O >= A_WOH ? owh2 + (O - A_WOH) : (O >= 65536 ? owh + (O - 65536) : ow + O);
    if constexpr (H == 0) wlo[GI & 3] = lds_tr(a); else if constexpr (S < 4) whi[GI & 3] = lds_tr(a);
  };
  bf16x8 of[4][4]; u32x2 of4[4];
  sfor<5>([&](auto Sc) { constexpr int S = decltype(Sc)::value;
    if constexpr (S < 3) { rdo(Sc, IC<0>{}); rdo(Sc, IC<1>{}); }
    sfor<4>([&](auto Tc) { constexpr int T = decltype(Tc)::value;
      if constexpr (S < 4) of[T][S & 3] = lds_rd128a8(ob + T * 16 * A_OS + 64 * S); else of4[T] = lds_rd64(ob4 + T * 16 * A_OS); });
    GAP(); });
  const __amdgpu_buffer_rsrc_t prs = __builtin_amdgcn_make_buffer_rsrc(ws + PART_OFF, 0, PART_BYTES, 0x00020000);
  const unsigned pso = (unsigned)(((rt * 9) * 16 + 12 + hg) * 16384 + w * 2048);
  f32x4 oc[4]; unsigned pk[8];
  GAP();
  sfor<45>([&](auto GIc) {
    constexpr int GI = decltype(GIc)::value, N = GI / 5, S = GI % 5;
    if constexpr (GI == 5) STAMP(7);
    if constexpr (GI == 15) STAMP(8);
    if constexpr (GI == 30) STAMP(9);
    const bf16x4 alo = wlo[GI & 3];
    bf16x8 a; if constexpr (S < 4) a = cat(alo, whi[GI & 3]);
    sfor<4>([&](auto Tc) {
      constexpr int T = decltype(Tc)::value;
      if constexpr (S == 0 && N > 0) { if constexpr (T == 0) { nop<2>(); GAP(); } pk[2 * T] = cvtpk(oc[T][0], oc[T][1]); pk[2 * T + 1] = cvtpk(oc[T][2], oc[T][3]); GAP(); }
      if constexpr (S == 0) mma0(oc[T], a, of[T][0]); else if constexpr (S < 4) mma(oc[T], a, of[T][S & 3]); else mma16(oc[T], alo, of4[T]);
      GAP();
      if constexpr (S == 2 && T == 0 && (N == 0 || N == 1 || N == 4)) {
        if constexpr (N == 0) waitl0();
        waitv<wo_allowed(N, N == 0 ? 1 : (N == 1 ? 5 : 8))>(); bar();
      }
      if constexpr (GI + 3 < 45 && T < 2) rdo(IC<GI + 3>{}, Tc);
      if constexpr (N == 0 && S >= 3 && T > 0) loado(IC<3 + S>{}, IC<T - 1>{});
      if constexpr (N == 1 && S == 2 && T > 0) loado(IC<8>{}, IC<T - 1>{});
      if constexpr (S == (N == 1 ? 3 : 0) && N > 0 && (T & 1))
        __builtin_amdgcn_raw_buffer_store_b128((u32x4){pk[2 * T - 2], pk[2 * T - 1], pk[2 * T], pk[2 * T + 1]}, prs, lane * 16 + (T >> 1) * 1024, pso + (N - 1) * 16 * 16384, ST_AUX);
      GAP();
    });
  });
  nop<7>(); GAP();
  sfor<2>([&](auto hc) {
    constexpr int H = decltype(hc)::value;
    u32x4 o = {nopoison(cvtpk(oc[2 * H][0], oc[2 * H][1])), nopoison(cvtpk(oc[2 * H][2], oc[2 * H][3])), nopoison(cvtpk(oc[2 * H + 1][0], oc[2 * H + 1][1])), nopoison(cvtpk(oc[2 * H + 1][2], oc[2 * H + 1][3]))};
    __builtin_amdgcn_raw_buffer_store_b128(o, prs, lane * 16 + H * 1024, pso + 8 * 16 * 16384, ST_AUX);
  });
  STAMP(10);
  waitv<0>(); bar();
  if (tid < 9) set_flag(ws, rt, tid, 12 + hg, tag);
  STAMP(11); STAMP_FLUSH(1);
}

DI void att_path(const u16* x, const u16* wqkv, const u16* wo, char* ws, int rt, int hg) {
  const int tid = threadIdx.x, lane = tid & 63;
  const int w = __builtin_amdgcn_readfirstlane(tid >> 6), tg = w >> 2, cg = w & 3;
  const int g = lane >> 4, q = (lane & 15) >> 2, p = lane & 3, c16 = lane & 15;
  STAMP_DECL; STAMP(0);
  const u32x4 srdx = make_srd(x, XN_BYTES), srdw = make_srd(wqkv, DM * 3 * DM * 2), srdo = make_srd(wo, DM * DM * 2);
  const int i8 = lane >> 3, c8 = lane & 7, l32 = lane & 31;
  const unsigned vox = i8 * (DM * 2) + c8 * 16;
  const unsigned sox = (unsigned)((rt * 128 + 16 * w) * (DM * 2));
  u64 epoch = rd_epoch(ws, rt);
  u32x4 xr[2][9], xh[3];
  {
    const unsigned soh = (unsigned)((rt > 0 ? rt * 128 - 16 + 2 * w : 0) * (DM * 2)), voh = (lane >> 5) * (DM * 2) + l32 * 16;
    xh[0] = gload<0>(voh, srdx, soh); xh[1] = gload<512>(voh, srdx, soh); xh[2] = gload<1024>(l32 < 8 ? voh : OOB, srdx, soh);
  }
  sfor<9>([&](auto tc) { constexpr int t = decltype(tc)::value;
    xr[0][t] = gload<t * 128>(vox, srdx, sox); xr[1][t] = gload<t * 128>(vox, srdx, sox + 8 * DM * 2); });
  unsigned vw[4];
#pragma unroll
  for (int i = 0; i < 4; i++) {
    const unsigned n = (w + 8 * i) * 64 + lane, r = (n * 9710u) >> 19, c = n - 54 * r;
    const unsigned k = 8 * ((r >> 2) & 3) + 4 * (r >> 4) + (r & 3);
    const unsigned sec = (c * 57u) >> 10;
    vw[i] = k * 3456 + c * 16 + sec * (1152 - 288);
  }
  auto loadw = [&](auto Sc, auto Ic) {
    constexpr int S = decltype(Sc)::value, I = decltype(Ic)::value;
    if constexpr (S < 18) { if (I < 3 || w < 3) dma(
(S & 3) * A_SLOT + (w + 8 * I) * 1024, vw[I], srdw, (unsigned)(S * 32 * 3456 + hg * 288)); }
  };
  sfor<4>([&](auto i) { loadw(IC<0>{}, i); });
  const unsigned tvw = (lane < 12 && p < 3) ? (unsigned)(((w >> 1) * 32 + 2 * rt + (w & 1)) * 3456 + 2 * hg * 128) + q * 1152 + p * 128 : OOB;
  unsigned wbs[7];
  auto img_off = [&](unsigned pos) { const unsigned left = 0u - (unsigned)(pos >= 24), odd = hg & 1;
    return ((288u * (pos >> 3) + 32u * ((pos & 7) + odd)) & ~left) | ((288u * (pos - 24) + 256u - 256u * odd) & left); };
  {
    const unsigned lr = (4 * g + q) * 864 + 8 * p;
    sfor<7>([&](auto Cc) { constexpr int C = decltype(Cc)::value;
      const unsigned pos = pos_of<C>(w);
      wbs[C] = lr + img_off(pos);
      pin(wbs[C]); });
  }
  const unsigned xs = (16 * g + (c16 ^ g)) * 16;
  unsigned xb0 = A_XR + tg * 4096 + xs, xb1 = xb0 ^ 64, hb0 = A_HALO + xs, hb1 = hb0 ^ 64; pin(xb0); pin(xb1); pin(hb0); pin(hb1);
  unsigned xw = A_XR + (c8 >> 2) * 8192 + (16 * (c8 & 3) + (i8 ^ c8)) * 16 + w * 1024; pin(xw);
  GAP();
  STAMP(12);
  {
    Stat st = {}; float r, nmr;
    sfor<3>([&](auto ic) { constexpr int i = decltype(ic)::value; waitv_tie<3 + 18 + 2 - i>(xh[i]); stat_acc(xh[i], st); });
    stat_fin<true>(st, lane, r, nmr);
    const unsigned hwa = A_HALO + (l32 >> 2) * 1024 + (16 * (l32 & 3) + ((2 * w + (lane >> 5)) ^ c8)) * 16;
    lds_wr128(hwa, norm8(xh[0], r, nmr)); lds_wr128(hwa + 8192, norm8(xh[1], r, nmr));
    if (l32 < 8) lds_wr128(hwa + 16384, norm8(xh[2], r, nmr));
  }
  GAP();
  float rs[2], nm[2];
  {
    Stat st[2] = {};
    sfor<18>([&](auto ic) { constexpr int i = decltype(ic)::value;
      waitv_tie<3 + (i > 7 ? 4 : 0) + 17 - i>(xr[i & 1][i >> 1]); stat_acc(xr[i & 1][i >> 1], st[i & 1]); GAP();
      if constexpr (i == 7) { sfor<4>([&](auto k) { loadw(IC<1>{}, k); }); touch(A_DUMMY, tvw, srdw, (unsigned)(2 * 32 * 3456)); } });
    stat_fin<false>(st[0], lane, rs[0], nm[0]); stat_fin<false>(st[1], lane, rs[1], nm[1]);
    epoch = epoch_uniform(epoch);
  }
  STAMP(13);
  NormSt ns;
  auto xop = [&](auto Tc, auto Oc) {
    constexpr int T = decltype(Tc)::value, O = decltype(Oc)::value;
    if constexpr (T < 9 && O < 42) norm_op<O>(ns, xr[0][T < 9 ? T : 0], xr[1][T < 9 ? T : 0], rs, nm, xw + (T & 1) * 16384);
  };
  sfor<2>([&](auto r) { constexpr int R = decltype(r)::value; lds_wr128(xw + R * 128, norm8(xr[R][0], rs[R], nm[R])); });
  lds_wr128(xw + 16384, norm8(xr[0][1], rs[0], nm[0]));

  const f32x4 zero4 = {0.f, 0.f, 0.f, 0.f};
  const u64 tag = next_tag(epoch);
  f32x4 acc[7][4], hac[3];
  sfor<4>([&](auto t) { acc[3][decltype(t)::value] = zero4; }); hac[2] = zero4;
  bf16x8 xf[4], xhf;
  bf16x4 wlo[4], whi[4];
  auto rdw = [&](auto GIc, auto Hc) {
    constexpr int GI = decltype(GIc)::value, H = decltype(Hc)::value, S = GI / 7, C = GI % 7;
    if constexpr (GI < 126) {
      const unsigned a = wbs[C] + (S & 1) * A_SLOT + H * 16 * 864;
      if constexpr (H == 0) wlo[GI & 3] = lds_tr(a); else whi[GI & 3] = lds_tr(a);
      if constexpr (H == 1 && (S & 1) == 1) { wbs[C] += (S & 2) ? -2 * A_SLOT : 2 * A_SLOT; pin(wbs[C]); }
    }
  };
  auto rdx = [&](auto Sc, auto Tc) {
    constexpr int S = decltype(Sc)::value, T = decltype(Tc)::value;
    xf[T] = lds_rd128(((S & 1) ? xb1 : xb0) + (S & 3) * 8192 + T * 1024);
  };
  auto rdxh = [&](auto Sc) { constexpr int S = decltype(Sc)::value; xhf = lds_rd128(((S & 1) ? hb1 : hb0) + S * 1024); };
  unsigned ih, ibase;
  unsigned toff[7];
  auto tileoff = [&](int C) { return toff[C]; };
  auto wimg = [&](unsigned a, const f32x4& v) { lds_wr64(a, (u32x2){cvtpk(v[0], v[1]), cvtpk(v[2], v[3])}); };
  STAMP(14);
  waitv<4>(); waitl0(); bar();
  STAMP(1);
  rdw(IC<0>{}, IC<0>{}); rdw(IC<0>{}, IC<1>{}); rdw(IC<1>{}, IC<0>{}); rdw(IC<1>{}, IC<1>{});
  sfor<4>([&](auto t) { rdx(IC<0>{}, t); });
  rdxh(IC<0>{});
  nop<1>();
  GAP();
  sfor<18>([&](auto Sc) {
    constexpr int S = decltype(Sc)::value, G0 = 7 * S;
    auto fill = [&](auto GPc) {
      constexpr int GP = decltype(GPc)::value;
      GAP();
      if constexpr (GP == 21) {
        if constexpr (S < 16) waitv<3 + a_touch(S) + a_touch(S - 1)>(); else if
 constexpr (S == 16) waitv<0>(); else waitl0();
        bar();
      }
      if constexpr (S == 17 && GP == 12) { int tz = tid; pin(tz); ih = (tz & 15) * A_IMG + 8 * ((tz >> 4) & 3); ibase = ih + (16 + tg * 64) * A_IMG;
        sfor<7>([&](auto Cc) { constexpr int C = decltype(Cc)::value; toff[C] = img_off(pos_of<C>(w)); }); }
      if constexpr (S == 0) xop(IC<1>{}, IC<GP + 21>{});
      else if constexpr ((S & 1) == 0) xop(IC<S / 2 + 1>{}, IC<GP + 8>{});
      else if constexpr (GP >= 22) xop(IC<(S + 3) / 2>{}, IC<GP - 22>{});
      else if constexpr (GP < 4 && S > 1) xop(IC<(S + 1) / 2>{}, IC<38 + GP>{});
      if constexpr (GP < 4) rdw(IC<G0 + 2 + (GP >> 1)>{}, IC<(GP & 1)>{});
      if constexpr (GP >= 8 && GP < 12) rdw(IC<G0 + 4 + ((GP - 8) >> 1)>{}, IC<(GP & 1)>{});
      if constexpr (GP == 15 || GP == 16) rdw(IC<G0 + 6>{}, IC<GP - 15>{});
      if constexpr (GP == 21 || GP == 22) rdw(IC<G0 + 7>{}, IC<GP - 21>{});
      if constexpr (GP == 24 || GP == 26) rdw(IC<G0 + 8>{}, IC<(GP - 24) / 2>{});
      if constexpr (S < 17) {
        if constexpr (GP >= 23 && (GP & 1) == 1) rdx(IC<S + 1>{}, IC<(GP - 23) / 2>{});
        if constexpr (GP == 28) rdxh(IC<S + 1>{});
      } else if constexpr (GP >= 21) {
        if constexpr (GP < 29) wimg(ibase + tileoff((GP - 21) >> 2) + ((GP - 21) & 3) * 16 * A_IMG, acc[(GP - 21) >> 2][(GP - 21) & 3]);
      }
      if constexpr (GP == 4 || GP == 6 || GP == 12 || GP == 17) loadw(IC<S + 2>{}, IC<(GP == 4 ? 0 : (GP == 6 ? 1 : (GP == 12 ? 2 : 3)))>{});
      if constexpr (GP == 18 && (S == 1 || S == 5 || S == 9)) touch(A_DUMMY, tvw, srdw, (unsigned)((S + 5) * 32 * 3456));
      if constexpr (GP == 18 && S == 3) touch(A_DUMMY, lane < 11 && w * 11 + lane < 81 ? lane * 128u : OOB, srdo, (unsigned)((hg * 144 + rt * 9) * (DM * 2) + w * 11 * 128));
      GAP();
    };
    auto mm = [&](f32x4& c, const bf16x8& a, const bf16x8& b) { if constexpr (S == 0) mma0(c, a, b); else mma(c, a, b); };
    const bf16x8 a0 = cat(wlo[G0 & 3], whi[G0 & 3]), a1 = cat(wlo[(G0 + 1) & 3], whi[(G0 + 1) & 3]);
    sfor<4>([&](auto Tc) { constexpr int T = decltype(Tc)::value;
      mm(acc[0][T], a0, xf[T]); fill(IC<2 * T>{}); mm(acc[1][T], a1, xf[T]); fill(IC<2 * T + 1>{}); });
    mm(hac[0], a0, xhf); fill(IC<8>{});
    mm(hac[1], a1, xhf); fill(IC<9>{});
    const bf16x8 a2 = cat(wlo[(G0 + 2) & 3], whi[(G0 + 2) & 3]);
    sfor<4>([&](auto Tc) { constexpr int T = decltype(Tc)::value; mm(acc[2][T], a2, xf[T]); fill(IC<10 + T>{}); });
    const bf16x8 a3 = cat(wlo[(G0 + 3) & 3], whi[(G0 + 3) & 3]);
    mma_alt3(acc[3][0], a3, xf[0], hac[2], a2, xhf, cg); fill(IC<14>{});
    sfor<3>([&](auto Tc) { constexpr int T = decltype(Tc)::value + 1; mma_not3(acc[3][T], a3, xf[T], cg); fill(IC<14 + T>{}); });
    const bf16x8 a4 = cat(wlo[(G0 + 4) & 3], whi[(G0 + 4) & 3]);
    sfor<4>([&](auto Tc) { constexpr int T = decltype(Tc)::value; mm(acc[4][T], a4, xf[T]); fill(IC<18 + T>{}); });
    const bf16x8 a5 = cat(wlo[(G0 + 5) & 3], whi[(G0 + 5) & 3]), a6 = cat(wlo[(G0 + 6) & 3], whi[(G0 + 6) & 3]);
    sfor<4>([&](auto Tc) { constexpr int T = decltype(Tc)::value;
      mm(acc[5][T], a5, xf[T]); fill(IC<22 + 2 * T>{}); mm(acc[6][T], a6, xf[T]); fill(IC<23 + 2 * T>{}); });
  });
  STAMP(2);
  {
    wimg(ih + tileoff(0), hac[0]); wimg(ih + tileoff(1), hac[1]);
    sfor<4>([&](auto Tc) { constexpr int T = decltype(Tc)::value; wimg(ibase + tileoff(2) + T * 16 * A_IMG, acc[2][T]); });
    if (cg < 3) { sfor<4>([&](auto Tc) { constexpr int T = decltype(Tc)::value; wimg(ibase + tileoff(3) + T * 16 * A_IMG, acc[3][T]); }); }
    else wimg(ih + tileoff(2), hac[2]);
    sfor<3>([&](auto Cc) { constexpr int C = 4 + decltype(Cc)::value;
      sfor<4>([&](auto Tc) { constexpr int T = decltype(Tc)::value; wimg(ibase + tileoff(C) + T * 16 * A_IMG, acc[C][T]); }); });
  }
  GAP();
  att_tail(wo, ws, rt, hg, w, tag
  );
}

__global__ __launch_bounds__(512, 2) void k1_main(const u16* __restrict__ x, const u16* __restrict__ wqkv, const u16* __restrict__ wo,
                                                  const u16* __restrict__ w1, const u16* __restrict__ w2, char* __restrict__ ws, u16* __restrict__ out) {
  const int b = blockIdx.x, xc = b & 7, i = b >> 3;
  const int rt = 4 * (xc >> 1) + (i & 3), j = i >> 2;
  { const int ln = threadIdx.x & 63, wv = threadIdx.x >> 6;
    touch(163584u, (ln < 2) ? (unsigned)((i * 16 + wv * 2 + ln) * 128) : OOB, code_srd(), 0u); }
  if (j < 6) ffn_path(x, w1, w2, ws, out, rt, 6 * (xc & 1) + j);
  else att_path(x, wqkv, wo, ws, rt, 2 * (xc & 1) + (j - 6));
}

extern "C" void kernel_launch(void* const* d_in, const int* in_sizes, int n_in, void* d_out, int out_size, void* d_ws, size_t ws_size, hipStream_t stream) {
  const u16* x = (const u16*)d_in[0]; const u16* wqkv = (const u16*)d_in[1]; const u16* wo = (const u16*)d_in[2];
  const u16* w1 = (const u16*)d_in[4]; const u16* w2 = (const u16*)d_in[5];
  (void)hipFuncSetAttribute((const void*)k1_main, hipFuncAttributeMaxDynamicSharedMemorySize, 163840);
  k1_main<<<256, 512, 163840, stream>>>(x, wqkv, wo, w1, w2, (char*)d_ws, (u16*)d_out);
}
```

```cpp
#include <hip/hip_runtime.h>
#include <utility>

typedef unsigned short u16;
typedef __bf16 bf16x2 __attribute__((ext_vector_type(2)));
typedef __bf16 bf16x4 __attribute__((ext_vector_type(4)));
typedef __bf16 bf16x8 __attribute__((ext_vector_type(8)));
typedef short s16x2 __attribute__((ext_vector_type(2)));
typedef float f32x2 __attribute__((ext_vector_type(2)));
typedef float f32x4 __attribute__((ext_vector_type(4)));
typedef unsigned u32x2 __attribute__((ext_vector_type(2)));
typedef unsigned u32x4 __attribute__((ext_vector_type(4)));
#define LDSP(T) __attribute__((address_space(3))) T*
#define DI __device__ __forceinline__
#define GAP() __builtin_amdgcn_sched_barrier(0)
template <int V> using IC = std::integral_constant<int, V>;
template <class F, int... Is> DI void sfor_(F&& f, std::integer_sequence<int, Is...>) { (f(IC<Is>{}), ...); }
template <int N, class F> DI void sfor(F&& f) { sfor_(f, std::make_integer_sequence<int, N>{}); }

constexpr int SEQ = 2048, DM = 576, DFF = 4608;
constexpr unsigned XN_BYTES = SEQ * DM * 2;
constexpr size_t PART_OFF = 0;
constexpr unsigned PART_BYTES = 16u * XN_BYTES;
constexpr size_t FLAG_OFF = PART_OFF + PART_BYTES;
constexpr size_t EPOCH_OFF = FLAG_OFF + 16 * 9 * 16 * 8;
typedef unsigned long long u64;
typedef __attribute__((address_space(1))) u64 gu64;
#define RLX_AGENT __ATOMIC_RELAXED, __HIP_MEMORY_SCOPE_AGENT
#define TAIL_SLEEP 16
#define ST_AUX 16

DI unsigned cvtpk(float lo, float hi) { f32x2 v = {lo, hi}; return __builtin_bit_cast(unsigned, __builtin_convertvector(v, bf16x2)); }
DI unsigned relupk(unsigned p) { s16x2 z = {0, 0}; return __builtin_bit_cast(unsigned, __builtin_elementwise_max(__builtin_bit_cast(s16x2, p), z)); }
DI float bflo(unsigned p) { return __builtin_bit_cast(float, p << 16); }
DI float bfhi(unsigned p) { return __builtin_bit_cast(float, p & 0xffff0000u); }
DI bf16x4 lds_tr(unsigned addr) { return __builtin_amdgcn_ds_read_tr16_b64_v4bf16((LDSP(bf16x4))(size_t)addr); }
DI bf16x8 lds_rd128(unsigned addr) { return *(LDSP(bf16x8))(size_t)addr; }
typedef bf16x8 bf16x8a8 __attribute__((aligned(8)));
DI bf16x8 lds_rd128a8(unsigned addr) { return *(LDSP(bf16x8a8))(size_t)addr; }
DI u32x2 lds_rd64(unsigned addr) { return *(LDSP(u32x2))(size_t)addr; }
DI void lds_wr128(unsigned addr, u32x4 v) { *(LDSP(u32x4))(size_t)addr = v; }
DI void lds_wr64(unsigned addr, u32x2 v) { *(LDSP(u32x2))(size_t)addr = v; }
DI bf16x8 cat(bf16x4 a, bf16x4 b) { return __builtin_shufflevector(a, b, 0, 1, 2, 3, 4, 5, 6, 7); }
DI f32x4 mfma(bf16x8 a, bf16x8 b, f32x4 c) { return __builtin_amdgcn_mfma_f32_16x16x32_bf16(a, b, c, 0, 0, 0); }
DI void mma(f32x4& c, bf16x8 a, bf16x8 b) { asm volatile("v_mfma_f32_16x16x32_bf16 %0, %1, %2, %0" : "+v"(c) : "v"(a), "v"(b)); }
DI void mma0(f32x4& c, bf16x8 a, bf16x8 b) { asm volatile("v_mfma_f32_16x16x32_bf16 %0, %1, %2, 0" : "=&v"(c) : "v"(a), "v"(b)); }
DI void mma16(f32x4& c, bf16x4 a, u32x2 b) { asm volatile("v_mfma_f32_16x16x16_bf16 %0, %1, %2, %0" : "+v"(c) : "v"(a), "v"(b)); }
DI void mma_not3(f32x4& c, bf16x8 a, bf16x8 b, int cg) {
  asm volatile("s_cmp_eq_u32 %3, 3\n\ts_cbranch_scc1 .Le%=\n\tv_mfma_f32_16x16x32_bf16 %0, %1, %2, %0\n.Le%=:" : "+v"(c) : "v"(a), "v"(b), "s"(cg) : "scc");
}
DI void mma_alt3(f32x4& c, bf16x8 a, bf16x8 b, f32x4& h, bf16x8 ah, bf16x8 bh, int cg) {
  asm volatile("s_cmp_eq_u32 %6, 3\n\ts_cbranch_scc1 .Lh%=\n\tv_mfma_f32_16x16x32_bf16 %0, %2, %3, %0\n\ts_branch .Le%=\n"
               ".Lh%=:\n\tv_mfma_f32_16x16x32_bf16 %1, %4, %5, %1\n.Le%=:" : "+v"(c), "+v"(h) : "v"(a), "v"(b), "v"(ah), "v"(bh), "s"(cg) : "scc");
}
template <int N> DI void nop() { asm volatile("s_nop %0" ::"i"(N)); }
template <int N> DI void waitv() { asm volatile("s_waitcnt vmcnt(%0)" ::"i"(N) : "memory"); }
DI void waitl0() { asm volatile("s_waitcnt lgkmcnt(0)" ::: "memory"); }
DI void bar() { asm volatile("s_barrier" ::: "memory"); }
DI u32x4 make_srd(const void* p, unsigned bytes) {
  size_t a = (size_t)p; u32x4 s;
  s[0] = __builtin_amdgcn_readfirstlane((unsigned)a); s[1] = __builtin_amdgcn_readfirstlane((unsigned)(a >> 32));
  s[2] = bytes; s[3] = 0x00020000u; return s;
}
DI void dma(unsigned lds_dst, unsigned voff, u32x4 srd, unsigned soff) {
  asm volatile("s_mov_b32 m0, %0\n\ts_nop 0\n\tbuffer_load_dwordx4 %1, %2, %3 offen lds" ::"s"(lds_dst), "v"(voff), "s"(srd), "s"(soff) : "memory");
}
DI void dma_sc1(unsigned lds_dst, unsigned voff, u32x4 srd, unsigned soff) {
  asm volatile("s_mov_b32 m0, %0\n\ts_nop 0\n\tbuffer_load_dwordx4 %1, %2, %3 offen sc1 lds" ::"s"(lds_dst), "v"(voff), "s"(srd), "s"(soff) : "memory");
}
template <int CTRL> DI float dppadd(float v) { return v + __builtin_bit_cast(float, __builtin_amdgcn_update_dpp(0, __builtin_bit_cast(int, v), CTRL, 0xF, 0xF, true)); }
DI float red8(float v) { v = dppadd<0xB1>(v); v = dppadd<0x4E>(v); return dppadd<0x141>(v); }
DI float fmx(float a, float b) { return __builtin_elementwise_maximum(a, b); }
DI void swap16(float& a, float& b) {
  auto r = __builtin_amdgcn_permlane16_swap(__builtin_bit_cast(unsigned, a), __builtin_bit_cast(unsigned, b), false, false);
  const unsigned r0 = r[0], r1 = r[1]; a = __builtin_bit_cast(float, r0); b = __builtin_bit_cast(float, r1);
}
DI void swap32(float& a, float& b) {
  auto r = __builtin_amdgcn_permlane32_swap(__builtin_bit_cast(unsigned, a), __builtin_bit_cast(unsigned, b), false, false);
  const unsigned r0 = r[0], r1 = r[1]; a = __builtin_bit_cast(float, r0); b = __builtin_bit_cast(float, r1);
}
template <class T> DI void pin(T& v) { asm volatile("" : "+v"(v)); }
DI float pack_max(float a, float b, float c, float d) { swap16(a, b); a = fmx(a, b); swap16(c, d); c = fmx(c, d); swap32(a, c); return fmx(a, c); }
DI float pack_sum(float a, float b, float c, float d) { swap16(a, b); a = a + b; swap16(c, d); c = c + d; swap32(a, c); return a + c; }
DI void unpack4(float x, float& a, float& b, float& c, float& d) { a = x; c = x; swap32(a, c); b = a; swap16(a, b); d = c; swap16(c, d); }
template <int OFF> DI u32x4 gload(unsigned voff, u32x4 srd, unsigned soff) {
  u32x4 r; asm volatile("buffer_load_dwordx4 %0, %1, %2, %3 offen offset:%4" : "=v"(r) : "v"(voff), "s"(srd), "s"(soff), "i"(OFF)); return r;
}
DI void touch(unsigned lds_dst, unsigned voff, u32x4 srd, unsigned soff) {
  asm volatile("s_mov_b32 m0, %0\n\ts_nop 0\n\tbuffer_load_dword %1, %2, %3 offen lds" ::"s"(lds_dst), "v"(voff), "s"(srd), "s"(soff) : "memory");
}
constexpr unsigned OOB = 0x7fffffffu;
DI u32x4 code_srd() {
  unsigned long long pc; unsigned len;
  asm volatile("s_getpc_b64 %0\n.Lcs%=:\n\ts_mov_b32 %1, .Lkernel_end-.Lcs%=\n\ts_max_i32 %1, %1, 0\n\ts_min_i32 %1, %1, 0x10000" : "=s"(pc), "=s"(len) :: "scc");
  u32x4 r; r[0] = (unsigned)pc; r[1] = (unsigned)(pc >> 32); r[2] = len; r[3] = 0x00020000u; return r;
}
DI u64 poll_load(const gu64* p) { u64 r; asm volatile("global_load_dwordx2 %0, %1, off sc1" : "=v"(r) : "v"(p) : "memory"); return r; }
template <int N> DI void waitv_tie(u64& r) { asm volatile("s_waitcnt vmcnt(%1)" : "+v"(r) : "i"(N)); }
template <int N> DI void waitv_tie(u32x4& r) { asm volatile("s_waitcnt vmcnt(%1)" : "+v"(r) : "i"(N)); }
constexpr unsigned POISON = 0xffffffffu;
DI unsigned nopoison(unsigned v) { return v < POISON - 1 ? v : POISON - 1; }
DI bool clean6(const u32x4 (&v)[6]) {
  unsigned m = 0;
#pragma unroll
  for (int i = 0; i < 6; i++) m = max(max(m, max(v[i][0], v[i][1])), max(v[i][2], v[i][3]));
  return __all(m != POISON) != 0;
}
DI u64 next_tag(u64 e) { return (e ^ 0x9E3779B97F4A7C15ull) * 0xD1342543DE82EF95ull + 0x632BE59BD9B4E019ull; }
DI u64 rd_epoch(char* ws, int rt) {
  return poll_load((const gu64*)(ws + EPOCH_OFF) + rt * 16);
}
DI u64 epoch_uniform(u64 e) {
  asm volatile("" : "+v"(e));
  return ((u64)(unsigned)__builtin_amdgcn_readfirstlane((unsigned)(e >> 32)) << 32) | (unsigned)__builtin_amdgcn_readfirstlane((unsigned)e);
}
DI void set_flag(char* ws, int rt, int n, int src, u64 tag) {
  __hip_atomic_store((gu64*)(ws + FLAG_OFF) + (rt * 9 + n) * 16 + src, tag, RLX_AGENT);
}
typedef short s16x4 __attribute__((ext_vector_type(4)));
struct Stat { f32x4 s1, s2; };
DI void stat_acc(const u32x4& c, Stat& st) {
  const s16x4 ones = {0x3f80, 0x3f80, 0x3f80, 0x3f80};
  const s16x4 a = __builtin_bit_cast(s16x4, (u32x2){c[0], c[1]}), b = __builtin_bit_cast(s16x4, (u32x2){c[2], c[3]});
  st.s1 = __builtin_amdgcn_mfma_f32_4x4x4bf16_1k(a, ones, st.s1, 0, 0, 0);
  st.s2 = __builtin_amdgcn_mfma_f32_4x4x4bf16_1k(a, a, st.s2, 0, 0, 0);
  st.s1 = __builtin_amdgcn_mfma_f32_4x4x4bf16_1k(b, ones, st.s1, 0, 0, 0);
  st.s2 = __builtin_amdgcn_mfma_f32_4x4x4bf16_1k(b, b, st.s2, 0, 0, 0);
}
template <bool WIDE> DI void stat_fin(const Stat& st, int lane, float& r, float& nmr) {
  float s1 = dppadd<0x141>((st.s1[0] + st.s1[1]) + (st.s1[2] + st.s1[3]));
  const float d01 = (lane & 1) ? st.s2[1] : st.s2[0], d23 = (lane & 1) ? st.s2[3] : st.s2[2];
  float s2 = red8((lane & 2) ? d23 : d01);
  if constexpr (WIDE) {
    s1 = dppadd<0x140>(s1); s2 = dppadd<0x140>(s2);
    float t1 = s1, t2 = s2; swap16(s1, t1); swap16(s2, t2); s1 += t1; s2 += t2;
  }
  const float m = s1 * (1.f / DM);
  const float var = fmaxf(s2 * (1.f / DM) - m * m, 0.f);
  r = __builtin_amdgcn_rsqf(var + 1e-5f); nmr = -m * r;
}
struct NormSt { float lo, hi; u32x4 o; };
template <int O> DI void norm_op(NormSt& st, const u32x4& c0, const u32x4& c1, const float (&rs)[2], const float (&nm)[2], unsigned addr) {
  constexpr int R = O / 21, Q = O % 21, J = Q / 5, K = Q % 5;
  const u32x4& c = R ? c1 : c0;
  if constexpr (Q == 20) lds_wr128(addr + R * 128, st.o);
  else if constexpr (K == 0) { st.lo = bflo(c[J]); pin(st.lo); }
  else if constexpr (K == 1) { st.hi = bfhi(c[J]); pin(st.hi); }
  else if constexpr (K == 2) { st.lo = __builtin_fmaf(st.lo, rs[R], nm[R]); pin(st.lo); }
  else if constexpr (K == 3) { st.hi = __builtin_fmaf(st.hi, rs[R], nm[R]); pin(st.hi); }
  else { unsigned t = cvtpk(st.lo, st.hi); pin(t); st.o[J] = t; }
}
DI unsigned norm2(unsigned p, float r, float nmr) { return cvtpk(__builtin_fmaf(bflo(p), r, nmr), __builtin_fmaf(bfhi(p), r, nmr)); }
DI u32x4 norm8(const u32x4& c, float r, float nmr) { return (u32x4){norm2(c[0], r, nmr), norm2(c[1], r, nmr), norm2(c[2], r, nmr), norm2(c[3], r, nmr)}; }

#define STAMP_DECL do {} while (0)
#define STAMP(idx) do {} while (0)
#define STAMP_FLUSH(kid) do {} while (0)

constexpr int p2_first(int j) { return j < 2 ? 0 : (j <= 7 ? 2 * j : j + 8); }
constexpr int p2_count(int j) { return j < 2 ? 0 : (j <= 7 ? 2 : (j + 8 <= 26 ? 1 : 0)); }
constexpr int POLL1 = 16, USE1 = 19, POLL2 = 25, LASTFLAG = 22;
constexpr int inbox_count(int j) { return (j >= USE1 + 1 && j <= USE1 + 3) ? 2 : 0; }
constexpr int f_touch(int t) { return 0; }
constexpr int vm_allowed(int T, int need) {
  int after = 0;
  auto op = [&](int tag, int n) { if (tag == need) after = 0; else after += n; };
  op(0, 3); op(1, 3); op(2, 3); op(298, 2);
  if (T < 0) return after;
  for (int t = 0; t < 18; t++) {
    if (t <= 14) op(t + 3, 3);
    if (f_touch(t)) op(298, 1);
    if (t >= 8 && t < 14) op(297, 1);
    else if (t == 15) { op(100, 2); op(101, 2); }
    else if (t == 16) op(102, 2);
    else op(103, 2);
    if (t == T) return after;
  }
  if (T == 18) return after;
  for (int j = 0; j < 27; j++) {
    for (int c = 0; c < p2_count(j); c++) op(100 + p2_first(j) + c, 2);
    op(300, inbox_count(j));
    if (j >= 3 && j % 3 == 0) op(200 + j / 3 - 1, 2);
    if (j == POLL1) op(301, 1);
    if (j == POLL2) op(302, 1);
    if (19 + j == T) return after;
  }
  return 0;
}
constexpr int imin(int a, int b) { return a < b ? a : b; }
constexpr int p2_wait(int j) {
  int a = j < 26 ? vm_allowed(19 + j, 100 + j + 1) : 63;
  if (j >= 7 && j % 3 == 1 && j <= LASTFLAG) a = imin(a, vm_allowed(19 + j, 200 + j / 3 - 2));
  return a;
}
constexpr unsigned w1_slot(int s) { constexpr int pi[5] = {2, 0, 1, 4, 3}; return 32768u + 24576u * pi[s % 5]; }
constexpr unsigned w2_slot(int j) { constexpr int pi[10] = {8, 9, 7, 6, 0, 1, 2, 3, 4, 5}; return 16384u * pi[j % 10]; }
constexpr unsigned inbox_at(int a, int i) { return w2_slot(17 + 3 * a + i / 2) + (i & 1) * 1024u; }
constexpr unsigned F_OWN = w2_slot(23);
constexpr unsigned F_DUMMY = 159744;

DI void ffn_path(const u16* x, const u16* w1, const u16* w2, char* ws, u16* out, int rt, int sp) {
  const int tid = threadIdx.x, lane = tid & 63;
  const int w = __builtin_amdgcn_readfirstlane(tid >> 6), tg = w >> 2, cg = w & 3;
  const int g = lane >> 4, q = (lane & 15) >> 2, p = lane & 3;
  STAMP_DECL; STAMP(0);
  const u32x4 srdx = make_srd(x, XN_BYTES), srd1 = make_srd(w1, DM * DFF * 2), srd2 = make_srd(w2, DM * DFF * 2);
  const int i8 = lane >> 3, c8 = lane & 7;
  const unsigned vox = i8 * (DM * 2) + c8 * 16;
  const unsigned sox = (unsigned)((rt * 128 + 16 * w) * (DM * 2));
  u64 epoch = rd_epoch(ws, rt);
  u32x4 xr[2][9];
  sfor<9>([&](auto tc) { constexpr int t = decltype(tc)::value;
    xr[0][t] = gload<t * 128>(vox, srdx, sox); xr[1][t] = gload<t * 128>(vox, srdx, sox + 8 * DM * 2); });
  const unsigned swz = 16u * (c8 ^ (2 * ((lane >> 4) & 3)));
  const unsigned vo1 = (8 * (i8 >> 2) + (i8 & 3)) * (DFF * 2) + swz, vo2 = i8 * (DM * 2) + swz;
  unsigned so1[3];
#pragma unroll
  for (int i = 0; i < 3; i++) { const int id = 3 * w + i, rg = id & 3; so1[i] = (unsigned)((16 * (rg & 1) + 4 * (rg >> 1)) * (DFF * 2) + (sp * 384 + (id >> 2) * 64) * 2); }
  const unsigned so2 = (unsigned)((sp * 384 + 16 * w) * (DM * 2));
  const int grp = sp >> 2, rr = sp & 3;
  int chunk[9];
#pragma unroll
  for (int pp = 0; pp < 9; pp++)
    chunk[pp] = pp < 6 ? 3 * ((grp + 1 + pp / 3) % 3) + (pp + rr) % 3 : 3 * grp + ((0x918486u >> (6 * rr + 2 * (pp - 6))) & 3);
  const unsigned m1 = (unsigned)(3 * w * 1024), m2 = (unsigned)(2 * w * 1024);
  auto load1 = [&](auto Sc, auto Ic) {
    constexpr int S = decltype(Sc)::value, I = decltype(Ic)::value;
    dma(w1_slot(S) + m1 + I * 1024, vo1, srd1, so1[I] + (unsigned)(S * 32 * DFF * 2));
  };
  auto load2 = [&](auto Jc, auto Ic) {
    constexpr int J = decltype(Jc)::value, I = decltype(Ic)::value;
    dma(w2_slot(J) + m2 + I * 1024, vo2, srd2, so2 + (unsigned)(((J % 3) * 128 + 8 * I) * (DM * 2)) + (unsigned)chunk[J / 3] * 128);
  };
  sfor<3>([&](auto i) { load1(IC<0>{}, i); });
  const unsigned tv1 = (lane < 16 && c8 < 6 && w < 5) ? (unsigned)((w * 32 + 2 * rt) * (DFF * 2)) + i8 * (DFF * 2) + c8 * 128 : OOB, tv2 = lane < 9 ? lane * 128u : OOB;
  float rs[2], nm[2];
  {
    Stat st[2] = {};
    sfor<18>([&](auto ic) { constexpr int i = decltype(ic)::value;
      waitv_tie<3 + (i > 5 ? 3 : 0) + (i > 11 ? 3 : 0) + 17 - i>(xr[i & 1][i >> 1]); stat_acc(xr[i & 1][i >> 1], st[i & 1]); GAP();
      if constexpr (i == 5) sfor<3>([&](auto k) { load1(IC<1>{}, k); });
      if constexpr (i == 11) { sfor<3>([&](auto k) { load1(IC<2>{}, k); }); } });
    {
      const int jj = (w * 36 >> 3) + (lane >> 3);
      touch(F_DUMMY, (c8 < 6 && lane < 40 && jj < 36) ? (unsigned)((jj * 16 + rt) * (DFF * 2) + c8 * 128) : OOB, srd1, (unsigned)(sp * 768));
      touch(F_DUMMY, lane < 27 ? lane * 128u : OOB, srd2, (unsigned)((sp * 384 + rt * 24 + w * 3) * (DM * 2))); }
    stat_fin<false>(st[0], lane, rs[0], nm[0]); stat_fin<false>(st[1], lane, rs[1], nm[1]);
    epoch = epoch_uniform(epoch);
  }
  const unsigned xw = (c8 >> 2) * 8192 + w * 1024 + (16 * (c8 & 3) + (i8 ^ c8)) * 16;
  NormSt ns;
  auto xop = [&](auto Tc, auto Oc) {
    constexpr int T = decltype(Tc)::value, O = decltype(Oc)::value;
    if constexpr (T < 9 && O < 42) norm_op<O>(ns, xr[0][T < 9 ? T : 0], xr[1][T < 9 ? T : 0], rs, nm, xw + (T & 1) * 16384);
  };
  sfor<2>([&](auto r) { constexpr int R = decltype(r)::value;
    lds_wr128(xw + R * 128, norm8(xr[R][0], rs[R], nm[R])); });
  sfor<12>([&](auto o) { xop(IC<1>{}, o); });

  const int row = 4 * g + q;
  const unsigned wb = row * 128 + 32 * (cg ^ ((row >> 1) & 3)) + 8 * p;
  unsigned wbh = wb + 65536, wbh2 = wb + 131072;
  pin(wbh); pin(wbh2);
  auto waddr = [&](auto Oc) { constexpr unsigned O = decltype(Oc)::value; return O < 65536 ? wb + O : (O < 131072 ? wbh + (O - 65536) : wbh2 + (O - 131072)); };
  const unsigned xb0 = tg * 4096 + (16 * g + ((lane & 15) ^ g)) * 16, xb1 = xb0 ^ 64;

  const __amdgpu_buffer_rsrc_t prs = __builtin_amdgcn_make_buffer_rsrc(ws + PART_OFF, 0, PART_BYTES, 0x00020000);
  const unsigned pso = (unsigned)(((rt * 9) * 16 + sp) * 16384 + w * 2048);
  const u64 tag = next_tag(epoch);
  unsigned poff[3];
#pragma unroll
  for (int i = 0; i < 3; i++) {
    const int sg = 6 * w + 2 * i, pc = 3 * sp + (sg >> 4);
    poff[i] = (unsigned)((rt * 9 + (pc >> 2)) * (16 * 16384) + ((pc >> 1) & 1) * 8192 + ((sg >> 2) & 3) * 2048 + (pc & 1) * 1024 + (sg & 3) * 256);
    asm volatile("" : "+s"(poff[i]));
  }
  f32x4 acc[6][4];
  bf16x8 xf[2][4];
  bf16x4 wlo[4], whi[4];
  auto rdw1 = [&](auto GIc, auto Hc) {
    constexpr int GI = decltype(GIc)::value, H = decltype(Hc)::value;
    constexpr int O = w1_slot(GI / 6) + (GI % 6) * 4096 + H * 2048;
    if constexpr (H == 0) wlo[GI & 3] = lds_tr(waddr(IC<O>{})); else whi[GI & 3] = lds_tr(waddr(IC<O>{}));
  };
  auto rdx = [&](auto Sc, auto Tc) {
    constexpr int S = decltype(Sc)::value, T = decltype(Tc)::value;
    xf[S & 1][T] = lds_rd128(((S & 1) ? xb1 : xb0) + (S & 3) * 8192 + T * 1024);
  };
  waitv<vm_allowed(-1, 0)>(); waitl0(); bar();
  STAMP(1);
  rdw1(IC<0>{}, IC<0>{}); rdw1(IC<0>{}, IC<1>{});
  sfor<4>([&](auto t) { rdx(IC<0>{}, t); });
  rdw1(IC<1>{}, IC<0>{}); rdw1(IC<1>{}, IC<1>{}); rdw1(IC<2>{}, IC<0>{}); rdw1(IC<2>{}, IC<1>{});
  const unsigned hw = lane * 16 + (tg * 16 + cg) * 1024;
  unsigned hwh = hw + 65536; pin(hwh);
  auto hpack = [&](auto Uc, auto Tc) {
    constexpr int U = decltype(Uc)::value, T = decltype(Tc)::value;
    u32x4 o;
    o[0] = relupk(cvtpk(acc[2 * U][T][0], acc[2 * U][T][1])); o[1] = relupk(cvtpk(acc[2 * U][T][2], acc[2 * U][T][3]));
    o[2] = relupk(cvtpk(acc[2 * U + 1][T][0], acc[2 * U + 1][T][1])); o[3] = relupk(cvtpk(acc[2 * U + 1][T][2], acc[2 * U + 1][T][3]));
    lds_wr128((U == 2 ? hwh : hw + U * 32768) + T * 4096, o);
  };
  GAP();
  sfor<108>([&](auto GIc) {
    constexpr int GI = decltype(GIc)::value, S = GI / 6, SB = GI % 6;
    const bf16x8 a = cat(wlo[GI & 3], whi[GI & 3]);
    sfor<4>([&](auto Tc) {
      constexpr int T = decltype(Tc)::value;
      if constexpr (S == 0) mma0(acc[SB][T], a, xf[0][T]); else mma(acc[SB][T], a, xf[S & 1][T]);
      GAP();
      if constexpr (SB == 3 && T == 0) {
        if constexpr (S < 17) { waitv<vm_allowed(S, S + 1)>(); if constexpr ((S & 1) == 1) asm volatile("s_waitcnt lgkmcnt(4)" ::: "memory");
          bar(); }
        else { waitl0(); bar(); }
      }
      {
        constexpr int GP = SB * 4 + T;
        if constexpr ((S & 1) == 1 && SB >= 3) xop(IC<(S + 3) / 2>{}, IC<GP - 12>{});
        else if constexpr ((S & 1) == 0) { xop(IC<S / 2 + 1>{}, IC<GP + 12 + (GP > 8) + (GP > 16)>{}); if constexpr (GP == 8 || GP == 16) xop(IC<S / 2 + 1>{}, IC<GP + 13 + (GP > 8)>{}); }
        else if constexpr (GP < 4) xop(IC<(S + 1) / 2>{}, IC<GP + 38>{});
      }
      if constexpr (SB == 2 && T == 3 && f_touch(S) == 1) touch(F_DUMMY, tv1, srd1, (unsigned)(sp * 768 + (S + 8) * 32 * DFF * 2));
      if constexpr (SB == 2 && T == 3 && f_touch(S) == 2) touch(F_DUMMY, tv2, srd2, (unsigned)((sp * 384 + rt * 24 + w * 3 + (S == 2 ? 0 : (S == 7 ? 1 : 2))) * (DM * 2)));
      if constexpr (SB == 2 && T == 3 && S >= 8 && S < 14)
        __builtin_amdgcn_raw_buffer_store_b128((u32x4){POISON, POISON, POISON, POISON}, prs, lane * 16 + (S & 1) * 1024, pso + (unsigned)(3 * grp + (S - 8) / 2) * (16 * 16384), ST_AUX);
      if constexpr (T < 2 && GI + 3 < 108) rdw1(IC<GI + 3>{}, Tc);
      if constexpr (T == 2 && SB < 3) {
        if constexpr (S <= 14) load1(IC<S + 3>{}, IC<SB % 3>{});
        else if constexpr (S == 15) { load2(IC<(SB >> 1)>{}, IC<(SB & 1)>{}); if constexpr (SB == 2) load2(IC<1>{}, IC<1>{}); }
        else if constexpr (SB < 2) load2(IC<S - 14>{}, IC<SB>{});
      }
      if constexpr (S < 17 && T >= 2 && (SB == 3 || SB == 4)) rdx(IC<S + 1>{}, IC<(SB - 3) * 2 + (T - 2)>{});
      if constexpr (S == 17 && (SB == 3 || SB == 5)) hpack(IC<(SB - 3) / 2>{}, Tc);
      GAP();
    });
  });
  STAMP(2);
  nop<7>(); GAP();
  sfor<4>([&](auto t) { hpack(IC<2>{}, t); });
  waitv<vm_allowed(18, 100)>(); waitl0(); bar();
  if (tid < 3) set_flag(ws, rt, 3 * grp + tid, sp, tag);
  STAMP(3);

  bf16x8 hf[4][12];
  f32x4 oc[4];
  const unsigned hb = lane * 16 + tg * 16384; unsigned hbh = hb + 65536; pin(hbh);
  auto rdh = [&](auto Ic, auto Tc) {
    constexpr int I = decltype(Ic)::value, T = decltype(Tc)::value; constexpr int U = I >> 2;
    hf[T][I] = lds_rd128((U == 2 ? hbh : hb + U * 32768) + T * 4096 + (I & 3) * 1024);
  };
  auto rdw2 = [&](auto GIc, auto Hc) {
    constexpr int GI = decltype(GIc)::value, H = decltype(Hc)::value;
    constexpr int O = w2_slot(GI >> 2) + (GI & 3) * 2048 + H * 8192;
    if constexpr (H == 0) wlo[GI & 3] = lds_tr(waddr(IC<O>{})); else whi[GI & 3] = lds_tr(waddr(IC<O>{}));
  };
  rdw2(IC<0>{}, IC<0>{}); rdw2(IC<0>{}, IC<1>{});
  sfor<4>([&](auto t) { rdh(IC<0>{}, t); });
  rdw2(IC<1>{}, IC<0>{}); rdw2(IC<1>{}, IC<1>{});
  sfor<4>([&](auto t) { rdh(IC<1>{}, t); });
  rdw2(IC<2>{}, IC<0>{}); rdw2(IC<2>{}, IC<1>{});
  unsigned pk[8];
  const u32x4 srdp = make_srd(ws + PART_OFF, PART_BYTES);
  const int c16 = lane & 15;
  const unsigned pvo = g * 16384 + c16 * 16;
  const gu64* fp = (const gu64*)(ws + FLAG_OFF) + (rt * 9 + ((lane & 16) ? (3 * sp + 2) >> 2 : (3 * sp) >> 2)) * 16 + c16;
  const int gsrc[2] = {(grp + 1) % 3, (grp + 2) % 3};
  u64 fl; bool pre0; unsigned pso2;
  auto inbox = [&](auto Ac, auto Ic, unsigned so) {
    constexpr int A = decltype(Ac)::value, I = decltype(Ic)::value;
    if constexpr (I >= 0 && I < 6) dma_sc1(inbox_at(A, I) + m2, pvo, srdp, so + poff[I / 2] + (I & 1) * 256);
  };
  auto own_idx = [&](int pp, int half) { return (unsigned)(4 * chunk[pp] + 2 * tg + half - 3 * sp); };
  const unsigned own_w = F_OWN + cg * 1024 + lane * 16;
  auto ready = [&](u64 f, int gs) { return __all(f == tag || (c16 >> 2) != gs || c16 == sp) != 0; };
  GAP();
  sfor<108>([&](auto GIc) {
    constexpr int GI = decltype(GIc)::value, J = GI >> 2, C = GI & 3, I = GI % 12, N = GI / 12;
    if constexpr (GI == 12) STAMP(4);
    if constexpr (GI == 36) STAMP(5);
    const bf16x8 a = cat(wlo[GI & 3], whi[GI & 3]);
    sfor<4>([&](auto Tc) {
      constexpr int T = decltype(Tc)::value;
      if constexpr (I == 0 && N > 0) { if constexpr (T == 0) { nop<2>(); GAP(); } pk[2 * T] = cvtpk(oc[T][0], oc[T][1]); pk[2 * T + 1] = cvtpk(oc[T][2], oc[T][3]); GAP(); }
      if constexpr (I == 0) mma0(oc[T], a, hf[T][I]); else mma(oc[T], a, hf[T][I]);
      GAP();
      if constexpr (I == 0 && N > 6) { pk[2 * T] = nopoison(pk[2 * T]); pk[2 * T + 1] = nopoison(pk[2 * T + 1]); }
      if constexpr (C == 1 && T == 0 && J < 26) {
        waitv<p2_wait(J)>();
        bar();
        if constexpr (J >= 7 && J % 3 == 1 && J <= LASTFLAG) { if (tid == 0) set_flag(ws, rt, chunk[J >= 6 ? J / 3 - 2 : 0], sp, tag); }
      }
      if constexpr (C == 1 && T == 1 && J == USE1) {
        waitv_tie<vm_allowed(19 + J, 301)>(fl);
        pre0 = ready(fl, gsrc[0]);
        pso2 = pre0 ? (unsigned)gsrc[0] * 65536u : 0x80000000u;
      }
      if constexpr (C == 0 && T == 3 && (J == POLL1 || J == POLL2)) fl = poll_load(fp);
      if constexpr (GI + 3 < 108 && T < 2) rdw2(IC<GI + 3>{}, Tc);
      if constexpr (GI + 2 < 12) rdh(IC<GI + 2>{}, Tc);
      if constexpr (I == 0 && N > 0 && (T & 1)) {
        unsigned so = pso + (unsigned)chunk[N > 0 ? N - 1 : 0] * (16 * 16384);
        if constexpr (N == 8) so = own_idx(7, T >> 1) < 3u ? 0x80000000u : so;
        __builtin_amdgcn_raw_buffer_store_b128((u32x4){pk[2 * T - 2], pk[2 * T - 1], pk[2 * T], pk[2 * T + 1]}, prs, lane * 16 + (T >> 1) * 1024, so, ST_AUX);
      }
      if constexpr (I == 1 && N == 8 && T >= 2) {
        const unsigned oi = own_idx(7, T - 2);
        if (oi < 3u) lds_wr128(own_w + oi * 4096, (u32x4){pk[4 * T - 8], pk[4 * T - 7], pk[4 * T - 6], pk[4 * T - 5]});
      }
      if constexpr (C >= 2 && J < 26) {
        constexpr int cnt = p2_count(J + 1);
        if constexpr (cnt == 1) { if constexpr (T == 2) load2(IC<p2_first(J + 1)>{}, IC<C - 2>{}); }
        if constexpr (cnt == 2) { if constexpr (T >= 2) load2(IC<p2_first(J + 1) + C - 2>{}, IC<T - 2>{}); }
      }
      if constexpr (C >= 2 && J >= USE1 && J < USE1 + 3 && T == 2) inbox(IC<0>{}, IC<2 * (J - USE1) + C - 2>{}, pso2);
      GAP();
    });
  });
  nop<7>(); GAP();
  sfor<2>([&](auto hc) {
    constexpr int H = decltype(hc)::value;
    u32x4 o = {nopoison(cvtpk(oc[2 * H][0], oc[2 * H][1])), nopoison(cvtpk(oc[2 * H][2], oc[2 * H][3])), nopoison(cvtpk(oc[2 * H + 1][0], oc[2 * H + 1][1])), nopoison(cvtpk(oc[2 * H + 1][2], oc[2 * H + 1][3]))};
    const unsigned oi = own_idx(8, H);
    __builtin_amdgcn_raw_buffer_store_b128(o, prs, lane * 16 + H * 1024, oi < 3u ? 0x80000000u : pso + (unsigned)chunk[8] * (16 * 16384), ST_AUX);
    if (oi < 3u) lds_wr128(own_w + oi * 4096, o);
  });
  STAMP(6);
  waitv_tie<2>(fl);
  bool pre1 = ready(fl, gsrc[1]);
  sfor<6>([&](auto i) { inbox(IC<1>{}, i, pre1 ? (unsigned)gsrc[1] * 65536u : 0x80000000u); });
  unsigned ooff[3]; u32x2 xv[3];
#pragma unroll
  for (int i = 0; i < 3; i++) {
    const int sg = 6 * w + 2 * i, pc = 3 * sp + (sg >> 4);
    ooff[i] = (unsigned)((rt * 128 + (pc & 3) * 32 + (g & 1) * 16 + c16) * DM + (pc >> 2) * 64 + (sg & 15) * 4 + (g >> 1) * 4);
    xv[i] = *(const u32x2*)(x + ooff[i]);
  }
  u32x4 sel;
#pragma unroll
  for (int j = 0; j < 4; j++) sel[j] = ((c16 & 7) >> 1) == j ? ((c16 & 1) ? 0x3f800000u : 0x00003f80u) : 0u;
  const u32x4 z4 = {0u, 0u, 0u, 0u};
  const bf16x8 af = __builtin_bit_cast(bf16x8, c16 < 8 ? sel : z4), as = __builtin_bit_cast(bf16x8, c16 < 8 ? z4 : sel);
  f32x4 dd[6];
  unsigned ib = lane * 16 + w * 2048, ibh = ib + 65536, ibh2 = ib + 131072; pin(ibh); pin(ibh2);
  auto rdin = [&](auto Ac, auto Ic) { constexpr unsigned O = inbox_at(decltype(Ac)::value, decltype(Ic)::value);
    return lds_rd128(O < 65536 ? ib + O : (O < 131072 ? ibh + (O - 65536) : ibh2 + (O - 131072))); };
  sfor<6>([&](auto ic) { dd[decltype(ic)::value] = mfma(af, rdin(IC<0>{}, ic), (f32x4){0.f, 0.f, 0.f, 0.f}); });
  waitl0(); bar();
  {
    const bf16x8 af0 = __builtin_bit_cast(bf16x8, lane < 8 ? sel : z4);
    const unsigned orb = F_OWN + c16 * 16 + w * 1536;
    sfor<6>([&](auto ic) { constexpr int i = decltype(ic)::value; dd[i] = mfma(af0, lds_rd128(orb + i * 256), dd[i]); });
  }
  int spin = 0;
  if (!__all(fl == tag || c16 == sp || c16 >= 12)) {
    for (; spin < 200000; spin++) {
      __builtin_amdgcn_s_sleep(1);
      fl = poll_load(fp); waitv_tie<0>(fl);
      if (__all(fl == tag || c16 == sp || c16 >= 12)) break;
    }
  }
  STAMP(7);
  auto ld6 = [&](u32x4 (&v)[6], unsigned vo, int gs) {
#pragma unroll
    for (int i = 0; i < 6; i++) v[i] = __builtin_amdgcn_raw_buffer_load_b128(prs, vo, poff[i >> 1] + (i & 1) * 256 + gs * 65536, 16);
  };
  u32x4 po[6], pa[6];
#pragma unroll
  for (int k = 0; k < 2; k++) {
    if (!(k ? pre1 : pre0)) {
      ld6(po, pvo, gsrc[k]);
#pragma unroll
      for (int i = 0; i < 6; i++) dd[i] = mfma(af, __builtin_bit_cast(bf16x8, po[i]), dd[i]);
    }
  }
  const unsigned pvo_o = g == rr ? OOB : pvo;
  bool oka = false, oko = false;
  for (; spin < 200000; spin++) {
    asm volatile("" ::: "memory");
    const bool ra = !oka && ready(fl, 3);
    if (!oko) ld6(po, pvo_o, grp);
    if (ra) { ld6(pa, pvo, 3); oka = true; }
    if (!oka) fl = __hip_atomic_load(fp, RLX_AGENT);
    oko = clean6(po);
    if (oka && oko) break;
    if (oko) __builtin_amdgcn_s_sleep(1);
  }
  STAMP(8);
  waitv<0>();
  sfor<6>([&](auto ic) { constexpr int i = decltype(ic)::value;
    dd[i] = mfma(af, rdin(IC<1>{}, ic), dd[i]); dd[i] = mfma(af, __builtin_bit_cast(bf16x8, po[i]), dd[i]); dd[i] = mfma(as, __builtin_bit_cast(bf16x8, pa[i]), dd[i]); });
  STAMP(9);
#pragma unroll
  for (int i = 0; i < 3; i++) {
    float o[4];
#pragma unroll
    for (int j = 0; j < 4; j++) {
      float fv = dd[2 * i][j], sv = dd[2 * i + 1][j];
      swap32(fv, sv);
      const float gt = fv * __builtin_amdgcn_rcpf(1.f + __builtin_amdgcn_exp2f(-1.4426950408889634f * fv));
      o[j] = ((j & 1) ? bfhi(xv[i][j >> 1]) : bflo(xv[i][j >> 1])) + sv * gt;
    }
    *(u32x2*)(out + ooff[i]) = (u32x2){cvtpk(o[0], o[1]), cvtpk(o[2], o[3])};
  }
  if (sp == 0 && tid == 0) __hip_atomic_store((gu64*)(ws + EPOCH_OFF) + rt * 16, tag, RLX_AGENT);
  STAMP_FLUSH(1);
  asm volatile(".Lkernel_end:");
}

constexpr unsigned A_SLOT = 27648, A_XR = 110592, A_HALO = 143360, A_DUMMY = 161792, A_WOH = 125952, A_IMG = 872, A_OB = 125568, A_OS = 296;
constexpr unsigned wo_slot(int n) { return n < 6 ? 18432u * n : (n < 8 ? A_WOH + 18432u * (n - 6) : 0u); }
constexpr int a_touch(int s) { return s == -1 ? 2 : 0; }
constexpr int wo_allowed(int N, int need) {
  int after = 0;
  auto op = [&](int tag) { if (tag == need) after = 0; else after += 2; };
  for (int n = 0; n < 6; n++) op(n);
  for (int n = 0; n <= N; n++) {
    if (n >= 2) op(20 + n - 1);
    if (n == N) break;
    if (n == 0) { op(6); op(7); }
    if (n == 1) { op(8); op(20); }
  }
  return after;
}
constexpr int item_pos(int cg, int tg, int C) {
  if (cg == 3) { const int i = (C < 3 ? C : C - 1) + 3 * tg, j = i >= 6 ? i - 6 : i; return j == 2 ? 25 : (j == 5 ? 26 : 20 + j - (j > 2)); }
  if (C < 4) return 8 + 4 * cg + (C ^ (2 * tg));
  return (cg == 2 && C == 6) ? 24 : 3 * cg + C - 4;
}

constexpr u64 pos_table(int C) { u64 t = 0; for (int w = 0; w < 8; w++) t |= (u64)item_pos(w & 3, w >> 2, C) << (5 * w); return t; }
template <int C> DI unsigned pos_of(int w) { return (unsigned)(pos_table(C) >> (5 * w)) & 31u; }

DI void att_tail(const u16* wo, char* ws, int rt, int hg, int w, u64 tag
) {
  int tid = threadIdx.x; pin(tid);
  const int lane = tid & 63, tg = w >> 2, cg = w & 3;
  const int g = lane >> 4, q = (lane & 15) >> 2, p = lane & 3, c16 = lane & 15, i8 = lane >> 3, c8 = lane & 7;
  const u32x4 srdo = make_srd(wo, DM * DM * 2);
  const f32x4 zero4 = {0.f, 0.f, 0.f, 0.f};
  bf16x4 wlo[4], whi[4];
  auto wimg = [&](unsigned a, const f32x4& v) { lds_wr64(a, (u32x2){cvtpk(v[0], v[1]), cvtpk(v[2], v[3])}); };
  const unsigned swz = 16u * (c8 ^ (2 * ((lane >> 4) & 3)));
  const unsigned vo1 = (8 * (i8 >> 2) + (i8 & 3)) * (DM * 2) + swz, vo2 = i8 * (DM * 2) + swz;
  const unsigned soo = (unsigned)((hg * 144 + 32 * (w >> 1) + 4 * (w & 1)) * (DM * 2));
  auto loado = [&](auto Nc, auto Ic) {
    constexpr int N = decltype(Nc)::value, I = decltype(Ic)::value;
    if constexpr (I < 2) dma(wo_slot(N) + (2 * w + I) * 1024, vo1, srdo, soo + I * 16 * DM * 2 + N * 128);
    else if (w < 2) dma(wo_slot(N) + (16 + w) * 1024, vo2, srdo, (unsigned)((hg * 144 + 128 + 8 * w) * (DM * 2)) + N * 128);
  };
  {
    const int hp = w >> 1, par = w & 1;
    const unsigned r0 = (2 * c16 + par + 16) * A_IMG + 72 * hp, rm = (unsigned)max(2 * c16 + par - 16, 0) * A_IMG + 72 * hp + 288;
    unsigned rq[3] = {r0 + 8 * g, r0 + 8 * g + 40, r0 + 32};
    unsigned rk[4] = {rq[0] + 288, rq[1] + 288, rq[2] + 288, rq[2] + 288};
    unsigned rkm[4] = {rm + 8 * g, rm + 8 * g + 40, rm + 32, rm + 32};
    const int kr = 4 * g + q;
    unsigned rv = (2 * kr + par + 16) * A_IMG + 576 + 72 * hp + 8 * p, rvp = rv, rv3 = rv + 96 * A_IMG;
    unsigned rvm = (unsigned)max(2 * kr + par - 16, 0) * A_IMG + 576 + 72 * hp + 8 * p;
    unsigned rq3[3], rk3[4];
#pragma unroll
    for (int i = 0; i < 4; i++) { if (i < 3) { rq3[i] = rq[i] + 96 * A_IMG; pin(rq[i]); pin(rq3[i]); } rk3[i] = rk[i] + 96 * A_IMG; pin(rk[i]); pin(rk3[i]); pin(rkm[i]); }
    pin(rv); pin(rvp); pin(rv3); pin(rvm);
    f32x4 biasA, biasA0, biasB; unsigned mA[2], mA0[2], mB[2];
#pragma unroll
    for (int j = 0; j < 4; j++) {
      const int kk = 4 * g + j; const bool va = kk >= c16 + 9, vb = (kk <= c16) && (kk + 7 >= c16);
      biasA[j] = va ? 0.f : -1e30f; biasB[j] = vb ? 0.f : -1e30f; biasA0[j] = (va && rt > 0) ? 0.f : -1e30f;
      const unsigned bm = (j & 1) ? 0xffff0000u : 0x0000ffffu;
      if ((j & 1) == 0) { mA[j >> 1] = 0; mB[j >> 1] = 0; }
      mA[j >> 1] |= va ? bm : 0u; mB[j >> 1] |= vb ? bm : 0u;
    }
    mA0[0] = rt > 0 ? mA[0] : 0u; mA0[1] = rt > 0 ? mA[1] : 0u;
    pin(biasA); pin(biasA0); pin(biasB); pin(mA[0]); pin(mA[1]); pin(mA0[0]); pin(mA0[1]); pin(mB[0]); pin(mB[1]);
    unsigned qmask = g == 0 ? 0xffffffffu : 0u; pin(qmask);
    unsigned ow0 = A_OB + (2 * c16 + par) * A_OS + 72 * hp, owa = ow0 + 8 * g, owb = owa + 40; pin(ow0); pin(owa); pin(owb);
    waitl0(); bar();
    STAMP(3);
    auto asb = [](u32x2 a, u32x2 b) { return __builtin_bit_cast(bf16x8, (u32x4){a[0], a[1], b[0], b[1]}); };
    bf16x8 kf[5][2], vf[4][3]; u32x2 qm[4][2], q4[4];
    sfor<2>([&](auto Hc) { constexpr int H = decltype(Hc)::value; kf[0][H] = asb(lds_rd64(rkm[H]), lds_rd64(rkm[2 + H])); });
    sfor<4>([&](auto Uc) { constexpr int U = decltype(Uc)::value; constexpr unsigned ro = U < 3 ? U * 32 * A_IMG : 0;
      q4[U] = lds_rd64((U < 3 ? rq[2] : rq3[2]) + ro);
      sfor<2>([&](auto Hc) { constexpr int H = decltype(Hc)::value;
        qm[U][H] = lds_rd64((U < 3 ? rq[H] : rq3[H]) + ro);
        kf[U + 1][H] = asb(lds_rd64((U < 3 ? rk[H] : rk3[H]) + ro), lds_rd64((U < 3 ? rk[2 + H] : rk3[2 + H]) + ro)); });
      GAP(); });
    sfor<4>([&](auto Uc) { constexpr int U = decltype(Uc)::value;
      sfor<3>([&](auto Dc) { constexpr int D = decltype(Dc)::value; constexpr unsigned co = D == 0 ? 0 : (D == 1 ? 40 : 32);
        vf[U][D] = cat(U == 0 ? lds_tr(rvm + co) : lds_tr(rvp + (U > 0 ? U - 1 : 0) * 32 * A_IMG + co), U < 3 ? lds_tr(rv + U * 32 * A_IMG + co) : lds_tr(rv3 + co)); }); });
    GAP();
    f32x4 sa[8], sb[8];
    sfor<8>([&](auto Ic) { constexpr int I = decltype(Ic)::value, U = I >> 1, H = I & 1;
      const bf16x8 qf = asb(qm[U][H], H ? (u32x2){0u, q4[U][1] & qmask} : (u32x2){q4[U][0] & qmask, 0u});
      sa[I] = mfma(kf[U][H], qf, U == 0 ? biasA0 : biasA); sb[I] = mfma(kf[U + 1][H], qf, biasB); });
    GAP();
    waitl0(); bar();
    STAMP(4);
    sfor<3>([&](auto i) { loado(IC<0>{}, i); }); sfor<3>([&](auto i) { loado(IC<1>{}, i); });
    GAP();
    float v[8][4], mx[8];
    sfor<8>([&](auto Ic) { constexpr int I = decltype(Ic)::value;
#pragma unroll
      for (int j = 0; j < 4; j++) v[I][j] = fmx(sa[I][j], sb[I][j]);
      mx[I] = fmx(fmx(fmx(v[I][0], v[I][1]), v[I][2]), v[I][3]); });
    constexpr float SC = 0.23570226039551584f * 1.4426950408889634f;
    sfor<2>([&](auto hc) { constexpr int B = 4 * decltype(hc)::value;
      unpack4(-SC * pack_max(mx[B], mx[B + 1], mx[B + 2], mx[B + 3]), mx[B], mx[B + 1], mx[B + 2], mx[B + 3]); });
    GAP();
    sfor<3>([&](auto i) { loado(IC<2>{}, i); }); sfor<3>([&](auto i) { loado(IC<3>{}, i); });
    GAP();
    float sm[8];
    sfor<8>([&](auto Ic) { constexpr int I = decltype(Ic)::value;
#pragma unroll
      for (int j = 0; j < 4; j++) v[I][j] = __builtin_amdgcn_exp2f(__builtin_fmaf(v[I][j], SC, mx[I]));
      sm[I] = (v[I][0] + v[I][1]) + (v[I][2] + v[I][3]); });
    sfor<2>([&](auto hc) { constexpr int B = 4 * decltype(hc)::value;
      unpack4(__builtin_amdgcn_rcpf(pack_sum(sm[B], sm[B + 1], sm[B + 2], sm[B + 3])), sm[B], sm[B + 1], sm[B + 2], sm[B + 3]); });
    GAP();
    STAMP(5);
    sfor<3>([&](auto i) { loado(IC<4>{}, i); }); sfor<3>([&](auto i) { loado(IC<5>{}, i); });
    GAP();
    bf16x8 pf[8];
    sfor<8>([&](auto Ic) { constexpr int I = decltype(Ic)::value, U = I >> 1;
      const unsigned p0 = cvtpk(v[I][0] * sm[I], v[I][1] * sm[I]), p1 = cvtpk(v[I][2] * sm[I], v[I][3] * sm[I]);
      pf[I] = __builtin_bit_cast(bf16x8, (u32x4){p0 & (U == 0 ? mA0[0] : mA[0]), p1 & (U == 0 ? mA0[1] : mA[1]), p0 & mB[0], p1 & mB[1]}); });
    GAP();
    f32x4 o0[8], o1[8];
    sfor<8>([&](auto Ic) { constexpr int I = decltype(Ic)::value; o0[I] = mfma(vf[I >> 1][I & 1], pf[I], zero4); });
    sfor<8>([&](auto Ic) { constexpr int I = decltype(Ic)::value; o1[I] = mfma(vf[I >> 1][2], pf[I], zero4); });
    GAP();
    sfor<8>([&](auto Ic) { constexpr int I = decltype(Ic)::value; wimg(((I & 1) ? owb : owa) + (I >> 1) * 32 * A_OS, o0[I]); });
    if (g == 0) {
      sfor<4>([&](auto Uc) { constexpr int U = decltype(Uc)::value;
        lds_wr64(ow0 + 32 + U * 32 * A_OS, (u32x2){cvtpk(o1[2 * U][0], o1[2 * U][1]), cvtpk(o1[2 * U + 1][2], o1[2 * U + 1][3])}); });
    }
  }
  const int row = 4 * g + q;
  const unsigned ow = row * 128 + 32 * (cg ^ ((row >> 1) & 3)) + 8 * p; unsigned owh = ow + 65536, owh2 = ow + A_WOH; pin(owh); pin(owh2);
  unsigned ob = A_OB + (tg * 64 + c16) * A_OS + 16 * g, ob4 = A_OB + (tg * 64 + c16) * A_OS + 256 + 8 * g; pin(ob); pin(ob4);
  waitv<10>(); waitl0(); bar();
  STAMP(6);
  auto rdo = [&](auto GIc, auto Hc) {
    constexpr int GI = decltype(GIc)::value, H = decltype(Hc)::value, N = GI / 5, S = GI % 5;
    constexpr unsigned O = wo_slot(N) + S * 4096 + H * 2048;
    const unsigned a = O >= A_WOH ? owh2 + (O - A_WOH) : (O >= 65536 ? owh + (O - 65536) : ow + O);
    if constexpr (H == 0) wlo[GI & 3] = lds_tr(a); else if constexpr (S < 4) whi[GI & 3] = lds_tr(a);
  };
  bf16x8 of[4][4]; u32x2 of4[4];
  sfor<5>([&](auto Sc) { constexpr int S = decltype(Sc)::value;
    if constexpr (S < 3) { rdo(Sc, IC<0>{}); rdo(Sc, IC<1>{}); }
    sfor<4>([&](auto Tc) { constexpr int T = decltype(Tc)::value;
      if constexpr (S < 4) of[T][S & 3] = lds_rd128a8(ob + T * 16 * A_OS + 64 * S); else of4[T] = lds_rd64(ob4 + T * 16 * A_OS); });
    GAP(); });
  const __amdgpu_buffer_rsrc_t prs = __builtin_amdgcn_make_buffer_rsrc(ws + PART_OFF, 0, PART_BYTES, 0x00020000);
  const unsigned pso = (unsigned)(((rt * 9) * 16 + 12 + hg) * 16384 + w * 2048);
  f32x4 oc[4]; unsigned pk[8];
  GAP();
  sfor<45>([&](auto GIc) {
    constexpr int GI = decltype(GIc)::value, N = GI / 5, S = GI % 5;
    if constexpr (GI == 5) STAMP(7);
    if constexpr (GI == 15) STAMP(8);
    if constexpr (GI == 30) STAMP(9);
    const bf16x4 alo = wlo[GI & 3];
    bf16x8 a; if constexpr (S < 4) a = cat(alo, whi[GI & 3]);
    sfor<4>([&](auto Tc) {
      constexpr int T = decltype(Tc)::value;
      if constexpr (S == 0 && N > 0) { if constexpr (T == 0) { nop<2>(); GAP(); } pk[2 * T] = cvtpk(oc[T][0], oc[T][1]); pk[2 * T + 1] = cvtpk(oc[T][2], oc[T][3]); GAP(); }
      if constexpr (S == 0) mma0(oc[T], a, of[T][0]); else if constexpr (S < 4) mma(oc[T], a, of[T][S & 3]); else mma16(oc[T], alo, of4[T]);
      GAP();
      if constexpr (S == 2 && T == 0 && (N == 0 || N == 1 || N == 4)) {
        if constexpr (N == 0) waitl0();
        waitv<wo_allowed(N, N == 0 ? 1 : (N == 1 ? 5 : 8))>(); bar();
      }
      if constexpr (GI + 3 < 45 && T < 2) rdo(IC<GI + 3>{}, Tc);
      if constexpr (N == 0 && S >= 3 && T > 0) loado(IC<3 + S>{}, IC<T - 1>{});
      if constexpr (N == 1 && S == 2 && T > 0) loado(IC<8>{}, IC<T - 1>{});
      if constexpr (S == (N == 1 ? 3 : 0) && N > 0 && (T & 1))
        __builtin_amdgcn_raw_buffer_store_b128((u32x4){pk[2 * T - 2], pk[2 * T - 1], pk[2 * T], pk[2 * T + 1]}, prs, lane * 16 + (T >> 1) * 1024, pso + (N - 1) * 16 * 16384, ST_AUX);
      GAP();
    });
  });
  nop<7>(); GAP();
  sfor<2>([&](auto hc) {
    constexpr int H = decltype(hc)::value;
    u32x4 o = {nopoison(cvtpk(oc[2 * H][0], oc[2 * H][1])), nopoison(cvtpk(oc[2 * H][2], oc[2 * H][3])), nopoison(cvtpk(oc[2 * H + 1][0], oc[2 * H + 1][1])), nopoison(cvtpk(oc[2 * H + 1][2], oc[2 * H + 1][3]))};
    __builtin_amdgcn_raw_buffer_store_b128(o, prs, lane * 16 + H * 1024, pso + 8 * 16 * 16384, ST_AUX);
  });
  STAMP(10);
  waitv<0>(); bar();
  if (tid < 9) set_flag(ws, rt, tid, 12 + hg, tag);
  STAMP(11); STAMP_FLUSH(1);
}

DI void att_path(const u16* x, const u16* wqkv, const u16* wo, char* ws, int rt, int hg) {
  const int tid = threadIdx.x, lane = tid & 63;
  const int w = __builtin_amdgcn_readfirstlane(tid >> 6), tg = w >> 2, cg = w & 3;
  const int g = lane >> 4, q = (lane & 15) >> 2, p = lane & 3, c16 = lane & 15;
  STAMP_DECL; STAMP(0);
  const u32x4 srdx = make_srd(x, XN_BYTES), srdw = make_srd(wqkv, DM * 3 * DM * 2), srdo = make_srd(wo, DM * DM * 2);
  const int i8 = lane >> 3, c8 = lane & 7, l32 = lane & 31;
  const unsigned vox = i8 * (DM * 2) + c8 * 16;
  const unsigned sox = (unsigned)((rt * 128 + 16 * w) * (DM * 2));
  u64 epoch = rd_epoch(ws, rt);
  u32x4 xr[2][9], xh[3];
  {
    const unsigned soh = (unsigned)((rt > 0 ? rt * 128 - 16 + 2 * w : 0) * (DM * 2)), voh = (lane >> 5) * (DM * 2) + l32 * 16;
    xh[0] = gload<0>(voh, srdx, soh); xh[1] = gload<512>(voh, srdx, soh); xh[2] = gload<1024>(l32 < 8 ? voh : OOB, srdx, soh);
  }
  sfor<9>([&](auto tc) { constexpr int t = decltype(tc)::value;
    xr[0][t] = gload<t * 128>(vox, srdx, sox); xr[1][t] = gload<t * 128>(vox, srdx, sox + 8 * DM * 2); });
  unsigned vw[4];
#pragma unroll
  for (int i = 0; i < 4; i++) {
    const unsigned n = (w + 8 * i) * 64 + lane, r = (n * 9710u) >> 19, c = n - 54 * r;
    const unsigned k = 8 * ((r >> 2) & 3) + 4 * (r >> 4) + (r & 3);
    const unsigned sec = (c * 57u) >> 10;
    vw[i] = k * 3456 + c * 16 + sec * (1152 - 288);
  }
  auto loadw = [&](auto Sc, auto Ic) {
    constexpr int S = decltype(Sc)::value, I = decltype(Ic)::value;
    if constexpr (S < 18) { if (I < 3 || w < 3) dma(
(S & 3) * A_SLOT + (w + 8 * I) * 1024, vw[I], srdw, (unsigned)(S * 32 * 3456 + hg * 288)); }
  };
  sfor<4>([&](auto i) { loadw(IC<0>{}, i); });
  const unsigned tvw = (lane < 12 && p < 3) ? (unsigned)(((w >> 1) * 32 + 2 * rt + (w & 1)) * 3456 + 2 * hg * 128) + q * 1152 + p * 128 : OOB;
  unsigned wbs[7];
  auto img_off = [&](unsigned pos) { const unsigned left = 0u - (unsigned)(pos >= 24), odd = hg & 1;
    return ((288u * (pos >> 3) + 32u * ((pos & 7) + odd)) & ~left) | ((288u * (pos - 24) + 256u - 256u * odd) & left); };
  {
    const unsigned lr = (4 * g + q) * 864 + 8 * p;
    sfor<7>([&](auto Cc) { constexpr int C = decltype(Cc)::value;
      const unsigned pos = pos_of<C>(w);
      wbs[C] = lr + img_off(pos);
      pin(wbs[C]); });
  }
  const unsigned xs = (16 * g + (c16 ^ g)) * 16;
  unsigned xb0 = A_XR + tg * 4096 + xs, xb1 = xb0 ^ 64, hb0 = A_HALO + xs, hb1 = hb0 ^ 64; pin(xb0); pin(xb1); pin(hb0); pin(hb1);
  unsigned xw = A_XR + (c8 >> 2) * 8192 + (16 * (c8 & 3) + (i8 ^ c8)) * 16 + w * 1024; pin(xw);
  GAP();
  STAMP(12);
  {
    Stat st = {}; float r, nmr;
    sfor<3>([&](auto ic) { constexpr int i = decltype(ic)::value; waitv_tie<3 + 18 + 2 - i>(xh[i]); stat_acc(xh[i], st); });
    stat_fin<true>(st, lane, r, nmr);
    const unsigned hwa = A_HALO + (l32 >> 2) * 1024 + (16 * (l32 & 3) + ((2 * w + (lane >> 5)) ^ c8)) * 16;
    lds_wr128(hwa, norm8(xh[0], r, nmr)); lds_wr128(hwa + 8192, norm8(xh[1], r, nmr));
    if (l32 < 8) lds_wr128(hwa + 16384, norm8(xh[2], r, nmr));
  }
  GAP();
  float rs[2], nm[2];
  {
    Stat st[2] = {};
    sfor<18>([&](auto ic) { constexpr int i = decltype(ic)::value;
      waitv_tie<3 + (i > 7 ? 3 : 0) + 17 - i>(xr[i & 1][i >> 1]); stat_acc(xr[i & 1][i >> 1], st[i & 1]); GAP();
      if constexpr (i == 7) { sfor<4>([&](auto k) { loadw(IC<1>{}, k); }); } });
    { const int jj = (w * 36 >> 3) + (lane / 12), l12 = lane % 12;
      touch(A_DUMMY, ((l12 & 3) < 3 && lane < 60 && jj < 36) ? (unsigned)((jj * 16 + rt) * 3456 + (l12 >> 2) * 1152 + (2 * hg + (l12 & 3)) * 128) : OOB, srdw, 0u);
      touch(A_DUMMY, lane < 11 && w * 11 + lane < 81 ? lane * 128u : OOB, srdo, (unsigned)((hg * 144 + rt * 9) * (DM * 2) + w * 11 * 128)); }
    stat_fin<false>(st[0], lane, rs[0], nm[0]); stat_fin<false>(st[1], lane, rs[1], nm[1]);
    epoch = epoch_uniform(epoch);
  }
  STAMP(13);
  NormSt ns;
  auto xop = [&](auto Tc, auto Oc) {
    constexpr int T = decltype(Tc)::value, O = decltype(Oc)::value;
    if constexpr (T < 9 && O < 42) norm_op<O>(ns, xr[0][T < 9 ? T : 0], xr[1][T < 9 ? T : 0], rs, nm, xw + (T & 1) * 16384);
  };
  sfor<2>([&](auto r) { constexpr int R = decltype(r)::value; lds_wr128(xw + R * 128, norm8(xr[R][0], rs[R], nm[R])); });
  lds_wr128(xw + 16384, norm8(xr[0][1], rs[0], nm[0]));

  const f32x4 zero4 = {0.f, 0.f, 0.f, 0.f};
  const u64 tag = next_tag(epoch);
  f32x4 acc[7][4], hac[3];
  sfor<4>([&](auto t) { acc[3][decltype(t)::value] = zero4; }); hac[2] = zero4;
  bf16x8 xf[4], xhf;
  bf16x4 wlo[4], whi[4];
  auto rdw = [&](auto GIc, auto Hc) {
    constexpr int GI = decltype(GIc)::value, H = decltype(Hc)::value, S = GI / 7, C = GI % 7;
    if constexpr (GI < 126) {
      const unsigned a = wbs[C] + (S & 1) * A_SLOT + H * 16 * 864;
      if constexpr (H == 0) wlo[GI & 3] = lds_tr(a); else whi[GI & 3] = lds_tr(a);
      if constexpr (H == 1 && (S & 1) == 1) { wbs[C] += (S & 2) ? -2 * A_SLOT : 2 * A_SLOT; pin(wbs[C]); }
    }
  };
  auto rdx = [&](auto Sc, auto Tc) {
    constexpr int S = decltype(Sc)::value, T = decltype(Tc)::value;
    xf[T] = lds_rd128(((S & 1) ? xb1 : xb0) + (S & 3) * 8192 + T * 1024);
  };
  auto rdxh = [&](auto Sc) { constexpr int S = decltype(Sc)::value; xhf = lds_rd128(((S & 1) ? hb1 : hb0) + S * 1024); };
  unsigned ih, ibase;
  unsigned toff[7];
  auto tileoff = [&](int C) { return toff[C]; };
  auto wimg = [&](unsigned a, const f32x4& v) { lds_wr64(a, (u32x2){cvtpk(v[0], v[1]), cvtpk(v[2], v[3])}); };
  STAMP(14);
  waitv<5>(); waitl0(); bar();
  STAMP(1);
  rdw(IC<0>{}, IC<0>{}); rdw(IC<0>{}, IC<1>{}); rdw(IC<1>{}, IC<0>{}); rdw(IC<1>{}, IC<1>{});
  sfor<4>([&](auto t) { rdx(IC<0>{}, t); });
  rdxh(IC<0>{});
  nop<1>();
  GAP();
  sfor<18>([&](auto Sc) {
    constexpr int S = decltype(Sc)::value, G0 = 7 * S;
    auto fill = [&](auto GPc) {
      constexpr int GP = decltype(GPc)::value;
      GAP();
      if constexpr (GP == 21) {
        if constexpr (S < 16) waitv<3 + a_touch(S) + a_touch(S - 1)>(); else if
 constexpr (S == 16) waitv<0>(); else waitl0();
        bar();
      }
      if constexpr (S == 17 && GP == 12) { int tz = tid; pin(tz); ih = (tz & 15) * A_IMG + 8 * ((tz >> 4) & 3); ibase = ih + (16 + tg * 64) * A_IMG;
        sfor<7>([&](auto Cc) { constexpr int C = decltype(Cc)::value; toff[C] = img_off(pos_of<C>(w)); }); }
      if constexpr (S == 0) xop(IC<1>{}, IC<GP + 21>{});
      else if constexpr ((S & 1) == 0) xop(IC<S / 2 + 1>{}, IC<GP + 8>{});
      else if constexpr (GP >= 22) xop(IC<(S + 3) / 2>{}, IC<GP - 22>{});
      else if constexpr (GP < 4 && S > 1) xop(IC<(S + 1) / 2>{}, IC<38 + GP>{});
      if constexpr (GP < 4) rdw(IC<G0 + 2 + (GP >> 1)>{}, IC<(GP & 1)>{});
      if constexpr (GP >= 8 && GP < 12) rdw(IC<G0 + 4 + ((GP - 8) >> 1)>{}, IC<(GP & 1)>{});
      if constexpr (GP == 15 || GP == 16) rdw(IC<G0 + 6>{}, IC<GP - 15>{});
      if constexpr (GP == 21 || GP == 22) rdw(IC<G0 + 7>{}, IC<GP - 21>{});
      if constexpr (GP == 24 || GP == 26) rdw(IC<G0 + 8>{}, IC<(GP - 24) / 2>{});
      if constexpr (S < 17) {
        if constexpr (GP >= 23 && (GP & 1) == 1) rdx(IC<S + 1>{}, IC<(GP - 23) / 2>{});
        if constexpr (GP == 28) rdxh(IC<S + 1>{});
      } else if constexpr (GP >= 21) {
        if constexpr (GP < 29) wimg(ibase + tileoff((GP - 21) >> 2) + ((GP - 21) & 3) * 16 * A_IMG, acc[(GP - 21) >> 2][(GP - 21) & 3]);
      }
      if constexpr (GP == 4 || GP == 6 || GP == 12 || GP == 17) loadw(IC<S + 2>{}, IC<(GP == 4 ? 0 : (GP == 6 ? 1 : (GP == 12 ? 2 : 3)))>{});
      if constexpr (false) touch(A_DUMMY, tvw, srdw, (unsigned)((S + 5) * 32 * 3456));
      if constexpr (false) touch(A_DUMMY, lane < 11 && w * 11 + lane < 81 ? lane * 128u : OOB, srdo, (unsigned)((hg * 144 + rt * 9) * (DM * 2) + w * 11 * 128));
      GAP();
    };
    auto mm = [&](f32x4& c, const bf16x8& a, const bf16x8& b) { if constexpr (S == 0) mma0(c, a, b); else mma(c, a, b); };
    const bf16x8 a0 = cat(wlo[G0 & 3], whi[G0 & 3]), a1 = cat(wlo[(G0 + 1) & 3], whi[(G0 + 1) & 3]);
    sfor<4>([&](auto Tc) { constexpr int T = decltype(Tc)::value;
      mm(acc[0][T], a0, xf[T]); fill(IC<2 * T>{}); mm(acc[1][T], a1, xf[T]); fill(IC<2 * T + 1>{}); });
    mm(hac[0], a0, xhf); fill(IC<8>{});
    mm(hac[1], a1, xhf); fill(IC<9>{});
    const bf16x8 a2 = cat(wlo[(G0 + 2) & 3], whi[(G0 + 2) & 3]);
    sfor<4>([&](auto Tc) { constexpr int T = decltype(Tc)::value; mm(acc[2][T], a2, xf[T]); fill(IC<10 + T>{}); });
    const bf16x8 a3 = cat(wlo[(G0 + 3) & 3], whi[(G0 + 3) & 3]);
    mma_alt3(acc[3][0], a3, xf[0], hac[2], a2, xhf, cg); fill(IC<14>{});
    sfor<3>([&](auto Tc) { constexpr int T = decltype(Tc)::value + 1; mma_not3(acc[3][T], a3, xf[T], cg); fill(IC<14 + T>{}); });
    const bf16x8 a4 = cat(wlo[(G0 + 4) & 3], whi[(G0 + 4) & 3]);
    sfor<4>([&](auto Tc) { constexpr int T = decltype(Tc)::value; mm(acc[4][T], a4, xf[T]); fill(IC<18 + T>{}); });
    const bf16x8 a5 = cat(wlo[(G0 + 5) & 3], whi[(G0 + 5) & 3]), a6 = cat(wlo[(G0 + 6) & 3], whi[(G0 + 6) & 3]);
    sfor<4>([&](auto Tc) { constexpr int T = decltype(Tc)::value;
      mm(acc[5][T], a5, xf[T]); fill(IC<22 + 2 * T>{}); mm(acc[6][T], a6, xf[T]); fill(IC<23 + 2 * T>{}); });
  });
  STAMP(2);
  {
    wimg(ih + tileoff(0), hac[0]); wimg(ih + tileoff(1), hac[1]);
    sfor<4>([&](auto Tc) { constexpr int T = decltype(Tc)::value; wimg(ibase + tileoff(2) + T * 16 * A_IMG, acc[2][T]); });
    if (cg < 3) { sfor<4>([&](auto Tc) { constexpr int T = decltype(Tc)::value; wimg(ibase + tileoff(3) + T * 16 * A_IMG, acc[3][T]); }); }
    else wimg(ih + tileoff(2), hac[2]);
    sfor<3>([&](auto Cc) { constexpr int C = 4 + decltype(Cc)::value;
      sfor<4>([&](auto Tc) { constexpr int T = decltype(Tc)::value; wimg(ibase + tileoff(C) + T * 16 * A_IMG, acc[C][T]); }); });
  }
  GAP();
  att_tail(wo, ws, rt, hg, w, tag
  );
}

__global__ __launch_bounds__(512, 2) void k1_main(const u16* __restrict__ x, const u16* __restrict__ wqkv, const u16* __restrict__ wo,
                                                  const u16* __restrict__ w1, const u16* __restrict__ w2, char* __restrict__ ws, u16* __restrict__ out) {
  const int b = blockIdx.x, xc = b & 7, i = b >> 3;
  const int rt = 4 * (xc >> 1) + (i & 3), j = i >> 2;
  { const int ln = threadIdx.x & 63, wv = threadIdx.x >> 6;
    touch(163584u, (ln < 2) ? (unsigned)((i * 16 + wv * 2 + ln) * 128) : OOB, code_srd(), 0u);
    if (wv == 0) touch(163584u, ln < 36 ? (unsigned)(rt * 9 * 16 * 16384 + ln * 65536) : OOB, make_srd(ws, PART_BYTES), 0u);
    if (wv == 1) touch(163584u, ln < 36 ? (unsigned)(rt * 128 * DM * 2 + ln * 4096) : OOB, make_srd(out, XN_BYTES), 0u); }
  if (j < 6) ffn_path(x, w1, w2, ws, out, rt, 6 * (xc & 1) + j);
  else att_path(x, wqkv, wo, ws, rt, 2 * (xc & 1) + (j - 6));
}

extern "C" void kernel_launch(void* const* d_in, const int* in_sizes, int n_in, void* d_out, int out_size, void* d_ws, size_t ws_size, hipStream_t stream) {
  const u16* x = (const u16*)d_in[0]; const u16* wqkv = (const u16*)d_in[1]; const u16* wo = (const u16*)d_in[2];
  const u16* w1 = (const u16*)d_in[4]; const u16* w2 = (const u16*)d_in[5];
  (void)hipFuncSetAttribute((const void*)k1_main, hipFuncAttributeMaxDynamicSharedMemorySize, 163840);
  k1_main<<<256, 512, 163840, stream>>>(x, wqkv, wo, w1, w2, (char*)d_ws, (u16*)d_out);
}
```

```cpp
#include <hip/hip_runtime.h>
#include <utility>

typedef unsigned short u16;
typedef __bf16 bf16x2 __attribute__((ext_vector_type(2)));
typedef __bf16 bf16x4 __attribute__((ext_vector_type(4)));
typedef __bf16 bf16x8 __attribute__((ext_vector_type(8)));
typedef short s16x2 __attribute__((ext_vector_type(2)));
typedef float f32x2 __attribute__((ext_vector_type(2)));
typedef float f32x4 __attribute__((ext_vector_type(4)));
typedef unsigned u32x2 __attribute__((ext_vector_type(2)));
typedef unsigned u32x4 __attribute__((ext_vector_type(4)));
#define LDSP(T) __attribute__((address_space(3))) T*
#define DI __device__ __forceinline__
#define GAP() __builtin_amdgcn_sched_barrier(0)
template <int V> using IC = std::integral_constant<int, V>;
template <class F, int... Is> DI void sfor_(F&& f, std::integer_sequence<int, Is...>) { (f(IC<Is>{}), ...); }
template <int N, class F> DI void sfor(F&& f) { sfor_(f, std::make_integer_sequence<int, N>{}); }

constexpr int SEQ = 2048, DM = 576, DFF = 4608;
constexpr unsigned XN_BYTES = SEQ * DM * 2;
constexpr size_t PART_OFF = 0;
constexpr unsigned PART_BYTES = 16u * XN_BYTES;
constexpr size_t FLAG_OFF = PART_OFF + PART_BYTES;
constexpr size_t ACK_OFF = FLAG_OFF + 16 * 9 * 16 * 8;
constexpr size_t EPOCH_OFF = ACK_OFF + 16 * 16 * 8;
typedef unsigned long long u64;
typedef __attribute__((address_space(1))) u64 gu64;
#define RLX_AGENT __ATOMIC_RELAXED, __HIP_MEMORY_SCOPE_AGENT
#define TAIL_SLEEP 4
#define ST_AUX 16

DI unsigned cvtpk(float lo, float hi) { f32x2 v = {lo, hi}; return __builtin_bit_cast(unsigned, __builtin_convertvector(v, bf16x2)); }
DI unsigned relupk(unsigned p) { s16x2 z = {0, 0}; return __builtin_bit_cast(unsigned, __builtin_elementwise_max(__builtin_bit_cast(s16x2, p), z)); }
DI float bflo(unsigned p) { return __builtin_bit_cast(float, p << 16); }
DI float bfhi(unsigned p) { return __builtin_bit_cast(float, p & 0xffff0000u); }
DI bf16x4 lds_tr(unsigned addr) { return __builtin_amdgcn_ds_read_tr16_b64_v4bf16((LDSP(bf16x4))(size_t)addr); }
DI bf16x8 lds_rd128(unsigned addr) { return *(LDSP(bf16x8))(size_t)addr; }
typedef bf16x8 bf16x8a8 __attribute__((aligned(8)));
DI bf16x8 lds_rd128a8(unsigned addr) { return *(LDSP(bf16x8a8))(size_t)addr; }
DI u32x2 lds_rd64(unsigned addr) { return *(LDSP(u32x2))(size_t)addr; }
DI void lds_wr128(unsigned addr, u32x4 v) { *(LDSP(u32x4))(size_t)addr = v; }
DI void lds_wr64(unsigned addr, u32x2 v) { *(LDSP(u32x2))(size_t)addr = v; }
DI bf16x8 cat(bf16x4 a, bf16x4 b) { return __builtin_shufflevector(a, b, 0, 1, 2, 3, 4, 5, 6, 7); }
DI f32x4 mfma(bf16x8 a, bf16x8 b, f32x4 c) { return __builtin_amdgcn_mfma_f32_16x16x32_bf16(a, b, c, 0, 0, 0); }
DI void mma(f32x4& c, bf16x8 a, bf16x8 b) { asm volatile("v_mfma_f32_16x16x32_bf16 %0, %1, %2, %0" : "+v"(c) : "v"(a), "v"(b)); }
DI void mma0(f32x4& c, bf16x8 a, bf16x8 b) { asm volatile("v_mfma_f32_16x16x32_bf16 %0, %1, %2, 0" : "=&v"(c) : "v"(a), "v"(b)); }
DI void mma16(f32x4& c, bf16x4 a, u32x2 b) { asm volatile("v_mfma_f32_16x16x16_bf16 %0, %1, %2, %0" : "+v"(c) : "v"(a), "v"(b)); }
DI void mma_not3(f32x4& c, bf16x8 a, bf16x8 b, int cg) {
  asm volatile("s_cmp_eq_u32 %3, 3\n\ts_cbranch_scc1 .Le%=\n\tv_mfma_f32_16x16x32_bf16 %0, %1, %2, %0\n.Le%=:" : "+v"(c) : "v"(a), "v"(b), "s"(cg) : "scc");
}
DI void mma_alt3(f32x4& c, bf16x8 a, bf16x8 b, f32x4& h, bf16x8 ah, bf16x8 bh, int cg) {
  asm volatile("s_cmp_eq_u32 %6, 3\n\ts_cbranch_scc1 .Lh%=\n\tv_mfma_f32_16x16x32_bf16 %0, %2, %3, %0\n\ts_branch .Le%=\n"
               ".Lh%=:\n\tv_mfma_f32_16x16x32_bf16 %1, %4, %5, %1\n.Le%=:" : "+v"(c), "+v"(h) : "v"(a), "v"(b), "v"(ah), "v"(bh), "s"(cg) : "scc");
}
template <int N> DI void nop() { asm volatile("s_nop %0" ::"i"(N)); }
template <int N> DI void waitv() { asm volatile("s_waitcnt vmcnt(%0)" ::"i"(N) : "memory"); }
DI void waitl0() { asm volatile("s_waitcnt lgkmcnt(0)" ::: "memory"); }
DI void bar() { asm volatile("s_barrier" ::: "memory"); }
DI u32x4 make_srd(const void* p, unsigned bytes) {
  size_t a = (size_t)p; u32x4 s;
  s[0] = __builtin_amdgcn_readfirstlane((unsigned)a); s[1] = __builtin_amdgcn_readfirstlane((unsigned)(a >> 32));
  s[2] = bytes; s[3] = 0x00020000u; return s;
}
DI void dma(unsigned lds_dst, unsigned voff, u32x4 srd, unsigned soff) {
  asm volatile("s_mov_b32 m0, %0\n\ts_nop 0\n\tbuffer_load_dwordx4 %1, %2, %3 offen lds" ::"s"(lds_dst), "v"(voff), "s"(srd), "s"(soff) : "memory");
}
DI void dma_sc1(unsigned lds_dst, unsigned voff, u32x4 srd, unsigned soff) {
  asm volatile("s_mov_b32 m0, %0\n\ts_nop 0\n\tbuffer_load_dwordx4 %1, %2, %3 offen sc1 lds" ::"s"(lds_dst), "v"(voff), "s"(srd), "s"(soff) : "memory");
}
template <int CTRL> DI float dppadd(float v) { return v + __builtin_bit_cast(float, __builtin_amdgcn_update_dpp(0, __builtin_bit_cast(int, v), CTRL, 0xF, 0xF, true)); }
DI float red8(float v) { v = dppadd<0xB1>(v); v = dppadd<0x4E>(v); return dppadd<0x141>(v); }
DI float fmx(float a, float b) { return __builtin_elementwise_maximum(a, b); }
DI void swap16(float& a, float& b) {
  auto r = __builtin_amdgcn_permlane16_swap(__builtin_bit_cast(unsigned, a), __builtin_bit_cast(unsigned, b), false, false);
  const unsigned r0 = r[0], r1 = r[1]; a = __builtin_bit_cast(float, r0); b = __builtin_bit_cast(float, r1);
}
DI void swap32(float& a, float& b) {
  auto r = __builtin_amdgcn_permlane32_swap(__builtin_bit_cast(unsigned, a), __builtin_bit_cast(unsigned, b), false, false);
  const unsigned r0 = r[0], r1 = r[1]; a = __builtin_bit_cast(float, r0); b = __builtin_bit_cast(float, r1);
}
template <class T> DI void pin(T& v) { asm volatile("" : "+v"(v)); }
DI float pack_max(float a, float b, float c, float d) { swap16(a, b); a = fmx(a, b); swap16(c, d); c = fmx(c, d); swap32(a, c); return fmx(a, c); }
DI float pack_sum(float a, float b, float c, float d) { swap16(a, b); a = a + b; swap16(c, d); c = c + d; swap32(a, c); return a + c; }
DI void unpack4(float x, float& a, float& b, float& c, float& d) { a = x; c = x; swap32(a, c); b = a; swap16(a, b); d = c; swap16(c, d); }
template <int OFF> DI u32x4 gload(unsigned voff, u32x4 srd, unsigned soff) {
  u32x4 r; asm volatile("buffer_load_dwordx4 %0, %1, %2, %3 offen offset:%4" : "=v"(r) : "v"(voff), "s"(srd), "s"(soff), "i"(OFF)); return r;
}
DI void touch(unsigned lds_dst, unsigned voff, u32x4 srd, unsigned soff) {
  asm volatile("s_mov_b32 m0, %0\n\ts_nop 0\n\tbuffer_load_dword %1, %2, %3 offen lds" ::"s"(lds_dst), "v"(voff), "s"(srd), "s"(soff) : "memory");
}
constexpr unsigned OOB = 0x7fffffffu;
DI u32x4 code_srd() {
  unsigned long long pc; unsigned len;
  asm volatile("s_getpc_b64 %0\n.Lcs%=:\n\ts_mov_b32 %1, .Lkernel_end-.Lcs%=\n\ts_max_i32 %1, %1, 0\n\ts_min_i32 %1, %1, 0x10000" : "=s"(pc), "=s"(len) :: "scc");
  u32x4 r; r[0] = (unsigned)pc; r[1] = (unsigned)(pc >> 32); r[2] = len; r[3] = 0x00020000u; return r;
}
DI u64 poll_load(const gu64* p) { u64 r; asm volatile("global_load_dwordx2 %0, %1, off sc1" : "=v"(r) : "v"(p) : "memory"); return r; }
template <int N> DI void waitv_tie(u64& r) { asm volatile("s_waitcnt vmcnt(%1)" : "+v"(r) : "i"(N)); }
template <int N> DI void waitv_tie(u32x4& r) { asm volatile("s_waitcnt vmcnt(%1)" : "+v"(r) : "i"(N)); }
constexpr unsigned POISON = 0xffffffffu;
DI unsigned nopoison(unsigned v) { return v < POISON - 1 ? v : POISON - 1; }
DI bool clean6(const u32x4 (&v)[6]) {
  unsigned m = 0;
#pragma unroll
  for (int i = 0; i < 6; i++) m = max(max(m, max(v[i][0], v[i][1])), max(v[i][2], v[i][3]));
  return __all(m != POISON) != 0;
}
DI u64 next_tag(u64 e) { return (e ^ 0x9E3779B97F4A7C15ull) * 0xD1342543DE82EF95ull + 0x632BE59BD9B4E019ull; }
DI u64 rd_epoch(char* ws, int rt) {
  return poll_load((const gu64*)(ws + EPOCH_OFF) + rt * 16);
}
DI u64 epoch_uniform(u64 e) {
  asm volatile("" : "+v"(e));
  return ((u64)(unsigned)__builtin_amdgcn_readfirstlane((unsigned)(e >> 32)) << 32) | (unsigned)__builtin_amdgcn_readfirstlane((unsigned)e);
}
DI void set_flag(char* ws, int rt, int n, int src, u64 tag) {
  __hip_atomic_store((gu64*)(ws + FLAG_OFF) + (rt * 9 + n) * 16 + src, tag, RLX_AGENT);
}
typedef short s16x4 __attribute__((ext_vector_type(4)));
struct Stat { f32x4 s1, s2; };
DI void stat_acc(const u32x4& c, Stat& st) {
  const s16x4 ones = {0x3f80, 0x3f80, 0x3f80, 0x3f80};
  const s16x4 a = __builtin_bit_cast(s16x4, (u32x2){c[0], c[1]}), b = __builtin_bit_cast(s16x4, (u32x2){c[2], c[3]});
  st.s1 = __builtin_amdgcn_mfma_f32_4x4x4bf16_1k(a, ones, st.s1, 0, 0, 0);
  st.s2 = __builtin_amdgcn_mfma_f32_4x4x4bf16_1k(a, a, st.s2, 0, 0, 0);
  st.s1 = __builtin_amdgcn_mfma_f32_4x4x4bf16_1k(b, ones, st.s1, 0, 0, 0);
  st.s2 = __builtin_amdgcn_mfma_f32_4x4x4bf16_1k(b, b, st.s2, 0, 0, 0);
}
template <bool WIDE> DI void stat_fin(const Stat& st, int lane, float& r, float& nmr) {
  float s1 = dppadd<0x141>((st.s1[0] + st.s1[1]) + (st.s1[2] + st.s1[3]));
  const float d01 = (lane & 1) ? st.s2[1] : st.s2[0], d23 = (lane & 1) ? st.s2[3] : st.s2[2];
  float s2 = red8((lane & 2) ? d23 : d01);
  if constexpr (WIDE) {
    s1 = dppadd<0x140>(s1); s2 = dppadd<0x140>(s2);
    float t1 = s1, t2 = s2; swap16(s1, t1); swap16(s2, t2); s1 += t1; s2 += t2;
  }
  const float m = s1 * (1.f / DM);
  const float var = fmaxf(s2 * (1.f / DM) - m * m, 0.f);
  r = __builtin_amdgcn_rsqf(var + 1e-5f); nmr = -m * r;
}
struct NormSt { float lo, hi; u32x4 o; };
template <int O> DI void norm_op(NormSt& st, const u32x4& c0, const u32x4& c1, const float (&rs)[2], const float (&nm)[2], unsigned addr) {
  constexpr int R = O / 21, Q = O % 21, J = Q / 5, K = Q % 5;
  const u32x4& c = R ? c1 : c0;
  if constexpr (Q == 20) lds_wr128(addr + R * 128, st.o);
  else if constexpr (K == 0) { st.lo = bflo(c[J]); pin(st.lo); }
  else if constexpr (K == 1) { st.hi = bfhi(c[J]); pin(st.hi); }
  else if constexpr (K == 2) { st.lo = __builtin_fmaf(st.lo, rs[R], nm[R]); pin(st.lo); }
  else if constexpr (K == 3) { st.hi = __builtin_fmaf(st.hi, rs[R], nm[R]); pin(st.hi); }
  else { unsigned t = cvtpk(st.lo, st.hi); pin(t); st.o[J] = t; }
}
DI unsigned norm2(unsigned p, float r, float nmr) { return cvtpk(__builtin_fmaf(bflo(p), r, nmr), __builtin_fmaf(bfhi(p), r, nmr)); }
DI u32x4 norm8(const u32x4& c, float r, float nmr) { return (u32x4){norm2(c[0], r, nmr), norm2(c[1], r, nmr), norm2(c[2], r, nmr), norm2(c[3], r, nmr)}; }

#define STAMP_DECL do {} while (0)
#define STAMP(idx) do {} while (0)
#define STAMP_FLUSH(kid) do {} while (0)

constexpr int p2_first(int j) { return j < 2 ? 0 : (j <= 7 ? 2 * j : j + 8); }
constexpr int p2_count(int j) { return j < 2 ? 0 : (j <= 7 ? 2 : (j + 8 <= 26 ? 1 : 0)); }
constexpr int POLL1 = 16, USE1 = 19, POLL2 = 25, LASTFLAG = 21;
constexpr int inbox_count(int j) { return (j >= USE1 + 1 && j <= USE1 + 3) ? 2 : 0; }
constexpr int vm_allowed(int T, int need) {
  int after = 0;
  auto op = [&](int tag, int n) { if (tag == need) after = 0; else after += n; };
  op(0, 3); op(1, 3); op(2, 3); op(298, 2);
  if (T < 0) return after;
  for (int t = 0; t < 18; t++) {
    if (t <= 14) op(t + 3, 3);
    if (t >= 3 && t < 15) op(297, 1);
    else if (t == 15) { op(100, 2); op(101, 2); }
    else if (t == 16) op(102, 2);
    else op(103, 2);
    if (t == T) return after;
  }
  if (T == 18) return after;
  for (int j = 0; j < 27; j++) {
    for (int c = 0; c < p2_count(j); c++) op(100 + p2_first(j) + c, 2);
    op(300, inbox_count(j));
    if (j == POLL1) op(301, 1);
    if (j == POLL2) op(302, 1);
    if (j >= 3 && j % 3 == 0) op(200 + j / 3 - 1, 2);
    if (19 + j == T) return after;
  }
  return 0;
}
constexpr int imin(int a, int b) { return a < b ? a : b; }
constexpr int p2_wait(int j) {
  int a = j < 26 ? vm_allowed(19 + j, 100 + j + 1) : 63;
  if (j >= 6 && j % 3 == 0 && j <= LASTFLAG) a = imin(a, vm_allowed(19 + j, 200 + j / 3 - 2));
  return a;
}
constexpr unsigned w1_slot(int s) { constexpr int pi[5] = {2, 0, 1, 4, 3}; return 32768u + 24576u * pi[s % 5]; }
constexpr unsigned w2_slot(int j) { constexpr int pi[10] = {8, 9, 7, 6, 0, 1, 2, 3, 4, 5}; return 16384u * pi[j % 10]; }
constexpr unsigned inbox_at(int a, int i) { return w2_slot(17 + 3 * a + i / 2) + (i & 1) * 1024u; }
constexpr unsigned F_OWN = w2_slot(23);
constexpr unsigned F_DUMMY = 159744;

DI void ffn_path(const u16* x, const u16* w1, const u16* w2, char* ws, u16* out, int rt, int sp) {
  const int tid = threadIdx.x, lane = tid & 63;
  const int w = __builtin_amdgcn_readfirstlane(tid >> 6), tg = w >> 2, cg = w & 3;
  const int g = lane >> 4, q = (lane & 15) >> 2, p = lane & 3;
  STAMP_DECL; STAMP(0);
  const u32x4 srdx = make_srd(x, XN_BYTES), srd1 = make_srd(w1, DM * DFF * 2), srd2 = make_srd(w2, DM * DFF * 2);
  const int i8 = lane >> 3, c8 = lane & 7;
  const unsigned vox = i8 * (DM * 2) + c8 * 16;
  const unsigned sox = (unsigned)((rt * 128 + 16 * w) * (DM * 2));
  u64 epoch = rd_epoch(ws, rt);
  u32x4 xr[2][9];
  sfor<9>([&](auto tc) { constexpr int t = decltype(tc)::value;
    xr[0][t] = gload<t * 128>(vox, srdx, sox); xr[1][t] = gload<t * 128>(vox, srdx, sox + 8 * DM * 2); });
  const unsigned swz = 16u * (c8 ^ (2 * ((lane >> 4) & 3)));
  const unsigned vo1 = (8 * (i8 >> 2) + (i8 & 3)) * (DFF * 2) + swz, vo2 = i8 * (DM * 2) + swz;
  unsigned so1[3];
#pragma unroll
  for (int i = 0; i < 3; i++) { const int id = 3 * w + i, rg = id & 3; so1[i] = (unsigned)((16 * (rg & 1) + 4 * (rg >> 1)) * (DFF * 2) + (sp * 384 + (id >> 2) * 64) * 2); }
  const unsigned so2 = (unsigned)((sp * 384 + 16 * w) * (DM * 2));
  const int grp = sp >> 2, rr = sp & 3;
  int chunk[9];
#pragma unroll
  for (int pp = 0; pp < 9; pp++)
    chunk[pp] = pp < 6 ? 3 * ((grp + 1 + pp / 3) % 3) + (pp + rr) % 3 : 3 * grp + ((0x918486u >> (6 * rr + 2 * (pp - 6))) & 3);
  const unsigned m1 = (unsigned)(3 * w * 1024), m2 = (unsigned)(2 * w * 1024);
  auto load1 = [&](auto Sc, auto Ic) {
    constexpr int S = decltype(Sc)::value, I = decltype(Ic)::value;
    dma(w1_slot(S) + m1 + I * 1024, vo1, srd1, so1[I] + (unsigned)(S * 32 * DFF * 2));
  };
  auto load2 = [&](auto Jc, auto Ic) {
    constexpr int J = decltype(Jc)::value, I = decltype(Ic)::value;
    dma(w2_slot(J) + m2 + I * 1024, vo2, srd2, so2 + (unsigned)(((J % 3) * 128 + 8 * I) * (DM * 2)) + (unsigned)chunk[J / 3] * 128);
  };
  sfor<3>([&](auto i) { load1(IC<0>{}, i); });
  float rs[2], nm[2];
  {
    Stat st[2] = {};
    sfor<18>([&](auto ic) { constexpr int i = decltype(ic)::value;
      waitv_tie<3 + (i > 5 ? 3 : 0) + (i > 11 ? 3 : 0) + 17 - i>(xr[i & 1][i >> 1]); stat_acc(xr[i & 1][i >> 1], st[i & 1]); GAP();
      if constexpr (i == 5) sfor<3>([&](auto k) { load1(IC<1>{}, k); });
      if constexpr (i == 11) { sfor<3>([&](auto k) { load1(IC<2>{}, k); }); } });
    {
      const int jj = (w * 36 >> 3) + i8;
      touch(F_DUMMY, (c8 < 6 && lane < 40 && jj < 36) ? (unsigned)((jj * 16 + rt) * (DFF * 2) + c8 * 128) : OOB, srd1, (unsigned)(sp * 768));
      touch(F_DUMMY, lane < 27 ? lane * 128u : OOB, srd2, (unsigned)((sp * 384 + rt * 24 + w * 3) * (DM * 2)));
    }
    stat_fin<false>(st[0], lane, rs[0], nm[0]); stat_fin<false>(st[1], lane, rs[1], nm[1]);
    epoch = epoch_uniform(epoch);
  }
  const unsigned xw = (c8 >> 2) * 8192 + w * 1024 + (16 * (c8 & 3) + (i8 ^ c8)) * 16;
  NormSt ns;
  auto xop = [&](auto Tc, auto Oc) {
    constexpr int T = decltype(Tc)::value, O = decltype(Oc)::value;
    if constexpr (T < 9 && O < 42) norm_op<O>(ns, xr[0][T < 9 ? T : 0], xr[1][T < 9 ? T : 0], rs, nm, xw + (T & 1) * 16384);
  };
  sfor<2>([&](auto r) { constexpr int R = decltype(r)::value;
    lds_wr128(xw + R * 128, norm8(xr[R][0], rs[R], nm[R])); });
  sfor<12>([&](auto o) { xop(IC<1>{}, o); });

  const int row = 4 * g + q;
  const unsigned wb = row * 128 + 32 * (cg ^ ((row >> 1) & 3)) + 8 * p;
  unsigned wbh = wb + 65536, wbh2 = wb + 131072;
  pin(wbh); pin(wbh2);
  auto waddr = [&](auto Oc) { constexpr unsigned O = decltype(Oc)::value; return O < 65536 ? wb + O : (O < 131072 ? wbh + (O - 65536) : wbh2 + (O - 131072)); };
  const unsigned xb0 = tg * 4096 + (16 * g + ((lane & 15) ^ g)) * 16, xb1 = xb0 ^ 64;

  const __amdgpu_buffer_rsrc_t prs = __builtin_amdgcn_make_buffer_rsrc(ws + PART_OFF, 0, PART_BYTES, 0x00020000);
  const unsigned pso = (unsigned)(((rt * 9) * 16 + sp) * 16384 + w * 2048);
  const u64 tag = next_tag(epoch);
  unsigned poff[3];
#pragma unroll
  for (int i = 0; i < 3; i++) {
    const int sg = 6 * w + 2 * i, pc = 3 * sp + (sg >> 4);
    poff[i] = (unsigned)((rt * 9 + (pc >> 2)) * (16 * 16384) + ((pc >> 1) & 1) * 8192 + ((sg >> 2) & 3) * 2048 + (pc & 1) * 1024 + (sg & 3) * 256);
    asm volatile("" : "+s"(poff[i]));
  }
  const unsigned pvo = g * 16384 + (lane & 15) * 16;
  f32x4 acc[6][4];
  bf16x8 xf[2][4];
  bf16x4 wlo[4], whi[4];
  auto rdw1 = [&](auto GIc, auto Hc) {
    constexpr int GI = decltype(GIc)::value, H = decltype(Hc)::value;
    constexpr int O = w1_slot(GI / 6) + (GI % 6) * 4096 + H * 2048;
    if constexpr (H == 0) wlo[GI & 3] = lds_tr(waddr(IC<O>{})); else whi[GI & 3] = lds_tr(waddr(IC<O>{}));
  };
  auto rdx = [&](auto Sc, auto Tc) {
    constexpr int S = decltype(Sc)::value, T = decltype(Tc)::value;
    xf[S & 1][T] = lds_rd128(((S & 1) ? xb1 : xb0) + (S & 3) * 8192 + T * 1024);
  };
  waitv<vm_allowed(-1, 0)>(); waitl0(); bar();
  STAMP(1);
  rdw1(IC<0>{}, IC<0>{}); rdw1(IC<0>{}, IC<1>{});
  sfor<4>([&](auto t) { rdx(IC<0>{}, t); });
  rdw1(IC<1>{}, IC<0>{}); rdw1(IC<1>{}, IC<1>{}); rdw1(IC<2>{}, IC<0>{}); rdw1(IC<2>{}, IC<1>{});
  const unsigned hw = lane * 16 + (tg * 16 + cg) * 1024;
  unsigned hwh = hw + 65536; pin(hwh);
  auto hpack = [&](auto Uc, auto Tc) {
    constexpr int U = decltype(Uc)::value, T = decltype(Tc)::value;
    u32x4 o;
    o[0] = relupk(cvtpk(acc[2 * U][T][0], acc[2 * U][T][1])); o[1] = relupk(cvtpk(acc[2 * U][T][2], acc[2 * U][T][3]));
    o[2] = relupk(cvtpk(acc[2 * U + 1][T][0], acc[2 * U + 1][T][1])); o[3] = relupk(cvtpk(acc[2 * U + 1][T][2], acc[2 * U + 1][T][3]));
    lds_wr128((U == 2 ? hwh : hw + U * 32768) + T * 4096, o);
  };
  GAP();
  sfor<108>([&](auto GIc) {
    constexpr int GI = decltype(GIc)::value, S = GI / 6, SB = GI % 6;
    const bf16x8 a = cat(wlo[GI & 3], whi[GI & 3]);
    sfor<4>([&](auto Tc) {
      constexpr int T = decltype(Tc)::value;
      if constexpr (S == 0) mma0(acc[SB][T], a, xf[0][T]); else mma(acc[SB][T], a, xf[S & 1][T]);
      GAP();
      if constexpr (SB == 3 && T == 0) {
        if constexpr (S < 17) { waitv<vm_allowed(S, S + 1)>(); if constexpr ((S & 1) == 1) asm volatile("s_waitcnt lgkmcnt(4)" ::: "memory");
          bar(); }
        else { waitl0(); bar(); }
      }
      {
        constexpr int GP = SB * 4 + T;
        if constexpr ((S & 1) == 1 && SB >= 3) xop(IC<(S + 3) / 2>{}, IC<GP - 12>{});
        else if constexpr ((S & 1) == 0) { xop(IC<S / 2 + 1>{}, IC<GP + 12 + (GP > 8) + (GP > 16)>{}); if constexpr (GP == 8 || GP == 16) xop(IC<S / 2 + 1>{}, IC<GP + 13 + (GP > 8)>{}); }
        else if constexpr (GP < 4) xop(IC<(S + 1) / 2>{}, IC<GP + 38>{});
      }
      if constexpr (SB == 2 && T == 3 && S >= 3 && S < 15) {
        constexpr int PI = (S >= 3 && S < 15) ? S - 3 : 0;
        __builtin_amdgcn_raw_buffer_store_b128((u32x4){POISON, POISON, POISON, POISON}, prs, pvo, poff[(PI % 6) >> 1] + (PI & 1) * 256 + (PI < 6 ? grp : 3) * 65536, ST_AUX);
      }
      if constexpr (T < 2 && GI + 3 < 108) rdw1(IC<GI + 3>{}, Tc);
      if constexpr (T == 2 && SB < 3) {
        if constexpr (S <= 14) load1(IC<S + 3>{}, IC<SB % 3>{});
        else if constexpr (S == 15) { load2(IC<(SB >> 1)>{}, IC<(SB & 1)>{}); if constexpr (SB == 2) load2(IC<1>{}, IC<1>{}); }
        else if constexpr (SB < 2) load2(IC<S - 14>{}, IC<SB>{});
      }
      if constexpr (S < 17 && T >= 2 && (SB == 3 || SB == 4)) rdx(IC<S + 1>{}, IC<(SB - 3) * 2 + (T - 2)>{});
      if constexpr (S == 17 && (SB == 3 || SB == 5)) hpack(IC<(SB - 3) / 2>{}, Tc);
      GAP();
    });
  });
  STAMP(2);
  nop<7>(); GAP();
  sfor<4>([&](auto t) { hpack(IC<2>{}, t); });
  waitv<vm_allowed(18, 100)>(); waitl0(); bar();
  if (tid == 0) __hip_atomic_store((gu64*)(ws + ACK_OFF) + rt * 16 + sp, tag, RLX_AGENT);
  STAMP(3);

  bf16x8 hf[4][12];
  f32x4 oc[4];
  const unsigned hb = lane * 16 + tg * 16384; unsigned hbh = hb + 65536; pin(hbh);
  auto rdh = [&](auto Ic, auto Tc) {
    constexpr int I = decltype(Ic)::value, T = decltype(Tc)::value; constexpr int U = I >> 2;
    hf[T][I] = lds_rd128((U == 2 ? hbh : hb + U * 32768) + T * 4096 + (I & 3) * 1024);
  };
  auto rdw2 = [&](auto GIc, auto Hc) {
    constexpr int GI = decltype(GIc)::value, H = decltype(Hc)::value;
    constexpr int O = w2_slot(GI >> 2) + (GI & 3) * 2048 + H * 8192;
    if constexpr (H == 0) wlo[GI & 3] = lds_tr(waddr(IC<O>{})); else whi[GI & 3] = lds_tr(waddr(IC<O>{}));
  };
  rdw2(IC<0>{}, IC<0>{}); rdw2(IC<0>{}, IC<1>{});
  sfor<4>([&](auto t) { rdh(IC<0>{}, t); });
  rdw2(IC<1>{}, IC<0>{}); rdw2(IC<1>{}, IC<1>{});
  sfor<4>([&](auto t) { rdh(IC<1>{}, t); });
  rdw2(IC<2>{}, IC<0>{}); rdw2(IC<2>{}, IC<1>{});
  unsigned pk[8];
  const u32x4 srdp = make_srd(ws + PART_OFF, PART_BYTES);
  const int c16 = lane & 15;
  const gu64* fp = lane < 32 ? (const gu64*)(ws + FLAG_OFF) + (rt * 9 + ((lane & 16) ? (3 * sp + 2) >> 2 : (3 * sp) >> 2)) * 16 + c16 : (const gu64*)(ws + ACK_OFF) + rt * 16 + c16;
  const int gsrc[2] = {(grp + 2) % 3, (grp + 1) % 3};
  u64 fl; bool pre0, pre1; unsigned pso2;
  auto inbox = [&](auto Ac, auto Ic, unsigned so) {
    constexpr int A = decltype(Ac)::value, I = decltype(Ic)::value;
    if constexpr (I >= 0 && I < 6) dma_sc1(inbox_at(A, I) + m2, pvo, srdp, so + poff[I / 2] + (I & 1) * 256);
  };
  auto own_idx = [&](int pp, int half) { return (unsigned)(4 * chunk[pp] + 2 * tg + half - 3 * sp); };
  const unsigned own_w = F_OWN + cg * 1024 + lane * 16;
  auto ready = [&](u64 f, int gs) { return __all(f == tag || (c16 >> 2) != gs || lane >= 32) != 0; };
  auto acked = [&](u64 f) { return __all(f == tag || (c16 >> 2) != grp || lane < 32) != 0; };
  int spin = 0;
  GAP();
  sfor<108>([&](auto GIc) {
    constexpr int GI = decltype(GIc)::value, J = GI >> 2, C = GI & 3, I = GI % 12, N = GI / 12;
    if constexpr (GI == 12) STAMP(4);
    if constexpr (GI == 36) STAMP(5);
    const bf16x8 a = cat(wlo[GI & 3], whi[GI & 3]);
    sfor<4>([&](auto Tc) {
      constexpr int T = decltype(Tc)::value;
      if constexpr (I == 0 && N > 0) { if constexpr (T == 0) { nop<2>(); GAP(); } pk[2 * T] = cvtpk(oc[T][0], oc[T][1]); pk[2 * T + 1] = cvtpk(oc[T][2], oc[T][3]); GAP(); }
      if constexpr (I == 0) mma0(oc[T], a, hf[T][I]); else mma(oc[T], a, hf[T][I]);
      GAP();
      if constexpr (I == 0 && N > 6) { pk[2 * T] = nopoison(pk[2 * T]); pk[2 * T + 1] = nopoison(pk[2 * T + 1]); }
      if constexpr (C == 1 && T == 0 && J < 26) {
        waitv<p2_wait(J)>();
        bar();
        if constexpr (J >= 6 && J % 3 == 0 && J <= LASTFLAG) { if (tid == 0) set_flag(ws, rt, chunk[J >= 6 ? J / 3 - 2 : 0], sp, tag); }
      }
      if constexpr (C == 1 && T == 1 && J == USE1) {
        waitv_tie<vm_allowed(19 + J, 301)>(fl);
        pre0 = ready(fl, gsrc[0]);
        if (!acked(fl)) {
          for (; spin < 200000; spin++) { __builtin_amdgcn_s_sleep(1); u64 f2 = poll_load(fp); waitv_tie<0>(f2); if (acked(f2)) break; }
        }
        pso2 = pre0 ? (unsigned)gsrc[0] * 65536u : 0x80000000u;
      }
      if constexpr (C == 0 && T == 0 && (J == POLL1 || J == POLL2)) fl = poll_load(fp);
      if constexpr (GI + 3 < 108 && T < 2) rdw2(IC<GI + 3>{}, Tc);
      if constexpr (GI + 2 < 12) rdh(IC<GI + 2>{}, Tc);
      if constexpr (I == 0 && N > 0 && (T & 1)) {
        unsigned so = pso + (unsigned)chunk[N > 0 ? N - 1 : 0] * (16 * 16384);
        if constexpr (N == 8) so = own_idx(7, T >> 1) < 3u ? 0x80000000u : so;
        __builtin_amdgcn_raw_buffer_store_b128((u32x4){pk[2 * T - 2], pk[2 * T - 1], pk[2 * T], pk[2 * T + 1]}, prs, lane * 16 + (T >> 1) * 1024, so, ST_AUX);
      }
      if constexpr (I == 1 && N == 8 && T >= 2) {
        const unsigned oi = own_idx(7, T - 2);
        if (oi < 3u) lds_wr128(own_w + oi * 4096, (u32x4){pk[4 * T - 8], pk[4 * T - 7], pk[4 * T - 6], pk[4 * T - 5]});
      }
      if constexpr (C >= 2 && J < 26) {
        constexpr int cnt = p2_count(J + 1);
        if constexpr (cnt == 1) { if constexpr (T == 2) load2(IC<p2_first(J + 1)>{}, IC<C - 2>{}); }
        if constexpr (cnt == 2) { if constexpr (T >= 2) load2(IC<p2_first(J + 1) + C - 2>{}, IC<T - 2>{}); }
      }
      if constexpr (C >= 2 && J >= USE1 && J < USE1 + 3 && T == 2) inbox(IC<0>{}, IC<2 * (J - USE1) + C - 2>{}, pso2);
      GAP();
    });
  });
  nop<7>(); GAP();
  sfor<2>([&](auto hc) {
    constexpr int H = decltype(hc)::value;
    u32x4 o = {nopoison(cvtpk(oc[2 * H][0], oc[2 * H][1])), nopoison(cvtpk(oc[2 * H][2], oc[2 * H][3])), nopoison(cvtpk(oc[2 * H + 1][0], oc[2 * H + 1][1])), nopoison(cvtpk(oc[2 * H + 1][2], oc[2 * H + 1][3]))};
    const unsigned oi = own_idx(8, H);
    __builtin_amdgcn_raw_buffer_store_b128(o, prs, lane * 16 + H * 1024, oi < 3u ? 0x80000000u : pso + (unsigned)chunk[8] * (16 * 16384), ST_AUX);
    if (oi < 3u) lds_wr128(own_w + oi * 4096, o);
  });
  STAMP(6);
  waitv_tie<2>(fl);
  pre1 = ready(fl, gsrc[1]);
  STAMP(11);
  if (!pre0) STAMP(12);
  if (!pre1) STAMP(13);
  sfor<6>([&](auto i) { inbox(IC<1>{}, i, pre1 ? (unsigned)gsrc[1] * 65536u : 0x80000000u); });
  unsigned ooff[3]; u32x2 xv[3];
#pragma unroll
  for (int i = 0; i < 3; i++) {
    const int sg = 6 * w + 2 * i, pc = 3 * sp + (sg >> 4);
    ooff[i] = (unsigned)((rt * 128 + (pc & 3) * 32 + (g & 1) * 16 + c16) * DM + (pc >> 2) * 64 + (sg & 15) * 4 + (g >> 1) * 4);
    xv[i] = *(const u32x2*)(x + ooff[i]);
  }
  u32x4 sel;
#pragma unroll
  for (int j = 0; j < 4; j++) sel[j] = ((c16 & 7) >> 1) == j ? ((c16 & 1) ? 0x3f800000u : 0x00003f80u) : 0u;
  const u32x4 z4 = {0u, 0u, 0u, 0u};
  const bf16x8 af = __builtin_bit_cast(bf16x8, c16 < 8 ? sel : z4), as = __builtin_bit_cast(bf16x8, c16 < 8 ? z4 : sel);
  f32x4 dd[6];
  unsigned ib = lane * 16 + w * 2048, ibh = ib + 65536, ibh2 = ib + 131072; pin(ibh); pin(ibh2);
  auto rdin = [&](auto Ac, auto Ic) { constexpr unsigned O = inbox_at(decltype(Ac)::value, decltype(Ic)::value);
    return lds_rd128(O < 65536 ? ib + O : (O < 131072 ? ibh + (O - 65536) : ibh2 + (O - 131072))); };
  sfor<6>([&](auto ic) { dd[decltype(ic)::value] = mfma(af, rdin(IC<0>{}, ic), (f32x4){0.f, 0.f, 0.f, 0.f}); });
  STAMP(14);
  waitl0(); bar();
  STAMP(15);
  {
    const bf16x8 af0 = __builtin_bit_cast(bf16x8, lane < 8 ? sel : z4);
    const unsigned orb = F_OWN + c16 * 16 + w * 1536;
    sfor<6>([&](auto ic) { constexpr int i = decltype(ic)::value; dd[i] = mfma(af0, lds_rd128(orb + i * 256), dd[i]); });
  }
  if (!(pre0 && pre1)) {
    for (; spin < 200000; spin++) {
      fl = poll_load(fp); waitv_tie<0>(fl);
      if (ready(fl, gsrc[0]) && ready(fl, gsrc[1])) break;
      __builtin_amdgcn_s_sleep(1);
    }
  }
  STAMP(7);
  auto ld6 = [&](u32x4 (&v)[6], unsigned vo, int gs) {
#pragma unroll
    for (int i = 0; i < 6; i++) v[i] = __builtin_amdgcn_raw_buffer_load_b128(prs, vo, poff[i >> 1] + (i & 1) * 256 + gs * 65536, 16);
  };
  u32x4 po[6], pa[6];
#pragma unroll
  for (int k = 0; k < 2; k++) {
    if (!(k ? pre1 : pre0)) {
      ld6(po, pvo, gsrc[k]);
#pragma unroll
      for (int i = 0; i < 6; i++) dd[i] = mfma(af, __builtin_bit_cast(bf16x8, po[i]), dd[i]);
    }
  }
  const unsigned pvo_o = g == rr ? OOB : pvo;
  bool oka = false, oko = false;
  for (; spin < 200000; spin++) {
    asm volatile("" ::: "memory");
    if (!oko) ld6(po, pvo_o, grp);
    if (!oka) ld6(pa, pvo, 3);
    oko = clean6(po); oka = clean6(pa);
    if (oka && oko) break;
    __builtin_amdgcn_s_sleep(TAIL_SLEEP);
  }
  STAMP(8);
  waitv<0>();
  sfor<6>([&](auto ic) { constexpr int i = decltype(ic)::value;
    dd[i] = mfma(af, rdin(IC<1>{}, ic), dd[i]); dd[i] = mfma(af, __builtin_bit_cast(bf16x8, po[i]), dd[i]); dd[i] = mfma(as, __builtin_bit_cast(bf16x8, pa[i]), dd[i]); });
  STAMP(9);
#pragma unroll
  for (int i = 0; i < 3; i++) {
    float o[4];
#pragma unroll
    for (int j = 0; j < 4; j++) {
      float fv = dd[2 * i][j], sv = dd[2 * i + 1][j];
      swap32(fv, sv);
      const float gt = fv * __builtin_amdgcn_rcpf(1.f + __builtin_amdgcn_exp2f(-1.4426950408889634f * fv));
      o[j] = ((j & 1) ? bfhi(xv[i][j >> 1]) : bflo(xv[i][j >> 1])) + sv * gt;
    }
    *(u32x2*)(out + ooff[i]) = (u32x2){cvtpk(o[0], o[1]), cvtpk(o[2], o[3])};
  }
  if (sp == 0 && tid == 0) __hip_atomic_store((gu64*)(ws + EPOCH_OFF) + rt * 16, tag, RLX_AGENT);
  STAMP_FLUSH(1);
  asm volatile(".Lkernel_end:");
}

constexpr unsigned A_SLOT = 27648, A_XR = 110592, A_HALO = 143360, A_DUMMY = 161792, A_WOH = 125952, A_IMG = 872, A_OB = 0, A_OS = 296;
constexpr unsigned wo_slot(int n) { return n < 2 ? A_WOH + 18432u * n : (n < 6 ? 38912u + 18432u * (n - 2) : (n < 8 ? 18432u * (n - 6) : A_WOH)); }
constexpr int a_touch(int s) { return s == -1 ? 3 : 0; }
constexpr int wo_allowed(int N, int need) {
  int after = 0;
  auto op = [&](int tag) { if (tag == need) after = 0; else after += 2; };
  for (int n = 0; n < 6; n++) op(n);
  for (int n = 0; n <= N; n++) {
    if (n >= 2) op(20 + n - 1);
    if (n == N) break;
    if (n == 0) { op(6); op(7); }
    if (n == 1) { op(8); op(20); }
  }
  return after;
}
constexpr int item_pos(int cg, int tg, int C) {
  if (cg == 3) { const int i = (C < 3 ? C : C - 1) + 3 * tg, j = i >= 6 ? i - 6 : i; return j == 2 ? 25 : (j == 5 ? 26 : 20 + j - (j > 2)); }
  if (C < 4) return 8 + 4 * cg + (C ^ (2 * tg));
  return (cg == 2 && C == 6) ? 24 : 3 * cg + C - 4;
}

constexpr u64 pos_table(int C) { u64 t = 0; for (int w = 0; w < 8; w++) t |= (u64)item_pos(w & 3, w >> 2, C) << (5 * w); return t; }
template <int C> DI unsigned pos_of(int w) { return (unsigned)(pos_table(C) >> (5 * w)) & 31u; }

struct WoLd { u32x4 srd; unsigned vo1, vo2, so, so2; int w; };
DI WoLd wo_setup(const u16* wo, int hg, int w, int lane) {
  const int i8 = lane >> 3, c8 = lane & 7; const unsigned swz = 16u * (c8 ^ (2 * ((lane >> 4) & 3)));
  return {make_srd(wo, DM * DM * 2), (8 * (i8 >> 2) + (i8 & 3)) * (DM * 2) + swz, i8 * (DM * 2) + swz,
          (unsigned)((hg * 144 + 32 * (w >> 1) + 4 * (w & 1)) * (DM * 2)), (unsigned)((hg * 144 + 128 + 8 * w) * (DM * 2)), w};
}
template <int N, int I> DI void wo_load(const WoLd& l) {
  if constexpr (I < 2) dma(wo_slot(N) + (2 * l.w + I) * 1024, l.vo1, l.srd, l.so + I * 16 * DM * 2 + N * 128);
  else if (l.w < 2) dma(wo_slot(N) + (16 + l.w) * 1024, l.vo2, l.srd, l.so2 + N * 128);
}

DI void att_tail(const WoLd& wl, char* ws, int rt, int hg, int w, u64 tag
) {
  int tid = threadIdx.x; pin(tid);
  const int lane = tid & 63, tg = w >> 2, cg = w & 3;
  const int g = lane >> 4, q = (lane & 15) >> 2, p = lane & 3, c16 = lane & 15;
  const f32x4 zero4 = {0.f, 0.f, 0.f, 0.f};
  bf16x4 wlo[4], whi[4];
  auto wimg = [&](unsigned a, const f32x4& v) { lds_wr64(a, (u32x2){cvtpk(v[0], v[1]), cvtpk(v[2], v[3])}); };
  const gu64* ap = (const gu64*)(ws + ACK_OFF) + rt * 16 + (c16 < 12 ? c16 : 0);
  u64 ack = poll_load(ap);
  auto loado = [&](auto Nc, auto Ic) { wo_load<decltype(Nc)::value, decltype(Ic)::value>(wl); };
  {
    const int hp = w >> 1, par = w & 1;
    const unsigned r0 = (2 * c16 + par + 16) * A_IMG + 72 * hp, rm = (unsigned)max(2 * c16 + par - 16, 0) * A_IMG + 72 * hp + 288;
    unsigned rq[3] = {r0 + 8 * g, r0 + 8 * g + 40, r0 + 32};
    unsigned rk[3] = {rq[0] + 288, rq[2] + 288, rq[1] + 288};
    unsigned rkm[3] = {rm + 8 * g, rm + 32, rm + 8 * g + 40};
    const int kr = 4 * g + q;
    unsigned rv = (2 * kr + par + 16) * A_IMG + 576 + 72 * hp + 8 * p, rv3 = rv + 96 * A_IMG;
    unsigned rvm = (unsigned)max(2 * kr + par - 16, 0) * A_IMG + 576 + 72 * hp + 8 * p;
    unsigned rq3[3], rk3[3];
#pragma unroll
    for (int i = 0; i < 3; i++) { rq3[i] = rq[i] + 96 * A_IMG; pin(rq[i]); pin(rq3[i]); rk3[i] = rk[i] + 96 * A_IMG; pin(rk[i]); pin(rk3[i]); pin(rkm[i]); }
    pin(rv); pin(rv3); pin(rvm);
    f32x4 biasA, biasA0, biasB; unsigned mA[2], mA0[2], mB[2];
#pragma unroll
    for (int j = 0; j < 4; j++) {
      const int kk = 4 * g + j; const bool va = kk >= c16 + 9, vb = (kk <= c16) && (kk + 7 >= c16);
      biasA[j] = va ? 0.f : -1e30f; biasB[j] = vb ? 0.f : -1e30f; biasA0[j] = (va && rt > 0) ? 0.f : -1e30f;
      const unsigned bm = (j & 1) ? 0xffff0000u : 0x0000ffffu;
      if ((j & 1) == 0) { mA[j >> 1] = 0; mB[j >> 1] = 0; }
      mA[j >> 1] |= va ? bm : 0u; mB[j >> 1] |= vb ? bm : 0u;
    }
    mA0[0] = rt > 0 ? mA[0] : 0u; mA0[1] = rt > 0 ? mA[1] : 0u;
    pin(biasA); pin(biasA0); pin(biasB); pin(mA[0]); pin(mA[1]); pin(mA0[0]); pin(mA0[1]); pin(mB[0]); pin(mB[1]);
    unsigned qmask = g == 0 ? 0xffffffffu : 0u; pin(qmask);
    unsigned ow0 = A_OB + (2 * c16 + par) * A_OS + 72 * hp, owa = ow0 + 8 * g, owb = owa + 40; pin(ow0); pin(owa); pin(owb);
    waitl0(); bar();
    STAMP(3);
    auto asb = [](u32x2 a, u32x2 b) { return __builtin_bit_cast(bf16x8, (u32x4){a[0], a[1], b[0], b[1]}); };
    typedef unsigned u32x8 __attribute__((ext_vector_type(8)));
    typedef unsigned u32x16 __attribute__((ext_vector_type(16)));
    u32x8 kb[5]; u32x16 vb[3]; u32x2 qm[4][2], q4[4];
    auto rdk = [&](auto Uc) { constexpr int U = decltype(Uc)::value;
      sfor<3>([&](auto jc) { constexpr int j = decltype(jc)::value;
        const u32x2 t = lds_rd64(U == 0 ? rkm[j] : (U < 4 ? rk[j] + (U > 0 ? U - 1 : 0) * 32 * A_IMG : rk3[j]));
        kb[U][2 * j] = t[0]; kb[U][2 * j + 1] = t[1]; });
      kb[U][6] = 0u; kb[U][7] = 0u; };
    rdk(IC<0>{});
    sfor<4>([&](auto Uc) { constexpr int U = decltype(Uc)::value; constexpr unsigned ro = U < 3 ? U * 32 * A_IMG : 0;
      q4[U] = lds_rd64((U < 3 ? rq[2] : rq3[2]) + ro);
      sfor<2>([&](auto Hc) { constexpr int H = decltype(Hc)::value; qm[U][H] = lds_rd64((U < 3 ? rq[H] : rq3[H]) + ro); });
      rdk(IC<U + 1>{});
      GAP(); });
    sfor<5>([&](auto Uc) { constexpr int U = decltype(Uc)::value;
      sfor<3>([&](auto Dc) { constexpr int D = decltype(Dc)::value; constexpr unsigned co = D == 0 ? 0 : (D == 1 ? 40 : 32);
        const u32x2 t = __builtin_bit_cast(u32x2, lds_tr((U == 0 ? rvm : (U < 4 ? rv + (U > 0 ? U - 1 : 0) * 32 * A_IMG : rv3)) + co));
        vb[D][2 * U] = t[0]; vb[D][2 * U + 1] = t[1]; }); });
    GAP();
    auto kfrag = [&](auto Uc, auto Hc) { constexpr int U = decltype(Uc)::value, O = 2 * decltype(Hc)::value;
      return __builtin_bit_cast(bf16x8, __builtin_shufflevector(kb[U], kb[U], O, O + 1, O + 2, O + 3)); };
    auto vfrag = [&](auto Uc, auto Dc) { constexpr int O = 2 * decltype(Uc)::value, D = decltype(Dc)::value;
      return __builtin_bit_cast(bf16x8, __builtin_shufflevector(vb[D], vb[D], O, O + 1, O + 2, O + 3)); };
    f32x4 sa[8], sb[8];
    sfor<8>([&](auto Ic) { constexpr int I = decltype(Ic)::value, U = I >> 1, H = I & 1;
      const bf16x8 qf = H ? asb((u32x2){0u, q4[U][1] & qmask}, qm[U][1]) : asb(qm[U][0], (u32x2){q4[U][0] & qmask, 0u});
      sa[I] = mfma(kfrag(IC<U>{}, IC<H>{}), qf, U == 0 ? biasA0 : biasA); sb[I] = mfma(kfrag(IC<U + 1>{}, IC<H>{}), qf, biasB); });
    GAP();
    float v[8][4], mx[8];
    sfor<8>([&](auto Ic) { constexpr int I = decltype(Ic)::value;
#pragma unroll
      for (int j = 0; j < 4; j++) v[I][j] = fmx(sa[I][j], sb[I][j]);
      mx[I] = fmx(fmx(fmx(v[I][0], v[I][1]), v[I][2]), v[I][3]); });
    constexpr float SC = 0.23570226039551584f * 1.4426950408889634f;
    sfor<2>([&](auto hc) { constexpr int B = 4 * decltype(hc)::value;
      unpack4(-SC * pack_max(mx[B], mx[B + 1], mx[B + 2], mx[B + 3]), mx[B], mx[B + 1], mx[B + 2], mx[B + 3]); });
    GAP();
    waitl0(); bar();
    STAMP(4);
    sfor<3>([&](auto i) { loado(IC<2>{}, i); }); sfor<3>([&](auto i) { loado(IC<3>{}, i); });
    GAP();
    float sm[8];
    sfor<8>([&](auto Ic) { constexpr int I = decltype(Ic)::value;
#pragma unroll
      for (int j = 0; j < 4; j++) v[I][j] = __builtin_amdgcn_exp2f(__builtin_fmaf(v[I][j], SC, mx[I]));
      sm[I] = (v[I][0] + v[I][1]) + (v[I][2] + v[I][3]); });
    sfor<2>([&](auto hc) { constexpr int B = 4 * decltype(hc)::value;
      unpack4(__builtin_amdgcn_rcpf(pack_sum(sm[B], sm[B + 1], sm[B + 2], sm[B + 3])), sm[B], sm[B + 1], sm[B + 2], sm[B + 3]); });
    GAP();
    STAMP(5);
    sfor<3>([&](auto i) { loado(IC<4>{}, i); }); sfor<3>([&](auto i) { loado(IC<5>{}, i); });
    GAP();
    bf16x8 pf[8];
    sfor<8>([&](auto Ic) { constexpr int I = decltype(Ic)::value, U = I >> 1;
      const unsigned p0 = cvtpk(v[I][0] * sm[I], v[I][1] * sm[I]), p1 = cvtpk(v[I][2] * sm[I], v[I][3] * sm[I]);
      pf[I] = __builtin_bit_cast(bf16x8, (u32x4){p0 & (U == 0 ? mA0[0] : mA[0]), p1 & (U == 0 ? mA0[1] : mA[1]), p0 & mB[0], p1 & mB[1]}); });
    GAP();
    f32x4 o0[8], o1[8];
    sfor<8>([&](auto Ic) { constexpr int I = decltype(Ic)::value; o0[I] = mfma(vfrag(IC<(I >> 1)>{}, IC<(I & 1)>{}), pf[I], zero4); });
    sfor<8>([&](auto Ic) { constexpr int I = decltype(Ic)::value; o1[I] = mfma(vfrag(IC<(I >> 1)>{}, IC<2>{}), pf[I], zero4); });
    GAP();
    sfor<8>([&](auto Ic) { constexpr int I = decltype(Ic)::value; wimg(((I & 1) ? owb : owa) + (I >> 1) * 32 * A_OS, o0[I]); });
    if (g == 0) {
      sfor<4>([&](auto Uc) { constexpr int U = decltype(Uc)::value;
        lds_wr64(ow0 + 32 + U * 32 * A_OS, (u32x2){cvtpk(o1[2 * U][0], o1[2 * U][1]), cvtpk(o1[2 * U + 1][2], o1[2 * U + 1][3])}); });
    }
  }
  const int row = 4 * g + q;
  const unsigned ow = row * 128 + 32 * (cg ^ ((row >> 1) & 3)) + 8 * p; unsigned owh = ow + 65536, owh2 = ow + A_WOH; pin(owh); pin(owh2);
  unsigned ob = A_OB + (tg * 64 + c16) * A_OS + 16 * g, ob4 = A_OB + (tg * 64 + c16) * A_OS + 256 + 8 * g; pin(ob); pin(ob4);
  waitv_tie<8>(ack);
  for (int spin = 0; spin < 200000 && !__all(ack == tag); spin++) { __builtin_amdgcn_s_sleep(1); ack = poll_load(ap); waitv_tie<0>(ack); }
  waitl0(); bar();
  STAMP(6);
  auto rdo = [&](auto GIc, auto Hc) {
    constexpr int GI = decltype(GIc)::value, H = decltype(Hc)::value, N = GI / 5, S = GI % 5;
    constexpr unsigned O = wo_slot(N) + S * 4096 + H * 2048;
    const unsigned a = O >= A_WOH ? owh2 + (O - A_WOH) : (O >= 65536 ? owh + (O - 65536) : ow + O);
    if constexpr (H == 0) wlo[GI & 3] = lds_tr(a); else if constexpr (S < 4) whi[GI & 3] = lds_tr(a);
  };
  bf16x8 of[4][4]; u32x2 of4[4];
  sfor<5>([&](auto Sc) { constexpr int S = decltype(Sc)::value;
    if constexpr (S < 3) { rdo(Sc, IC<0>{}); rdo(Sc, IC<1>{}); }
    sfor<4>([&](auto Tc) { constexpr int T = decltype(Tc)::value;
      if constexpr (S < 4) of[T][S & 3] = lds_rd128a8(ob + T * 16 * A_OS + 64 * S); else of4[T] = lds_rd64(ob4 + T * 16 * A_OS); });
    GAP(); });
  const __amdgpu_buffer_rsrc_t prs = __builtin_amdgcn_make_buffer_rsrc(ws + PART_OFF, 0, PART_BYTES, 0x00020000);
  const unsigned pso = (unsigned)(((rt * 9) * 16 + 12 + hg) * 16384 + w * 2048);
  f32x4 oc[4]; unsigned pk[8];
  GAP();
  sfor<45>([&](auto GIc) {
    constexpr int GI = decltype(GIc)::value, N = GI / 5, S = GI % 5;
    if constexpr (GI == 5) STAMP(7);
    if constexpr (GI == 15) STAMP(8);
    if constexpr (GI == 30) STAMP(9);
    const bf16x4 alo = wlo[GI & 3];
    bf16x8 a; if constexpr (S < 4) a = cat(alo, whi[GI & 3]);
    sfor<4>([&](auto Tc) {
      constexpr int T = decltype(Tc)::value;
      if constexpr (S == 0 && N > 0) { if constexpr (T == 0) { nop<2>(); GAP(); } pk[2 * T] = cvtpk(oc[T][0], oc[T][1]); pk[2 * T + 1] = cvtpk(oc[T][2], oc[T][3]); GAP(); }
      if constexpr (S == 0) mma0(oc[T], a, of[T][0]); else if constexpr (S < 4) mma(oc[T], a, of[T][S & 3]); else mma16(oc[T], alo, of4[T]);
      GAP();
      if constexpr (S == 0 && N > 0) { pk[2 * T] = nopoison(pk[2 * T]); pk[2 * T + 1] = nopoison(pk[2 * T + 1]); }
      if constexpr (S == 2 && T == 0 && (N == 0 || N == 1 || N == 4)) {
        if constexpr (N == 0) waitl0();
        waitv<wo_allowed(N, N == 0 ? 1 : (N == 1 ? 5 : 8))>(); bar();
      }
      if constexpr (GI + 3 < 45 && T < 2) rdo(IC<GI + 3>{}, Tc);
      if constexpr (N == 0 && S >= 3 && T > 0) loado(IC<3 + S>{}, IC<T - 1>{});
      if constexpr (N == 1 && S == 2 && T > 0) loado(IC<8>{}, IC<T - 1>{});
      if constexpr (S == (N == 1 ? 3 : 0) && N > 0 && (T & 1))
        __builtin_amdgcn_raw_buffer_store_b128((u32x4){pk[2 * T - 2], pk[2 * T - 1], pk[2 * T], pk[2 * T + 1]}, prs, lane * 16 + (T >> 1) * 1024, pso + (N - 1) * 16 * 16384, ST_AUX);
      GAP();
    });
  });
  nop<7>(); GAP();
  sfor<2>([&](auto hc) {
    constexpr int H = decltype(hc)::value;
    u32x4 o = {nopoison(cvtpk(oc[2 * H][0], oc[2 * H][1])), nopoison(cvtpk(oc[2 * H][2], oc[2 * H][3])), nopoison(cvtpk(oc[2 * H + 1][0], oc[2 * H + 1][1])), nopoison(cvtpk(oc[2 * H + 1][2], oc[2 * H + 1][3]))};
    __builtin_amdgcn_raw_buffer_store_b128(o, prs, lane * 16 + H * 1024, pso + 8 * 16 * 16384, ST_AUX);
  });
  STAMP(10);
  STAMP(11); STAMP_FLUSH(1);
}

DI void att_path(const u16* x, const u16* wqkv, const u16* wo, char* ws, int rt, int hg) {
  const int tid = threadIdx.x, lane = tid & 63;
  const int w = __builtin_amdgcn_readfirstlane(tid >> 6), tg = w >> 2, cg = w & 3;
  const int g = lane >> 4, q = (lane & 15) >> 2, p = lane & 3, c16 = lane & 15;
  STAMP_DECL; STAMP(0);
  const u32x4 srdx = make_srd(x, XN_BYTES), srdw = make_srd(wqkv, DM * 3 * DM * 2), srdo = make_srd(wo, DM * DM * 2);
  const int i8 = lane >> 3, c8 = lane & 7, l32 = lane & 31;
  const unsigned vox = i8 * (DM * 2) + c8 * 16;
  const unsigned sox = (unsigned)((rt * 128 + 16 * w) * (DM * 2));
  u64 epoch = rd_epoch(ws, rt);
  u32x4 xr[2][9], xh[3];
  {
    const unsigned soh = (unsigned)((rt > 0 ? rt * 128 - 16 + 2 * w : 0) * (DM * 2)), voh = (lane >> 5) * (DM * 2) + l32 * 16;
    xh[0] = gload<0>(voh, srdx, soh); xh[1] = gload<512>(voh, srdx, soh); xh[2] = gload<1024>(l32 < 8 ? voh : OOB, srdx, soh);
  }
  sfor<9>([&](auto tc) { constexpr int t = decltype(tc)::value;
    xr[0][t] = gload<t * 128>(vox, srdx, sox); xr[1][t] = gload<t * 128>(vox, srdx, sox + 8 * DM * 2); });
  unsigned vw[4];
#pragma unroll
  for (int i = 0; i < 4; i++) {
    const unsigned n = (w + 8 * i) * 64 + lane, r = (n * 9710u) >> 19, c = n - 54 * r;
    const unsigned k = 8 * ((r >> 2) & 3) + 4 * (r >> 4) + (r & 3);
    const unsigned sec = (c * 57u) >> 10;
    vw[i] = k * 3456 + c * 16 + sec * (1152 - 288);
  }
  auto loadw = [&](auto Sc, auto Ic) {
    constexpr int S = decltype(Sc)::value, I = decltype(Ic)::value;
    if constexpr (S < 18) { if (I < 3 || w < 3) dma(
(S & 3) * A_SLOT + (w + 8 * I) * 1024, vw[I], srdw, (unsigned)(S * 32 * 3456 + hg * 288)); }
  };
  sfor<4>([&](auto i) { loadw(IC<0>{}, i); });
  unsigned wbs[7];
  auto img_off = [&](unsigned pos) { const unsigned left = 0u - (unsigned)(pos >= 24), odd = hg & 1;
    return ((288u * (pos >> 3) + 32u * ((pos & 7) + odd)) & ~left) | ((288u * (pos - 24) + 256u - 256u * odd) & left); };
  {
    const unsigned lr = (4 * g + q) * 864 + 8 * p;
    sfor<7>([&](auto Cc) { constexpr int C = decltype(Cc)::value;
      const unsigned pos = pos_of<C>(w);
      wbs[C] = lr + img_off(pos);
      pin(wbs[C]); });
  }
  const unsigned xs = (16 * g + (c16 ^ g)) * 16;
  unsigned xb0 = A_XR + tg * 4096 + xs, xb1 = xb0 ^ 64, hb0 = A_HALO + xs, hb1 = hb0 ^ 64; pin(xb0); pin(xb1); pin(hb0); pin(hb1);
  unsigned xw = A_XR + (c8 >> 2) * 8192 + (16 * (c8 & 3) + (i8 ^ c8)) * 16 + w * 1024; pin(xw);
  GAP();
  STAMP(12);
  {
    Stat st = {}; float r, nmr;
    sfor<3>([&](auto ic) { constexpr int i = decltype(ic)::value; waitv_tie<3 + 18 + 2 - i>(xh[i]); stat_acc(xh[i], st); });
    stat_fin<true>(st, lane, r, nmr);
    const unsigned hwa = A_HALO + (l32 >> 2) * 1024 + (16 * (l32 & 3) + ((2 * w + (lane >> 5)) ^ c8)) * 16;
    lds_wr128(hwa, norm8(xh[0], r, nmr)); lds_wr128(hwa + 8192, norm8(xh[1], r, nmr));
    if (l32 < 8) lds_wr128(hwa + 16384, norm8(xh[2], r, nmr));
  }
  GAP();
  float rs[2], nm[2];
  {
    Stat st[2] = {};
    sfor<18>([&](auto ic) { constexpr int i = decltype(ic)::value;
      waitv_tie<3 + (i > 7 ? 3 : 0) + 17 - i>(xr[i & 1][i >> 1]); stat_acc(xr[i & 1][i >> 1], st[i & 1]); GAP();
      if constexpr (i == 7) { sfor<4>([&](auto k) { loadw(IC<1>{}, k); }); } });
    {
      const int jj = (w * 36 >> 3) + g;
      touch(A_DUMMY, (p < 3 && q < 3 && jj < 36) ? (unsigned)((jj * 16 + rt) * 3456 + q * 1152 + (2 * hg + p) * 128) : OOB, srdw, 0u);
      touch(A_DUMMY, (w & 1) && c16 < 9 && g == 0 ? (unsigned)(((w * 36 >> 3) + 4) * 16 + rt) * 3456 + (c16 / 3) * 1152 + (2 * hg + c16 % 3) * 128 : OOB, srdw, 0u);
      touch(A_DUMMY, lane < 11 && w * 11 + lane < 81 ? lane * 128u : OOB, srdo, (unsigned)((hg * 144 + rt * 9) * (DM * 2) + w * 11 * 128));
    }
    stat_fin<false>(st[0], lane, rs[0], nm[0]); stat_fin<false>(st[1], lane, rs[1], nm[1]);
    epoch = epoch_uniform(epoch);
  }
  STAMP(13);
  NormSt ns;
  auto xop = [&](auto Tc, auto Oc) {
    constexpr int T = decltype(Tc)::value, O = decltype(Oc)::value;
    if constexpr (T < 9 && O < 42) norm_op<O>(ns, xr[0][T < 9 ? T : 0], xr[1][T < 9 ? T : 0], rs, nm, xw + (T & 1) * 16384);
  };
  sfor<2>([&](auto r) { constexpr int R = decltype(r)::value; lds_wr128(xw + R * 128, norm8(xr[R][0], rs[R], nm[R])); });
  lds_wr128(xw + 16384, norm8(xr[0][1], rs[0], nm[0]));

  const f32x4 zero4 = {0.f, 0.f, 0.f, 0.f};
  const u64 tag = next_tag(epoch);
  f32x4 acc[7][4], hac[3];
  sfor<4>([&](auto t) { acc[3][decltype(t)::value] = zero4; }); hac[2] = zero4;
  bf16x8 xf[4], xhf;
  bf16x4 wlo[8], whi[8];
  WoLd wl;
  auto rdw = [&](auto GIc, auto Hc) {
    constexpr int GI = decltype(GIc)::value, H = decltype(Hc)::value, S = GI / 7, C = GI % 7;
    if constexpr (GI < 126) {
      const unsigned a = wbs[C] + (S & 1) * A_SLOT + H * 16 * 864;
      if constexpr (H == 0) wlo[GI & 7] = lds_tr(a); else whi[GI & 7] = lds_tr(a);
      if constexpr (H == 1 && (S & 1) == 1) { wbs[C] += (S & 2) ? -2 * A_SLOT : 2 * A_SLOT; pin(wbs[C]); }
    }
  };
  auto rdx = [&](auto Sc, auto Tc) {
    constexpr int S = decltype(Sc)::value, T = decltype(Tc)::value;
    xf[T] = lds_rd128(((S & 1) ? xb1 : xb0) + (S & 3) * 8192 + T * 1024);
  };
  auto rdxh = [&](auto Sc) { constexpr int S = decltype(Sc)::value; xhf = lds_rd128(((S & 1) ? hb1 : hb0) + S * 1024); };
  unsigned ih, ibase;
  unsigned toff[7];
  auto tileoff = [&](int C) { return toff[C]; };
  auto wimg = [&](unsigned a, const f32x4& v) { lds_wr64(a, (u32x2){cvtpk(v[0], v[1]), cvtpk(v[2], v[3])}); };
  STAMP(14);
  waitv<6>(); waitl0(); bar();
  STAMP(1);
  rdw(IC<0>{}, IC<0>{}); rdw(IC<0>{}, IC<1>{}); rdw(IC<1>{}, IC<0>{}); rdw(IC<1>{}, IC<1>{});
  sfor<4>([&](auto t) { rdx(IC<0>{}, t); });
  rdxh(IC<0>{});
  nop<1>();
  GAP();
  sfor<18>([&](auto Sc) {
    constexpr int S = decltype(Sc)::value, G0 = 7 * S;
    auto fill = [&](auto GPc) {
      constexpr int GP = decltype(GPc)::value;
      GAP();
      if constexpr (GP == 21 && S < 17) {
        if constexpr (S < 16) waitv<3 + a_touch(S) + a_touch(S - 1)>(); else waitv<0>();
        bar();
      }
      if constexpr (GP == 12 && S == 17) { waitl0(); bar(); }
      if constexpr (S == 17 && GP == 10) { int tz = tid; pin(tz); wl = wo_setup(wo, hg, w, tz & 63); }
      if constexpr (S == 17 && GP == 11) { int tz = tid; pin(tz); ih = (tz & 15) * A_IMG + 8 * ((tz >> 4) & 3); ibase = ih + (16 + tg * 64) * A_IMG;
        sfor<7>([&](auto Cc) { constexpr int C = decltype(Cc)::value; toff[C] = img_off(pos_of<C>(w)); }); }
      if constexpr (S == 0) xop(IC<1>{}, IC<GP + 21>{});
      else if constexpr ((S & 1) == 0) xop(IC<S / 2 + 1>{}, IC<GP + 8>{});
      else if constexpr (GP >= 22) xop(IC<(S + 3) / 2>{}, IC<GP - 22>{});
      else if constexpr (GP < 4 && S > 1) xop(IC<(S + 1) / 2>{}, IC<38 + GP>{});
      if constexpr (S < 17) {
        if constexpr (GP < 4) rdw(IC<G0 + 2 + (GP >> 1)>{}, IC<(GP & 1)>{});
        if constexpr (GP >= 8 && GP < 12) rdw(IC<G0 + 4 + ((GP - 8) >> 1)>{}, IC<(GP & 1)>{});
        if constexpr (GP == 15 || GP == 16) rdw(IC<G0 + 6>{}, IC<GP - 15>{});
        if constexpr (GP == 21 || GP == 22) rdw(IC<G0 + 7>{}, IC<GP - 21>{});
        if constexpr (GP == 24 || GP == 26) rdw(IC<G0 + 8>{}, IC<(GP - 24) / 2>{});
        if constexpr (GP >= 23 && (GP & 1) == 1) rdx(IC<S + 1>{}, IC<(GP - 23) / 2>{});
        if constexpr (GP == 28) rdxh(IC<S + 1>{});
      } else {
        if constexpr (GP < 10) rdw(IC<G0 + 2 + (GP >> 1)>{}, IC<(GP & 1)>{});
        if constexpr (GP >= 12 && GP < 20) wimg(ibase + tileoff((GP - 12) & 1) + ((GP - 12) >> 1) * 16 * A_IMG, acc[(GP - 12) & 1][(GP - 12) >> 1]);
        if constexpr (GP == 13 || GP == 15) wimg(ih + tileoff((GP - 13) / 2), hac[(GP - 13) / 2]);
        if constexpr (GP >= 20 && GP < 24) wimg(ibase + tileoff(2) + (GP - 20) * 16 * A_IMG, acc[2][GP - 20]);
        if constexpr (GP >= 26) wimg(ibase + tileoff(4) + (GP - 26) * 16 * A_IMG, acc[4][GP - 26]);
        if constexpr (GP == 14 || GP == 17 || GP == 19) wo_load<0, (GP == 14 ? 0 : (GP == 17 ? 1 : 2))>(wl);
        if constexpr (GP == 21 || GP == 24 || GP == 25) wo_load<1, (GP == 21 ? 0 : (GP == 24 ? 1 : 2))>(wl);
      }
      if constexpr (GP == 4 || GP == 6 || GP == 12 || GP == 17) loadw(IC<S + 2>{}, IC<(GP == 4 ? 0 : (GP == 6 ? 1 : (GP == 12 ? 2 : 3)))>{});
      GAP();
    };
    auto mm = [&](f32x4& c, const bf16x8& a, const bf16x8& b) { if constexpr (S == 0) mma0(c, a, b); else mma(c, a, b); };
    const bf16x8 a0 = cat(wlo[G0 & 7], whi[G0 & 7]), a1 = cat(wlo[(G0 + 1) & 7], whi[(G0 + 1) & 7]);
    sfor<4>([&](auto Tc) { constexpr int T = decltype(Tc)::value;
      mm(acc[0][T], a0, xf[T]); fill(IC<2 * T>{}); mm(acc[1][T], a1, xf[T]); fill(IC<2 * T + 1>{}); });
    mm(hac[0], a0, xhf); fill(IC<8>{});
    mm(hac[1], a1, xhf); fill(IC<9>{});
    const bf16x8 a2 = cat(wlo[(G0 + 2) & 7], whi[(G0 + 2) & 7]);
    sfor<4>([&](auto Tc) { constexpr int T = decltype(Tc)::value; mm(acc[2][T], a2, xf[T]); fill(IC<10 + T>{}); });
    const bf16x8 a3 = cat(wlo[(G0 + 3) & 7], whi[(G0 + 3) & 7]);
    mma_alt3(acc[3][0], a3, xf[0], hac[2], a2, xhf, cg); fill(IC<14>{});
    sfor<3>([&](auto Tc) { constexpr int T = decltype(Tc)::value + 1; mma_not3(acc[3][T], a3, xf[T], cg); fill(IC<14 + T>{}); });
    const bf16x8 a4 = cat(wlo[(G0 + 4) & 7], whi[(G0 + 4) & 7]);
    sfor<4>([&](auto Tc) { constexpr int T = decltype(Tc)::value; mm(acc[4][T], a4, xf[T]); fill(IC<18 + T>{}); });
    const bf16x8 a5 = cat(wlo[(G0 + 5) & 7], whi[(G0 + 5) & 7]), a6 = cat(wlo[(G0 + 6) & 7], whi[(G0 + 6) & 7]);
    sfor<4>([&](auto Tc) { constexpr int T = decltype(Tc)::value;
      mm(acc[5][T], a5, xf[T]); fill(IC<22 + 2 * T>{}); mm(acc[6][T], a6, xf[T]); fill(IC<23 + 2 * T>{}); });
  });
  STAMP(2);
  {
    if (cg < 3) { sfor<4>([&](auto Tc) { constexpr int T = decltype(Tc)::value; wimg(ibase + tileoff(3) + T * 16 * A_IMG, acc[3][T]); }); }
    else wimg(ih + tileoff(2), hac[2]);
    sfor<4>([&](auto Tc) { constexpr int T = decltype(Tc)::value;
      wimg(ibase + tileoff(5) + T * 16 * A_IMG, acc[5][T]); wimg(ibase + tileoff(6) + T * 16 * A_IMG, acc[6][T]); });
  }
  GAP();
  att_tail(wl, ws, rt, hg, w, tag
  );
}

__global__ __launch_bounds__(512, 2) void k1_main(const u16* __restrict__ x, const u16* __restrict__ wqkv, const u16* __restrict__ wo,
                                                  const u16* __restrict__ w1, const u16* __restrict__ w2, char* __restrict__ ws, u16* __restrict__ out) {
  const int b = blockIdx.x, xc = b & 7, i = b >> 3;
  const int rt = 4 * (xc >> 1) + (i & 3), j = i >> 2;
  { const int ln = threadIdx.x & 63, wv = threadIdx.x >> 6;
    touch(163584u, (ln < 2) ? (unsigned)((i * 16 + wv * 2 + ln) * 128) : OOB, code_srd(), 0u); }
  if (j < 6) ffn_path(x, w1, w2, ws, out, rt, 6 * (xc & 1) + j);
  else att_path(x, wqkv, wo, ws, rt, 2 * (xc & 1) + (j - 6));
}

extern "C" void kernel_launch(void* const* d_in, const int* in_sizes, int n_in, void* d_out, int out_size, void* d_ws, size_t ws_size, hipStream_t stream) {
  const u16* x = (const u16*)d_in[0]; const u16* wqkv = (const u16*)d_in[1]; const u16* wo = (const u16*)d_in[2];
  const u16* w1 = (const u16*)d_in[4]; const u16* w2 = (const u16*)d_in[5];
  (void)hipFuncSetAttribute((const void*)k1_main, hipFuncAttributeMaxDynamicSharedMemorySize, 163840);
  k1_main<<<256, 512, 163840, stream>>>(x, wqkv, wo, w1, w2, (char*)d_ws, (u16*)d_out);
}
```

```cpp
#include <hip/hip_runtime.h>
#include <utility>
#define STAMPS

typedef unsigned short u16;
typedef __bf16 bf16x2 __attribute__((ext_vector_type(2)));
typedef __bf16 bf16x4 __attribute__((ext_vector_type(4)));
typedef __bf16 bf16x8 __attribute__((ext_vector_type(8)));
typedef short s16x2 __attribute__((ext_vector_type(2)));
typedef float f32x2 __attribute__((ext_vector_type(2)));
typedef float f32x4 __attribute__((ext_vector_type(4)));
typedef unsigned u32x2 __attribute__((ext_vector_type(2)));
typedef unsigned u32x4 __attribute__((ext_vector_type(4)));
#define LDSP(T) __attribute__((address_space(3))) T*
#define DI __device__ __forceinline__
#define GAP() __builtin_amdgcn_sched_barrier(0)
template <int V> using IC = std::integral_constant<int, V>;
template <class F, int... Is> DI void sfor_(F&& f, std::integer_sequence<int, Is...>) { (f(IC<Is>{}), ...); }
template <int N, class F> DI void sfor(F&& f) { sfor_(f, std::make_integer_sequence<int, N>{}); }

constexpr int SEQ = 2048, DM = 576, DFF = 4608;
constexpr unsigned XN_BYTES = SEQ * DM * 2;
constexpr size_t PART_OFF = 0;
constexpr unsigned PART_BYTES = 16u * XN_BYTES;
constexpr size_t FLAG_OFF = PART_OFF + PART_BYTES;
constexpr size_t ACK_OFF = FLAG_OFF + 16 * 9 * 16 * 8;
constexpr size_t EPOCH_OFF = ACK_OFF + 16 * 16 * 8;
typedef unsigned long long u64;
typedef __attribute__((address_space(1))) u64 gu64;
#define RLX_AGENT __ATOMIC_RELAXED, __HIP_MEMORY_SCOPE_AGENT
#define TAIL_SLEEP 4
#define ST_AUX 16

DI unsigned cvtpk(float lo, float hi) { f32x2 v = {lo, hi}; return __builtin_bit_cast(unsigned, __builtin_convertvector(v, bf16x2)); }
DI unsigned relupk(unsigned p) { s16x2 z = {0, 0}; return __builtin_bit_cast(unsigned, __builtin_elementwise_max(__builtin_bit_cast(s16x2, p), z)); }
DI float bflo(unsigned p) { return __builtin_bit_cast(float, p << 16); }
DI float bfhi(unsigned p) { return __builtin_bit_cast(float, p & 0xffff0000u); }
DI bf16x4 lds_tr(unsigned addr) { return __builtin_amdgcn_ds_read_tr16_b64_v4bf16((LDSP(bf16x4))(size_t)addr); }
DI bf16x8 lds_rd128(unsigned addr) { return *(LDSP(bf16x8))(size_t)addr; }
typedef bf16x8 bf16x8a8 __attribute__((aligned(8)));
DI bf16x8 lds_rd128a8(unsigned addr) { return *(LDSP(bf16x8a8))(size_t)addr; }
DI u32x2 lds_rd64(unsigned addr) { return *(LDSP(u32x2))(size_t)addr; }
DI void lds_wr128(unsigned addr, u32x4 v) { *(LDSP(u32x4))(size_t)addr = v; }
DI void lds_wr64(unsigned addr, u32x2 v) { *(LDSP(u32x2))(size_t)addr = v; }
DI bf16x8 cat(bf16x4 a, bf16x4 b) { return __builtin_shufflevector(a, b, 0, 1, 2, 3, 4, 5, 6, 7); }
DI f32x4 mfma(bf16x8 a, bf16x8 b, f32x4 c) { return __builtin_amdgcn_mfma_f32_16x16x32_bf16(a, b, c, 0, 0, 0); }
DI void mma(f32x4& c, bf16x8 a, bf16x8 b) { asm volatile("v_mfma_f32_16x16x32_bf16 %0, %1, %2, %0" : "+v"(c) : "v"(a), "v"(b)); }
DI void mma0(f32x4& c, bf16x8 a, bf16x8 b) { asm volatile("v_mfma_f32_16x16x32_bf16 %0, %1, %2, 0" : "=&v"(c) : "v"(a), "v"(b)); }
DI void mma16(f32x4& c, bf16x4 a, u32x2 b) { asm volatile("v_mfma_f32_16x16x16_bf16 %0, %1, %2, %0" : "+v"(c) : "v"(a), "v"(b)); }
DI void mma_not3(f32x4& c, bf16x8 a, bf16x8 b, int cg) {
  asm volatile("s_cmp_eq_u32 %3, 3\n\ts_cbranch_scc1 .Le%=\n\tv_mfma_f32_16x16x32_bf16 %0, %1, %2, %0\n.Le%=:" : "+v"(c) : "v"(a), "v"(b), "s"(cg) : "scc");
}
DI void mma_alt3(f32x4& c, bf16x8 a, bf16x8 b, f32x4& h, bf16x8 ah, bf16x8 bh, int cg) {
  asm volatile("s_cmp_eq_u32 %6, 3\n\ts_cbranch_scc1 .Lh%=\n\tv_mfma_f32_16x16x32_bf16 %0, %2, %3, %0\n\ts_branch .Le%=\n"
               ".Lh%=:\n\tv_mfma_f32_16x16x32_bf16 %1, %4, %5, %1\n.Le%=:" : "+v"(c), "+v"(h) : "v"(a), "v"(b), "v"(ah), "v"(bh), "s"(cg) : "scc");
}
template <int N> DI void nop() { asm volatile("s_nop %0" ::"i"(N)); }
template <int N> DI void waitv() { asm volatile("s_waitcnt vmcnt(%0)" ::"i"(N) : "memory"); }
DI void waitl0() { asm volatile("s_waitcnt lgkmcnt(0)" ::: "memory"); }
DI void bar() { asm volatile("s_barrier" ::: "memory"); }
DI u32x4 make_srd(const void* p, unsigned bytes) {
  size_t a = (size_t)p; u32x4 s;
  s[0] = __builtin_amdgcn_readfirstlane((unsigned)a); s[1] = __builtin_amdgcn_readfirstlane((unsigned)(a >> 32));
  s[2] = bytes; s[3] = 0x00020000u; return s;
}
DI void dma(unsigned lds_dst, unsigned voff, u32x4 srd, unsigned soff) {
  asm volatile("s_mov_b32 m0, %0\n\ts_nop 0\n\tbuffer_load_dwordx4 %1, %2, %3 offen lds" ::"s"(lds_dst), "v"(voff), "s"(srd), "s"(soff) : "memory");
}
DI void dma_sc1(unsigned lds_dst, unsigned voff, u32x4 srd, unsigned soff) {
  asm volatile("s_mov_b32 m0, %0\n\ts_nop 0\n\tbuffer_load_dwordx4 %1, %2, %3 offen sc1 lds" ::"s"(lds_dst), "v"(voff), "s"(srd), "s"(soff) : "memory");
}
template <int CTRL> DI float dppadd(float v) { return v + __builtin_bit_cast(float, __builtin_amdgcn_update_dpp(0, __builtin_bit_cast(int, v), CTRL, 0xF, 0xF, true)); }
DI float red8(float v) { v = dppadd<0xB1>(v); v = dppadd<0x4E>(v); return dppadd<0x141>(v); }
DI float fmx(float a, float b) { return __builtin_elementwise_maximum(a, b); }
DI void swap16(float& a, float& b) {
  auto r = __builtin_amdgcn_permlane16_swap(__builtin_bit_cast(unsigned, a), __builtin_bit_cast(unsigned, b), false, false);
  const unsigned r0 = r[0], r1 = r[1]; a = __builtin_bit_cast(float, r0); b = __builtin_bit_cast(float, r1);
}
DI void swap32(float& a, float& b) {
  auto r = __builtin_amdgcn_permlane32_swap(__builtin_bit_cast(unsigned, a), __builtin_bit_cast(unsigned, b), false, false);
  const unsigned r0 = r[0], r1 = r[1]; a = __builtin_bit_cast(float, r0); b = __builtin_bit_cast(float, r1);
}
template <class T> DI void pin(T& v) { asm volatile("" : "+v"(v)); }
DI float pack_max(float a, float b, float c, float d) { swap16(a, b); a = fmx(a, b); swap16(c, d); c = fmx(c, d); swap32(a, c); return fmx(a, c); }
DI float pack_sum(float a, float b, float c, float d) { swap16(a, b); a = a + b; swap16(c, d); c = c + d; swap32(a, c); return a + c; }
DI void unpack4(float x, float& a, float& b, float& c, float& d) { a = x; c = x; swap32(a, c); b = a; swap16(a, b); d = c; swap16(c, d); }
template <int OFF> DI u32x4 gload(unsigned voff, u32x4 srd, unsigned soff) {
  u32x4 r; asm volatile("buffer_load_dwordx4 %0, %1, %2, %3 offen offset:%4" : "=v"(r) : "v"(voff), "s"(srd), "s"(soff), "i"(OFF)); return r;
}
DI void touch(unsigned lds_dst, unsigned voff, u32x4 srd, unsigned soff) {
  asm volatile("s_mov_b32 m0, %0\n\ts_nop 0\n\tbuffer_load_dword %1, %2, %3 offen lds" ::"s"(lds_dst), "v"(voff), "s"(srd), "s"(soff) : "memory");
}
constexpr unsigned OOB = 0x7fffffffu;
DI u32x4 code_srd() {
  unsigned long long pc; unsigned len;
  asm volatile("s_getpc_b64 %0\n.Lcs%=:\n\ts_mov_b32 %1, .Lkernel_end-.Lcs%=\n\ts_max_i32 %1, %1, 0\n\ts_min_i32 %1, %1, 0x10000" : "=s"(pc), "=s"(len) :: "scc");
  u32x4 r; r[0] = (unsigned)pc; r[1] = (unsigned)(pc >> 32); r[2] = len; r[3] = 0x00020000u; return r;
}
DI u64 poll_load(const gu64* p) { u64 r; asm volatile("global_load_dwordx2 %0, %1, off sc1" : "=v"(r) : "v"(p) : "memory"); return r; }
template <int N> DI void waitv_tie(u64& r) { asm volatile("s_waitcnt vmcnt(%1)" : "+v"(r) : "i"(N)); }
template <int N> DI void waitv_tie(u32x4& r) { asm volatile("s_waitcnt vmcnt(%1)" : "+v"(r) : "i"(N)); }
constexpr unsigned POISON = 0xffffffffu;
DI unsigned nopoison(unsigned v) { return v < POISON - 1 ? v : POISON - 1; }
DI bool clean6(const u32x4 (&v)[6]) {
  unsigned m = 0;
#pragma unroll
  for (int i = 0; i < 6; i++) m = max(max(m, max(v[i][0], v[i][1])), max(v[i][2], v[i][3]));
  return __all(m != POISON) != 0;
}
DI u64 next_tag(u64 e) { return (e ^ 0x9E3779B97F4A7C15ull) * 0xD1342543DE82EF95ull + 0x632BE59BD9B4E019ull; }
DI u64 rd_epoch(char* ws, int rt) {
  return poll_load((const gu64*)(ws + EPOCH_OFF) + rt * 16);
}
DI u64 epoch_uniform(u64 e) {
  asm volatile("" : "+v"(e));
  return ((u64)(unsigned)__builtin_amdgcn_readfirstlane((unsigned)(e >> 32)) << 32) | (unsigned)__builtin_amdgcn_readfirstlane((unsigned)e);
}
DI void set_flag(char* ws, int rt, int n, int src, u64 tag) {
  __hip_atomic_store((gu64*)(ws + FLAG_OFF) + (rt * 9 + n) * 16 + src, tag, RLX_AGENT);
}
typedef short s16x4 __attribute__((ext_vector_type(4)));
struct Stat { f32x4 s1, s2; };
DI void stat_acc(const u32x4& c, Stat& st) {
  const s16x4 ones = {0x3f80, 0x3f80, 0x3f80, 0x3f80};
  const s16x4 a = __builtin_bit_cast(s16x4, (u32x2){c[0], c[1]}), b = __builtin_bit_cast(s16x4, (u32x2){c[2], c[3]});
  st.s1 = __builtin_amdgcn_mfma_f32_4x4x4bf16_1k(a, ones, st.s1, 0, 0, 0);
  st.s2 = __builtin_amdgcn_mfma_f32_4x4x4bf16_1k(a, a, st.s2, 0, 0, 0);
  st.s1 = __builtin_amdgcn_mfma_f32_4x4x4bf16_1k(b, ones, st.s1, 0, 0, 0);
  st.s2 = __builtin_amdgcn_mfma_f32_4x4x4bf16_1k(b, b, st.s2, 0, 0, 0);
}
template <bool WIDE> DI void stat_fin(const Stat& st, int lane, float& r, float& nmr) {
  float s1 = dppadd<0x141>((st.s1[0] + st.s1[1]) + (st.s1[2] + st.s1[3]));
  const float d01 = (lane & 1) ? st.s2[1] : st.s2[0], d23 = (lane & 1) ? st.s2[3] : st.s2[2];
  float s2 = red8((lane & 2) ? d23 : d01);
  if constexpr (WIDE) {
    s1 = dppadd<0x140>(s1); s2 = dppadd<0x140>(s2);
    float t1 = s1, t2 = s2; swap16(s1, t1); swap16(s2, t2); s1 += t1; s2 += t2;
  }
  const float m = s1 * (1.f / DM);
  const float var = fmaxf(s2 * (1.f / DM) - m * m, 0.f);
  r = __builtin_amdgcn_rsqf(var + 1e-5f); nmr = -m * r;
}
struct NormSt { float lo, hi; u32x4 o; };
template <int O> DI void norm_op(NormSt& st, const u32x4& c0, const u32x4& c1, const float (&rs)[2], const float (&nm)[2], unsigned addr) {
  constexpr int R = O / 21, Q = O % 21, J = Q / 5, K = Q % 5;
  const u32x4& c = R ? c1 : c0;
  if constexpr (Q == 20) lds_wr128(addr + R * 128, st.o);
  else if constexpr (K == 0) { st.lo = bflo(c[J]); pin(st.lo); }
  else if constexpr (K == 1) { st.hi = bfhi(c[J]); pin(st.hi); }
  else if constexpr (K == 2) { st.lo = __builtin_fmaf(st.lo, rs[R], nm[R]); pin(st.lo); }
  else if constexpr (K == 3) { st.hi = __builtin_fmaf(st.hi, rs[R], nm[R]); pin(st.hi); }
  else { unsigned t = cvtpk(st.lo, st.hi); pin(t); st.o[J] = t; }
}
DI unsigned norm2(unsigned p, float r, float nmr) { return cvtpk(__builtin_fmaf(bflo(p), r, nmr), __builtin_fmaf(bfhi(p), r, nmr)); }
DI u32x4 norm8(const u32x4& c, float r, float nmr) { return (u32x4){norm2(c[0], r, nmr), norm2(c[1], r, nmr), norm2(c[2], r, nmr), norm2(c[3], r, nmr)}; }

constexpr size_t STAMP_OFF = 64u << 20;
#define STAMP_DECL unsigned long long st_[16] = {}, sc_[16] = {}
#define STAMP(idx) do { GAP(); st_[idx] = __builtin_amdgcn_s_memrealtime(); sc_[idx] = __builtin_amdgcn_s_memtime(); __builtin_amdgcn_s_waitcnt(0xC07F); GAP(); } while (0)
#define STAMP_FLUSH(kid) do { if (threadIdx.x == 0) printf("ST %d %llu %llu %llu %llu %llu %llu %llu %llu\n", (int)blockIdx.x, st_[0], st_[1], st_[2], st_[4], st_[6], st_[7], st_[8], st_[10]); } while (0)

constexpr int p2_first(int j) { return j < 2 ? 0 : (j <= 7 ? 2 * j : j + 8); }
constexpr int p2_count(int j) { return j < 2 ? 0 : (j <= 7 ? 2 : (j + 8 <= 26 ? 1 : 0)); }
constexpr int POLL1 = 16, USE1 = 19, POLL2 = 25, LASTFLAG = 21;
constexpr int inbox_count(int j) { return (j >= USE1 + 1 && j <= USE1 + 3) ? 2 : 0; }
constexpr int vm_allowed(int T, int need) {
  int after = 0;
  auto op = [&](int tag, int n) { if (tag == need) after = 0; else after += n; };
  op(0, 3); op(1, 3); op(2, 3); op(298, 2);
  if (T < 0) return after;
  for (int t = 0; t < 18; t++) {
    if (t <= 14) op(t + 3, 3);
    if (t >= 3 && t < 15) op(297, 1);
    else if (t == 15) { op(100, 2); op(101, 2); }
    else if (t == 16) op(102, 2);
    else op(103, 2);
    if (t == T) return after;
  }
  if (T == 18) return after;
  for (int j = 0; j < 27; j++) {
    for (int c = 0; c < p2_count(j); c++) op(100 + p2_first(j) + c, 2);
    op(300, inbox_count(j));
    if (j == POLL1) op(301, 1);
    if (j == POLL2) op(302, 1);
    if (j >= 3 && j % 3 == 0) op(200 + j / 3 - 1, 2);
    if (19 + j == T) return after;
  }
  return 0;
}
constexpr int imin(int a, int b) { return a < b ? a : b; }
constexpr int p2_wait(int j) {
  int a = j < 26 ? vm_allowed(19 + j, 100 + j + 1) : 63;
  if (j >= 6 && j % 3 == 0 && j <= LASTFLAG) a = imin(a, vm_allowed(19 + j, 200 + j / 3 - 2));
  return a;
}
constexpr unsigned w1_slot(int s) { constexpr int pi[5] = {2, 0, 1, 4, 3}; return 32768u + 24576u * pi[s % 5]; }
constexpr unsigned w2_slot(int j) { constexpr int pi[10] = {8, 9, 7, 6, 0, 1, 2, 3, 4, 5}; return 16384u * pi[j % 10]; }
constexpr unsigned inbox_at(int a, int i) { return w2_slot(17 + 3 * a + i / 2) + (i & 1) * 1024u; }
constexpr unsigned F_OWN = w2_slot(23);
constexpr unsigned F_DUMMY = 159744;

DI void ffn_path(const u16* x, const u16* w1, const u16* w2, char* ws, u16* out, int rt, int sp) {
  const int tid = threadIdx.x, lane = tid & 63;
  const int w = __builtin_amdgcn_readfirstlane(tid >> 6), tg = w >> 2, cg = w & 3;
  const int g = lane >> 4, q = (lane & 15) >> 2, p = lane & 3;
  STAMP_DECL; STAMP(0);
  const u32x4 srdx = make_srd(x, XN_BYTES), srd1 = make_srd(w1, DM * DFF * 2), srd2 = make_srd(w2, DM * DFF * 2);
  const int i8 = lane >> 3, c8 = lane & 7;
  const unsigned vox = i8 * (DM * 2) + c8 * 16;
  const unsigned sox = (unsigned)((rt * 128 + 16 * w) * (DM * 2));
  u64 epoch = rd_epoch(ws, rt);
  u32x4 xr[2][9];
  sfor<9>([&](auto tc) { constexpr int t = decltype(tc)::value;
    xr[0][t] = gload<t * 128>(vox, srdx, sox); xr[1][t] = gload<t * 128>(vox, srdx, sox + 8 * DM * 2); });
  const unsigned swz = 16u * (c8 ^ (2 * ((lane >> 4) & 3)));
  const unsigned vo1 = (8 * (i8 >> 2) + (i8 & 3)) * (DFF * 2) + swz, vo2 = i8 * (DM * 2) + swz;
  unsigned so1[3];
#pragma unroll
  for (int i = 0; i < 3; i++) { const int id = 3 * w + i, rg = id & 3; so1[i] = (unsigned)((16 * (rg & 1) + 4 * (rg >> 1)) * (DFF * 2) + (sp * 384 + (id >> 2) * 64) * 2); }
  const unsigned so2 = (unsigned)((sp * 384 + 16 * w) * (DM * 2));
  const int grp = sp >> 2, rr = sp & 3;
  int chunk[9];
#pragma unroll
  for (int pp = 0; pp < 9; pp++)
    chunk[pp] = pp < 6 ? 3 * ((grp + 1 + pp / 3) % 3) + (pp + rr) % 3 : 3 * grp + ((0x918486u >> (6 * rr + 2 * (pp - 6))) & 3);
  const unsigned m1 = (unsigned)(3 * w * 1024), m2 = (unsigned)(2 * w * 1024);
  auto load1 = [&](auto Sc, auto Ic) {
    constexpr int S = decltype(Sc)::value, I = decltype(Ic)::value;
    dma(w1_slot(S) + m1 + I * 1024, vo1, srd1, so1[I] + (unsigned)(S * 32 * DFF * 2));
  };
  auto load2 = [&](auto Jc, auto Ic) {
    constexpr int J = decltype(Jc)::value, I = decltype(Ic)::value;
    dma(w2_slot(J) + m2 + I * 1024, vo2, srd2, so2 + (unsigned)(((J % 3) * 128 + 8 * I) * (DM * 2)) + (unsigned)chunk[J / 3] * 128);
  };
  sfor<3>([&](auto i) { load1(IC<0>{}, i); });
  float rs[2], nm[2];
  {
    Stat st[2] = {};
    sfor<18>([&](auto ic) { constexpr int i = decltype(ic)::value;
      waitv_tie<3 + (i > 5 ? 3 : 0) + (i > 11 ? 3 : 0) + 17 - i>(xr[i & 1][i >> 1]); stat_acc(xr[i & 1][i >> 1], st[i & 1]); GAP();
      if constexpr (i == 5) sfor<3>([&](auto k) { load1(IC<1>{}, k); });
      if constexpr (i == 11) { sfor<3>([&](auto k) { load1(IC<2>{}, k); }); } });
    {
      const int jj = (w * 36 >> 3) + i8;
      touch(F_DUMMY, (c8 < 6 && lane < 40 && jj < 36) ? (unsigned)((jj * 16 + rt) * (DFF * 2) + c8 * 128) : OOB, srd1, (unsigned)(sp * 768));
      touch(F_DUMMY, lane < 27 ? lane * 128u : OOB, srd2, (unsigned)((sp * 384 + rt * 24 + w * 3) * (DM * 2)));
    }
    stat_fin<false>(st[0], lane, rs[0], nm[0]); stat_fin<false>(st[1], lane, rs[1], nm[1]);
    epoch = epoch_uniform(epoch);
  }
  const unsigned xw = (c8 >> 2) * 8192 + w * 1024 + (16 * (c8 & 3) + (i8 ^ c8)) * 16;
  NormSt ns;
  auto xop = [&](auto Tc, auto Oc) {
    constexpr int T = decltype(Tc)::value, O = decltype(Oc)::value;
    if constexpr (T < 9 && O < 42) norm_op<O>(ns, xr[0][T < 9 ? T : 0], xr[1][T < 9 ? T : 0], rs, nm, xw + (T & 1) * 16384);
  };
  sfor<2>([&](auto r) { constexpr int R = decltype(r)::value;
    lds_wr128(xw + R * 128, norm8(xr[R][0], rs[R], nm[R])); });
  sfor<12>([&](auto o) { xop(IC<1>{}, o); });

  const int row = 4 * g + q;
  const unsigned wb = row * 128 + 32 * (cg ^ ((row >> 1) & 3)) + 8 * p;
  unsigned wbh = wb + 65536, wbh2 = wb + 131072;
  pin(wbh); pin(wbh2);
  auto waddr = [&](auto Oc) { constexpr unsigned O = decltype(Oc)::value; return O < 65536 ? wb + O : (O < 131072 ? wbh + (O - 65536) : wbh2 + (O - 131072)); };
  const unsigned xb0 = tg * 4096 + (16 * g + ((lane & 15) ^ g)) * 16, xb1 = xb0 ^ 64;

  const __amdgpu_buffer_rsrc_t prs = __builtin_amdgcn_make_buffer_rsrc(ws + PART_OFF, 0, PART_BYTES, 0x00020000);
  const unsigned pso = (unsigned)(((rt * 9) * 16 + sp) * 16384 + w * 2048);
  const u64 tag = next_tag(epoch);
  unsigned poff[3];
#pragma unroll
  for (int i = 0; i < 3; i++) {
    const int sg = 6 * w + 2 * i, pc = 3 * sp + (sg >> 4);
    poff[i] = (unsigned)((rt * 9 + (pc >> 2)) * (16 * 16384) + ((pc >> 1) & 1) * 8192 + ((sg >> 2) & 3) * 2048 + (pc & 1) * 1024 + (sg & 3) * 256);
    asm volatile("" : "+s"(poff[i]));
  }
  const unsigned pvo = g * 16384 + (lane & 15) * 16;
  f32x4 acc[6][4];
  bf16x8 xf[2][4];
  bf16x4 wlo[4], whi[4];
  auto rdw1 = [&](auto GIc, auto Hc) {
    constexpr int GI = decltype(GIc)::value, H = decltype(Hc)::value;
    constexpr int O = w1_slot(GI / 6) + (GI % 6) * 4096 + H * 2048;
    if constexpr (H == 0) wlo[GI & 3] = lds_tr(waddr(IC<O>{})); else whi[GI & 3] = lds_tr(waddr(IC<O>{}));
  };
  auto rdx = [&](auto Sc, auto Tc) {
    constexpr int S = decltype(Sc)::value, T = decltype(Tc)::value;
    xf[S & 1][T] = lds_rd128(((S & 1) ? xb1 : xb0) + (S & 3) * 8192 + T * 1024);
  };
  waitv<vm_allowed(-1, 0)>(); waitl0(); bar();
  STAMP(1);
  rdw1(IC<0>{}, IC<0>{}); rdw1(IC<0>{}, IC<1>{});
  sfor<4>([&](auto t) { rdx(IC<0>{}, t); });
  rdw1(IC<1>{}, IC<0>{}); rdw1(IC<1>{}, IC<1>{}); rdw1(IC<2>{}, IC<0>{}); rdw1(IC<2>{}, IC<1>{});
  const unsigned hw = lane * 16 + (tg * 16 + cg) * 1024;
  unsigned hwh = hw + 65536; pin(hwh);
  auto hpack = [&](auto Uc, auto Tc) {
    constexpr int U = decltype(Uc)::value, T = decltype(Tc)::value;
    u32x4 o;
    o[0] = relupk(cvtpk(acc[2 * U][T][0], acc[2 * U][T][1])); o[1] = relupk(cvtpk(acc[2 * U][T][2], acc[2 * U][T][3]));
    o[2] = relupk(cvtpk(acc[2 * U + 1][T][0], acc[2 * U + 1][T][1])); o[3] = relupk(cvtpk(acc[2 * U + 1][T][2], acc[2 * U + 1][T][3]));
    lds_wr128((U == 2 ? hwh : hw + U * 32768) + T * 4096, o);
  };
  GAP();
  sfor<108>([&](auto GIc) {
    constexpr int GI = decltype(GIc)::value, S = GI / 6, SB = GI % 6;
    const bf16x8 a = cat(wlo[GI & 3], whi[GI & 3]);
    sfor<4>([&](auto Tc) {
      constexpr int T = decltype(Tc)::value;
      if constexpr (S == 0) mma0(acc[SB][T], a, xf[0][T]); else mma(acc[SB][T], a, xf[S & 1][T]);
      GAP();
      if constexpr (SB == 3 && T == 0) {
        if constexpr (S < 17) { waitv<vm_allowed(S, S + 1)>(); if constexpr ((S & 1) == 1) asm volatile("s_waitcnt lgkmcnt(4)" ::: "memory");
          bar(); }
        else { waitl0(); bar(); }
      }
      {
        constexpr int GP = SB * 4 + T;
        if constexpr ((S & 1) == 1 && SB >= 3) xop(IC<(S + 3) / 2>{}, IC<GP - 12>{});
        else if constexpr ((S & 1) == 0) { xop(IC<S / 2 + 1>{}, IC<GP + 12 + (GP > 8) + (GP > 16)>{}); if constexpr (GP == 8 || GP == 16) xop(IC<S / 2 + 1>{}, IC<GP + 13 + (GP > 8)>{}); }
        else if constexpr (GP < 4) xop(IC<(S + 1) / 2>{}, IC<GP + 38>{});
      }
      if constexpr (SB == 2 && T == 3 && S >= 3 && S < 15) {
        constexpr int PI = (S >= 3 && S < 15) ? S - 3 : 0;
        __builtin_amdgcn_raw_buffer_store_b128((u32x4){POISON, POISON, POISON, POISON}, prs, pvo, poff[(PI % 6) >> 1] + (PI & 1) * 256 + (PI < 6 ? grp : 3) * 65536, ST_AUX);
      }
      if constexpr (T < 2 && GI + 3 < 108) rdw1(IC<GI + 3>{}, Tc);
      if constexpr (T == 2 && SB < 3) {
        if constexpr (S <= 14) load1(IC<S + 3>{}, IC<SB % 3>{});
        else if constexpr (S == 15) { load2(IC<(SB >> 1)>{}, IC<(SB & 1)>{}); if constexpr (SB == 2) load2(IC<1>{}, IC<1>{}); }
        else if constexpr (SB < 2) load2(IC<S - 14>{}, IC<SB>{});
      }
      if constexpr (S < 17 && T >= 2 && (SB == 3 || SB == 4)) rdx(IC<S + 1>{}, IC<(SB - 3) * 2 + (T - 2)>{});
      if constexpr (S == 17 && (SB == 3 || SB == 5)) hpack(IC<(SB - 3) / 2>{}, Tc);
      GAP();
    });
  });
  STAMP(2);
  nop<7>(); GAP();
  sfor<4>([&](auto t) { hpack(IC<2>{}, t); });
  waitv<vm_allowed(18, 100)>(); waitl0(); bar();
  if (tid == 0) __hip_atomic_store((gu64*)(ws + ACK_OFF) + rt * 16 + sp, tag, RLX_AGENT);
  STAMP(3);

  bf16x8 hf[4][12];
  f32x4 oc[4];
  const unsigned hb = lane * 16 + tg * 16384; unsigned hbh = hb + 65536; pin(hbh);
  auto rdh = [&](auto Ic, auto Tc) {
    constexpr int I = decltype(Ic)::value, T = decltype(Tc)::value; constexpr int U = I >> 2;
    hf[T][I] = lds_rd128((U == 2 ? hbh : hb + U * 32768) + T * 4096 + (I & 3) * 1024);
  };
  auto rdw2 = [&](auto GIc, auto Hc) {
    constexpr int GI = decltype(GIc)::value, H = decltype(Hc)::value;
    constexpr int O = w2_slot(GI >> 2) + (GI & 3) * 2048 + H * 8192;
    if constexpr (H == 0) wlo[GI & 3] = lds_tr(waddr(IC<O>{})); else whi[GI & 3] = lds_tr(waddr(IC<O>{}));
  };
  rdw2(IC<0>{}, IC<0>{}); rdw2(IC<0>{}, IC<1>{});
  sfor<4>([&](auto t) { rdh(IC<0>{}, t); });
  rdw2(IC<1>{}, IC<0>{}); rdw2(IC<1>{}, IC<1>{});
  sfor<4>([&](auto t) { rdh(IC<1>{}, t); });
  rdw2(IC<2>{}, IC<0>{}); rdw2(IC<2>{}, IC<1>{});
  unsigned pk[8];
  const u32x4 srdp = make_srd(ws + PART_OFF, PART_BYTES);
  const int c16 = lane & 15;
  const gu64* fp = lane < 32 ? (const gu64*)(ws + FLAG_OFF) + (rt * 9 + ((lane & 16) ? (3 * sp + 2) >> 2 : (3 * sp) >> 2)) * 16 + c16 : (const gu64*)(ws + ACK_OFF) + rt * 16 + c16;
  const int gsrc[2] = {(grp + 2) % 3, (grp + 1) % 3};
  u64 fl; bool pre0, pre1; unsigned pso2;
  auto inbox = [&](auto Ac, auto Ic, unsigned so) {
    constexpr int A = decltype(Ac)::value, I = decltype(Ic)::value;
    if constexpr (I >= 0 && I < 6) dma_sc1(inbox_at(A, I) + m2, pvo, srdp, so + poff[I / 2] + (I & 1) * 256);
  };
  auto own_idx = [&](int pp, int half) { return (unsigned)(4 * chunk[pp] + 2 * tg + half - 3 * sp); };
  const unsigned own_w = F_OWN + cg * 1024 + lane * 16;
  auto ready = [&](u64 f, int gs) { return __all(f == tag || (c16 >> 2) != gs || lane >= 32) != 0; };
  auto acked = [&](u64 f) { return __all(f == tag || (c16 >> 2) != grp || lane < 32) != 0; };
  int spin = 0;
  GAP();
  sfor<108>([&](auto GIc) {
    constexpr int GI = decltype(GIc)::value, J = GI >> 2, C = GI & 3, I = GI % 12, N = GI / 12;
    if constexpr (GI == 12) STAMP(4);
    if constexpr (GI == 36) STAMP(5);
    const bf16x8 a = cat(wlo[GI & 3], whi[GI & 3]);
    sfor<4>([&](auto Tc) {
      constexpr int T = decltype(Tc)::value;
      if constexpr (I == 0 && N > 0) { if constexpr (T == 0) { nop<2>(); GAP(); } pk[2 * T] = cvtpk(oc[T][0], oc[T][1]); pk[2 * T + 1] = cvtpk(oc[T][2], oc[T][3]); GAP(); }
      if constexpr (I == 0) mma0(oc[T], a, hf[T][I]); else mma(oc[T], a, hf[T][I]);
      GAP();
      if constexpr (I == 0 && N > 6) { pk[2 * T] = nopoison(pk[2 * T]); pk[2 * T + 1] = nopoison(pk[2 * T + 1]); }
      if constexpr (C == 1 && T == 0 && J < 26) {
        waitv<p2_wait(J)>();
        bar();
        if constexpr (J >= 6 && J % 3 == 0 && J <= LASTFLAG) { if (tid == 0) set_flag(ws, rt, chunk[J >= 6 ? J / 3 - 2 : 0], sp, tag); }
      }
      if constexpr (C == 1 && T == 1 && J == USE1) {
        waitv_tie<vm_allowed(19 + J, 301)>(fl);
        pre0 = ready(fl, gsrc[0]);
        if (!acked(fl)) {
          for (; spin < 200000; spin++) { __builtin_amdgcn_s_sleep(1); u64 f2 = poll_load(fp); waitv_tie<0>(f2); if (acked(f2)) break; }
        }
        pso2 = pre0 ? (unsigned)gsrc[0] * 65536u : 0x80000000u;
      }
      if constexpr (C == 0 && T == 0 && (J == POLL1 || J == POLL2)) fl = poll_load(fp);
      if constexpr (GI + 3 < 108 && T < 2) rdw2(IC<GI + 3>{}, Tc);
      if constexpr (GI + 2 < 12) rdh(IC<GI + 2>{}, Tc);
      if constexpr (I == 0 && N > 0 && (T & 1)) {
        unsigned so = pso + (unsigned)chunk[N > 0 ? N - 1 : 0] * (16 * 16384);
        if constexpr (N == 8) so = own_idx(7, T >> 1) < 3u ? 0x80000000u : so;
        __builtin_amdgcn_raw_buffer_store_b128((u32x4){pk[2 * T - 2], pk[2 * T - 1], pk[2 * T], pk[2 * T + 1]}, prs, lane * 16 + (T >> 1) * 1024, so, ST_AUX);
      }
      if constexpr (I == 1 && N == 8 && T >= 2) {
        const unsigned oi = own_idx(7, T - 2);
        if (oi < 3u) lds_wr128(own_w + oi * 4096, (u32x4){pk[4 * T - 8], pk[4 * T - 7], pk[4 * T - 6], pk[4 * T - 5]});
      }
      if constexpr (C >= 2 && J < 26) {
        constexpr int cnt = p2_count(J + 1);
        if constexpr (cnt == 1) { if constexpr (T == 2) load2(IC<p2_first(J + 1)>{}, IC<C - 2>{}); }
        if constexpr (cnt == 2) { if constexpr (T >= 2) load2(IC<p2_first(J + 1) + C - 2>{}, IC<T - 2>{}); }
      }
      if constexpr (C >= 2 && J >= USE1 && J < USE1 + 3 && T == 2) inbox(IC<0>{}, IC<2 * (J - USE1) + C - 2>{}, pso2);
      GAP();
    });
  });
  nop<7>(); GAP();
  sfor<2>([&](auto hc) {
    constexpr int H = decltype(hc)::value;
    u32x4 o = {nopoison(cvtpk(oc[2 * H][0], oc[2 * H][1])), nopoison(cvtpk(oc[2 * H][2], oc[2 * H][3])), nopoison(cvtpk(oc[2 * H + 1][0], oc[2 * H + 1][1])), nopoison(cvtpk(oc[2 * H + 1][2], oc[2 * H + 1][3]))};
    const unsigned oi = own_idx(8, H);
    __builtin_amdgcn_raw_buffer_store_b128(o, prs, lane * 16 + H * 1024, oi < 3u ? 0x80000000u : pso + (unsigned)chunk[8] * (16 * 16384), ST_AUX);
    if (oi < 3u) lds_wr128(own_w + oi * 4096, o);
  });
  STAMP(6);
  waitv_tie<2>(fl);
  pre1 = ready(fl, gsrc[1]);
  STAMP(11);
  if (!pre0) STAMP(12);
  if (!pre1) STAMP(13);
  sfor<6>([&](auto i) { inbox(IC<1>{}, i, pre1 ? (unsigned)gsrc[1] * 65536u : 0x80000000u); });
  unsigned ooff[3]; u32x2 xv[3];
#pragma unroll
  for (int i = 0; i < 3; i++) {
    const int sg = 6 * w + 2 * i, pc = 3 * sp + (sg >> 4);
    ooff[i] = (unsigned)((rt * 128 + (pc & 3) * 32 + (g & 1) * 16 + c16) * DM + (pc >> 2) * 64 + (sg & 15) * 4 + (g >> 1) * 4);
    xv[i] = *(const u32x2*)(x + ooff[i]);
  }
  u32x4 sel;
#pragma unroll
  for (int j = 0; j < 4; j++) sel[j] = ((c16 & 7) >> 1) == j ? ((c16 & 1) ? 0x3f800000u : 0x00003f80u) : 0u;
  const u32x4 z4 = {0u, 0u, 0u, 0u};
  const bf16x8 af = __builtin_bit_cast(bf16x8, c16 < 8 ? sel : z4), as = __builtin_bit_cast(bf16x8, c16 < 8 ? z4 : sel);
  f32x4 dd[6];
  unsigned ib = lane * 16 + w * 2048, ibh = ib + 65536, ibh2 = ib + 131072; pin(ibh); pin(ibh2);
  auto rdin = [&](auto Ac, auto Ic) { constexpr unsigned O = inbox_at(decltype(Ac)::value, decltype(Ic)::value);
    return lds_rd128(O < 65536 ? ib + O : (O < 131072 ? ibh + (O - 65536) : ibh2 + (O - 131072))); };
  sfor<6>([&](auto ic) { dd[decltype(ic)::value] = mfma(af, rdin(IC<0>{}, ic), (f32x4){0.f, 0.f, 0.f, 0.f}); });
  STAMP(14);
  waitl0(); bar();
  STAMP(15);
  {
    const bf16x8 af0 = __builtin_bit_cast(bf16x8, lane < 8 ? sel : z4);
    const unsigned orb = F_OWN + c16 * 16 + w * 1536;
    sfor<6>([&](auto ic) { constexpr int i = decltype(ic)::value; dd[i] = mfma(af0, lds_rd128(orb + i * 256), dd[i]); });
  }
  if (!(pre0 && pre1)) {
    for (; spin < 200000; spin++) {
      fl = poll_load(fp); waitv_tie<0>(fl);
      if (ready(fl, gsrc[0]) && ready(fl, gsrc[1])) break;
      __builtin_amdgcn_s_sleep(1);
    }
  }
  STAMP(7);
  auto ld6 = [&](u32x4 (&v)[6], unsigned vo, int gs) {
#pragma unroll
    for (int i = 0; i < 6; i++) v[i] = __builtin_amdgcn_raw_buffer_load_b128(prs, vo, poff[i >> 1] + (i & 1) * 256 + gs * 65536, 16);
  };
  u32x4 po[6], pa[6];
#pragma unroll
  for (int k = 0; k < 2; k++) {
    if (!(k ? pre1 : pre0)) {
      ld6(po, pvo, gsrc[k]);
#pragma unroll
      for (int i = 0; i < 6; i++) dd[i] = mfma(af, __builtin_bit_cast(bf16x8, po[i]), dd[i]);
    }
  }
  const unsigned pvo_o = g == rr ? OOB : pvo;
  bool oka = false, oko = false;
  for (; spin < 200000; spin++) {
    asm volatile("" ::: "memory");
    if (!oko) ld6(po, pvo_o, grp);
    if (!oka) ld6(pa, pvo, 3);
    oko = clean6(po); oka = clean6(pa);
    if (oka && oko) break;
    __builtin_amdgcn_s_sleep(TAIL_SLEEP);
  }
  STAMP(8);
  waitv<0>();
  sfor<6>([&](auto ic) { constexpr int i = decltype(ic)::value;
    dd[i] = mfma(af, rdin(IC<1>{}, ic), dd[i]); dd[i] = mfma(af, __builtin_bit_cast(bf16x8, po[i]), dd[i]); dd[i] = mfma(as, __builtin_bit_cast(bf16x8, pa[i]), dd[i]); });
  STAMP(9);
#pragma unroll
  for (int i = 0; i < 3; i++) {
    float o[4];
#pragma unroll
    for (int j = 0; j < 4; j++) {
      float fv = dd[2 * i][j], sv = dd[2 * i + 1][j];
      swap32(fv, sv);
      const float gt = fv * __builtin_amdgcn_rcpf(1.f + __builtin_amdgcn_exp2f(-1.4426950408889634f * fv));
      o[j] = ((j & 1) ? bfhi(xv[i][j >> 1]) : bflo(xv[i][j >> 1])) + sv * gt;
    }
    *(u32x2*)(out + ooff[i]) = (u32x2){cvtpk(o[0], o[1]), cvtpk(o[2], o[3])};
  }
  if (sp == 0 && tid == 0) __hip_atomic_store((gu64*)(ws + EPOCH_OFF) + rt * 16, tag, RLX_AGENT);
  waitv<0>();
  STAMP(10);
  STAMP_FLUSH(1);
  asm volatile(".Lkernel_end:");
}

constexpr unsigned A_SLOT = 27648, A_XR = 110592, A_HALO = 143360, A_DUMMY = 161792, A_WOH = 125952, A_IMG = 872, A_OB = 0, A_OS = 296;
constexpr unsigned wo_slot(int n) { return n < 2 ? A_WOH + 18432u * n : (n < 6 ? 38912u + 18432u * (n - 2) : (n < 8 ? 18432u * (n - 6) : A_WOH)); }
constexpr int a_touch(int s) { return s == -1 ? 3 : 0; }
constexpr int wo_allowed(int N, int need) {
  int after = 0;
  auto op = [&](int tag) { if (tag == need) after = 0; else after += 2; };
  for (int n = 0; n < 6; n++) op(n);
  for (int n = 0; n <= N; n++) {
    if (n >= 2) op(20 + n - 1);
    if (n == N) break;
    if (n == 0) { op(6); op(7); }
    if (n == 1) { op(8); op(20); }
  }
  return after;
}
constexpr int item_pos(int cg, int tg, int C) {
  if (cg == 3) { const int i = (C < 3 ? C : C - 1) + 3 * tg, j = i >= 6 ? i - 6 : i; return j == 2 ? 25 : (j == 5 ? 26 : 20 + j - (j > 2)); }
  if (C < 4) return 8 + 4 * cg + (C ^ (2 * tg));
  return (cg == 2 && C == 6) ? 24 : 3 * cg + C - 4;
}

constexpr u64 pos_table(int C) { u64 t = 0; for (int w = 0; w < 8; w++) t |= (u64)item_pos(w & 3, w >> 2, C) << (5 * w); return t; }
template <int C> DI unsigned pos_of(int w) { return (unsigned)(pos_table(C) >> (5 * w)) & 31u; }

struct WoLd { u32x4 srd; unsigned vo1, vo2, so, so2; int w; };
DI WoLd wo_setup(const u16* wo, int hg, int w, int lane) {
  const int i8 = lane >> 3, c8 = lane & 7; const unsigned swz = 16u * (c8 ^ (2 * ((lane >> 4) & 3)));
  return {make_srd(wo, DM * DM * 2), (8 * (i8 >> 2) + (i8 & 3)) * (DM * 2) + swz, i8 * (DM * 2) + swz,
          (unsigned)((hg * 144 + 32 * (w >> 1) + 4 * (w & 1)) * (DM * 2)), (unsigned)((hg * 144 + 128 + 8 * w) * (DM * 2)), w};
}
template <int N, int I> DI void wo_load(const WoLd& l) {
  if constexpr (I < 2) dma(wo_slot(N) + (2 * l.w + I) * 1024, l.vo1, l.srd, l.so + I * 16 * DM * 2 + N * 128);
  else if (l.w < 2) dma(wo_slot(N) + (16 + l.w) * 1024, l.vo2, l.srd, l.so2 + N * 128);
}

DI void att_tail(const WoLd& wl, char* ws, int rt, int hg, int w, u64 tag
  , unsigned long long (&st_)[16], unsigned long long (&sc_)[16]
) {
  int tid = threadIdx.x; pin(tid);
  const int lane = tid & 63, tg = w >> 2, cg = w & 3;
  const int g = lane >> 4, q = (lane & 15) >> 2, p = lane & 3, c16 = lane & 15;
  const f32x4 zero4 = {0.f, 0.f, 0.f, 0.f};
  bf16x4 wlo[4], whi[4];
  auto wimg = [&](unsigned a, const f32x4& v) { lds_wr64(a, (u32x2){cvtpk(v[0], v[1]), cvtpk(v[2], v[3])}); };
  const gu64* ap = (const gu64*)(ws + ACK_OFF) + rt * 16 + (c16 < 12 ? c16 : 0);
  u64 ack = poll_load(ap);
  auto loado = [&](auto Nc, auto Ic) { wo_load<decltype(Nc)::value, decltype(Ic)::value>(wl); };
  {
    const int hp = w >> 1, par = w & 1;
    const unsigned r0 = (2 * c16 + par + 16) * A_IMG + 72 * hp, rm = (unsigned)max(2 * c16 + par - 16, 0) * A_IMG + 72 * hp + 288;
    unsigned rq[3] = {r0 + 8 * g, r0 + 8 * g + 40, r0 + 32};
    unsigned rk[3] = {rq[0] + 288, rq[2] + 288, rq[1] + 288};
    unsigned rkm[3] = {rm + 8 * g, rm + 32, rm + 8 * g + 40};
    const int kr = 4 * g + q;
    unsigned rv = (2 * kr + par + 16) * A_IMG + 576 + 72 * hp + 8 * p, rv3 = rv + 96 * A_IMG;
    unsigned rvm = (unsigned)max(2 * kr + par - 16, 0) * A_IMG + 576 + 72 * hp + 8 * p;
    unsigned rq3[3], rk3[3];
#pragma unroll
    for (int i = 0; i < 3; i++) { rq3[i] = rq[i] + 96 * A_IMG; pin(rq[i]); pin(rq3[i]); rk3[i] = rk[i] + 96 * A_IMG; pin(rk[i]); pin(rk3[i]); pin(rkm[i]); }
    pin(rv); pin(rv3); pin(rvm);
    f32x4 biasA, biasA0, biasB; unsigned mA[2], mA0[2], mB[2];
#pragma unroll
    for (int j = 0; j < 4; j++) {
      const int kk = 4 * g + j; const bool va = kk >= c16 + 9, vb = (kk <= c16) && (kk + 7 >= c16);
      biasA[j] = va ? 0.f : -1e30f; biasB[j] = vb ? 0.f : -1e30f; biasA0[j] = (va && rt > 0) ? 0.f : -1e30f;
      const unsigned bm = (j & 1) ? 0xffff0000u : 0x0000ffffu;
      if ((j & 1) == 0) { mA[j >> 1] = 0; mB[j >> 1] = 0; }
      mA[j >> 1] |= va ? bm : 0u; mB[j >> 1] |= vb ? bm : 0u;
    }
    mA0[0] = rt > 0 ? mA[0] : 0u; mA0[1] = rt > 0 ? mA[1] : 0u;
    pin(biasA); pin(biasA0); pin(biasB); pin(mA[0]); pin(mA[1]); pin(mA0[0]); pin(mA0[1]); pin(mB[0]); pin(mB[1]);
    unsigned qmask = g == 0 ? 0xffffffffu : 0u; pin(qmask);
    unsigned ow0 = A_OB + (2 * c16 + par) * A_OS + 72 * hp, owa = ow0 + 8 * g, owb = owa + 40; pin(ow0); pin(owa); pin(owb);
    waitl0(); bar();
    STAMP(3);
    auto asb = [](u32x2 a, u32x2 b) { return __builtin_bit_cast(bf16x8, (u32x4){a[0], a[1], b[0], b[1]}); };
    typedef unsigned u32x8 __attribute__((ext_vector_type(8)));
    typedef unsigned u32x16 __attribute__((ext_vector_type(16)));
    u32x8 kb[5]; u32x16 vb[3]; u32x2 qm[4][2], q4[4];
    auto rdk = [&](auto Uc) { constexpr int U = decltype(Uc)::value;
      sfor<3>([&](auto jc) { constexpr int j = decltype(jc)::value;
        const u32x2 t = lds_rd64(U == 0 ? rkm[j] : (U < 4 ? rk[j] + (U > 0 ? U - 1 : 0) * 32 * A_IMG : rk3[j]));
        kb[U][2 * j] = t[0]; kb[U][2 * j + 1] = t[1]; });
      kb[U][6] = 0u; kb[U][7] = 0u; };
    rdk(IC<0>{});
    sfor<4>([&](auto Uc) { constexpr int U = decltype(Uc)::value; constexpr unsigned ro = U < 3 ? U * 32 * A_IMG : 0;
      q4[U] = lds_rd64((U < 3 ? rq[2] : rq3[2]) + ro);
      sfor<2>([&](auto Hc) { constexpr int H = decltype(Hc)::value; qm[U][H] = lds_rd64((U < 3 ? rq[H] : rq3[H]) + ro); });
      rdk(IC<U + 1>{});
      GAP(); });
    sfor<5>([&](auto Uc) { constexpr int U = decltype(Uc)::value;
      sfor<3>([&](auto Dc) { constexpr int D = decltype(Dc)::value; constexpr unsigned co = D == 0 ? 0 : (D == 1 ? 40 : 32);
        const u32x2 t = __builtin_bit_cast(u32x2, lds_tr((U == 0 ? rvm : (U < 4 ? rv + (U > 0 ? U - 1 : 0) * 32 * A_IMG : rv3)) + co));
        vb[D][2 * U] = t[0]; vb[D][2 * U + 1] = t[1]; }); });
    GAP();
    auto kfrag = [&](auto Uc, auto Hc) { constexpr int U = decltype(Uc)::value, O = 2 * decltype(Hc)::value;
      return __builtin_bit_cast(bf16x8, __builtin_shufflevector(kb[U], kb[U], O, O + 1, O + 2, O + 3)); };
    auto vfrag = [&](auto Uc, auto Dc) { constexpr int O = 2 * decltype(Uc)::value, D = decltype(Dc)::value;
      return __builtin_bit_cast(bf16x8, __builtin_shufflevector(vb[D], vb[D], O, O + 1, O + 2, O + 3)); };
    f32x4 sa[8], sb[8];
    sfor<8>([&](auto Ic) { constexpr int I = decltype(Ic)::value, U = I >> 1, H = I & 1;
      const bf16x8 qf = H ? asb((u32x2){0u, q4[U][1] & qmask}, qm[U][1]) : asb(qm[U][0], (u32x2){q4[U][0] & qmask, 0u});
      sa[I] = mfma(kfrag(IC<U>{}, IC<H>{}), qf, U == 0 ? biasA0 : biasA); sb[I] = mfma(kfrag(IC<U + 1>{}, IC<H>{}), qf, biasB); });
    GAP();
    float v[8][4], mx[8];
    sfor<8>([&](auto Ic) { constexpr int I = decltype(Ic)::value;
#pragma unroll
      for (int j = 0; j < 4; j++) v[I][j] = fmx(sa[I][j], sb[I][j]);
      mx[I] = fmx(fmx(fmx(v[I][0], v[I][1]), v[I][2]), v[I][3]); });
    constexpr float SC = 0.23570226039551584f * 1.4426950408889634f;
    sfor<2>([&](auto hc) { constexpr int B = 4 * decltype(hc)::value;
      unpack4(-SC * pack_max(mx[B], mx[B + 1], mx[B + 2], mx[B + 3]), mx[B], mx[B + 1], mx[B + 2], mx[B + 3]); });
    GAP();
    waitl0(); bar();
    STAMP(4);
    sfor<3>([&](auto i) { loado(IC<2>{}, i); }); sfor<3>([&](auto i) { loado(IC<3>{}, i); });
    GAP();
    float sm[8];
    sfor<8>([&](auto Ic) { constexpr int I = decltype(Ic)::value;
#pragma unroll
      for (int j = 0; j < 4; j++) v[I][j] = __builtin_amdgcn_exp2f(__builtin_fmaf(v[I][j], SC, mx[I]));
      sm[I] = (v[I][0] + v[I][1]) + (v[I][2] + v[I][3]); });
    sfor<2>([&](auto hc) { constexpr int B = 4 * decltype(hc)::value;
      unpack4(__builtin_amdgcn_rcpf(pack_sum(sm[B], sm[B + 1], sm[B + 2], sm[B + 3])), sm[B], sm[B + 1], sm[B + 2], sm[B + 3]); });
    GAP();
    STAMP(5);
    sfor<3>([&](auto i) { loado(IC<4>{}, i); }); sfor<3>([&](auto i) { loado(IC<5>{}, i); });
    GAP();
    bf16x8 pf[8];
    sfor<8>([&](auto Ic) { constexpr int I = decltype(Ic)::value, U = I >> 1;
      const unsigned p0 = cvtpk(v[I][0] * sm[I], v[I][1] * sm[I]), p1 = cvtpk(v[I][2] * sm[I], v[I][3] * sm[I]);
      pf[I] = __builtin_bit_cast(bf16x8, (u32x4){p0 & (U == 0 ? mA0[0] : mA[0]), p1 & (U == 0 ? mA0[1] : mA[1]), p0 & mB[0], p1 & mB[1]}); });
    GAP();
    f32x4 o0[8], o1[8];
    sfor<8>([&](auto Ic) { constexpr int I = decltype(Ic)::value; o0[I] = mfma(vfrag(IC<(I >> 1)>{}, IC<(I & 1)>{}), pf[I], zero4); });
    sfor<8>([&](auto Ic) { constexpr int I = decltype(Ic)::value; o1[I] = mfma(vfrag(IC<(I >> 1)>{}, IC<2>{}), pf[I], zero4); });
    GAP();
    sfor<8>([&](auto Ic) { constexpr int I = decltype(Ic)::value; wimg(((I & 1) ? owb : owa) + (I >> 1) * 32 * A_OS, o0[I]); });
    if (g == 0) {
      sfor<4>([&](auto Uc) { constexpr int U = decltype(Uc)::value;
        lds_wr64(ow0 + 32 + U * 32 * A_OS, (u32x2){cvtpk(o1[2 * U][0], o1[2 * U][1]), cvtpk(o1[2 * U + 1][2], o1[2 * U + 1][3])}); });
    }
  }
  const int row = 4 * g + q;
  const unsigned ow = row * 128 + 32 * (cg ^ ((row >> 1) & 3)) + 8 * p; unsigned owh = ow + 65536, owh2 = ow + A_WOH; pin(owh); pin(owh2);
  unsigned ob = A_OB + (tg * 64 + c16) * A_OS + 16 * g, ob4 = A_OB + (tg * 64 + c16) * A_OS + 256 + 8 * g; pin(ob); pin(ob4);
  waitv_tie<8>(ack);
  for (int spin = 0; spin < 200000 && !__all(ack == tag); spin++) { __builtin_amdgcn_s_sleep(1); ack = poll_load(ap); waitv_tie<0>(ack); }
  waitl0(); bar();
  STAMP(6);
  auto rdo = [&](auto GIc, auto Hc) {
    constexpr int GI = decltype(GIc)::value, H = decltype(Hc)::value, N = GI / 5, S = GI % 5;
    constexpr unsigned O = wo_slot(N) + S * 4096 + H * 2048;
    const unsigned a = O >= A_WOH ? owh2 + (O - A_WOH) : (O >= 65536 ? owh + (O - 65536) : ow + O);
    if constexpr (H == 0) wlo[GI & 3] = lds_tr(a); else if constexpr (S < 4) whi[GI & 3] = lds_tr(a);
  };
  bf16x8 of[4][4]; u32x2 of4[4];
  sfor<5>([&](auto Sc) { constexpr int S = decltype(Sc)::value;
    if constexpr (S < 3) { rdo(Sc, IC<0>{}); rdo(Sc, IC<1>{}); }
    sfor<4>([&](auto Tc) { constexpr int T = decltype(Tc)::value;
      if constexpr (S < 4) of[T][S & 3] = lds_rd128a8(ob + T * 16 * A_OS + 64 * S); else of4[T] = lds_rd64(ob4 + T * 16 * A_OS); });
    GAP(); });
  const __amdgpu_buffer_rsrc_t prs = __builtin_amdgcn_make_buffer_rsrc(ws + PART_OFF, 0, PART_BYTES, 0x00020000);
  const unsigned pso = (unsigned)(((rt * 9) * 16 + 12 + hg) * 16384 + w * 2048);
  f32x4 oc[4]; unsigned pk[8];
  GAP();
  sfor<45>([&](auto GIc) {
    constexpr int GI = decltype(GIc)::value, N = GI / 5, S = GI % 5;
    if constexpr (GI == 5) STAMP(7);
    if constexpr (GI == 15) STAMP(8);
    if constexpr (GI == 30) STAMP(9);
    const bf16x4 alo = wlo[GI & 3];
    bf16x8 a; if constexpr (S < 4) a = cat(alo, whi[GI & 3]);
    sfor<4>([&](auto Tc) {
      constexpr int T = decltype(Tc)::value;
      if constexpr (S == 0 && N > 0) { if constexpr (T == 0) { nop<2>(); GAP(); } pk[2 * T] = cvtpk(oc[T][0], oc[T][1]); pk[2 * T + 1] = cvtpk(oc[T][2], oc[T][3]); GAP(); }
      if constexpr (S == 0) mma0(oc[T], a, of[T][0]); else if constexpr (S < 4) mma(oc[T], a, of[T][S & 3]); else mma16(oc[T], alo, of4[T]);
      GAP();
      if constexpr (S == 0 && N > 0) { pk[2 * T] = nopoison(pk[2 * T]); pk[2 * T + 1] = nopoison(pk[2 * T + 1]); }
      if constexpr (S == 2 && T == 0 && (N == 0 || N == 1 || N == 4)) {
        if constexpr (N == 0) waitl0();
        waitv<wo_allowed(N, N == 0 ? 1 : (N == 1 ? 5 : 8))>(); bar();
      }
      if constexpr (GI + 3 < 45 && T < 2) rdo(IC<GI + 3>{}, Tc);
      if constexpr (N == 0 && S >= 3 && T > 0) loado(IC<3 + S>{}, IC<T - 1>{});
      if constexpr (N == 1 && S == 2 && T > 0) loado(IC<8>{}, IC<T - 1>{});
      if constexpr (S == (N == 1 ? 3 : 0) && N > 0 && (T & 1))
        __builtin_amdgcn_raw_buffer_store_b128((u32x4){pk[2 * T - 2], pk[2 * T - 1], pk[2 * T], pk[2 * T + 1]}, prs, lane * 16 + (T >> 1) * 1024, pso + (N - 1) * 16 * 16384, ST_AUX);
      GAP();
    });
  });
  nop<7>(); GAP();
  sfor<2>([&](auto hc) {
    constexpr int H = decltype(hc)::value;
    u32x4 o = {nopoison(cvtpk(oc[2 * H][0], oc[2 * H][1])), nopoison(cvtpk(oc[2 * H][2], oc[2 * H][3])), nopoison(cvtpk(oc[2 * H + 1][0], oc[2 * H + 1][1])), nopoison(cvtpk(oc[2 * H + 1][2], oc[2 * H + 1][3]))};
    __builtin_amdgcn_raw_buffer_store_b128(o, prs, lane * 16 + H * 1024, pso + 8 * 16 * 16384, ST_AUX);
  });
  STAMP(10);
  waitv<0>();
  STAMP(11); STAMP_FLUSH(1);
}

DI void att_path(const u16* x, const u16* wqkv, const u16* wo, char* ws, int rt, int hg) {
  const int tid = threadIdx.x, lane = tid & 63;
  const int w = __builtin_amdgcn_readfirstlane(tid >> 6), tg = w >> 2, cg = w & 3;
  const int g = lane >> 4, q = (lane & 15) >> 2, p = lane & 3, c16 = lane & 15;
  STAMP_DECL; STAMP(0);
  const u32x4 srdx = make_srd(x, XN_BYTES), srdw = make_srd(wqkv, DM * 3 * DM * 2), srdo = make_srd(wo, DM * DM * 2);
  const int i8 = lane >> 3, c8 = lane & 7, l32 = lane & 31;
  const unsigned vox = i8 * (DM * 2) + c8 * 16;
  const unsigned sox = (unsigned)((rt * 128 + 16 * w) * (DM * 2));
  u64 epoch = rd_epoch(ws, rt);
  u32x4 xr[2][9], xh[3];
  {
    const unsigned soh = (unsigned)((rt > 0 ? rt * 128 - 16 + 2 * w : 0) * (DM * 2)), voh = (lane >> 5) * (DM * 2) + l32 * 16;
    xh[0] = gload<0>(voh, srdx, soh); xh[1] = gload<512>(voh, srdx, soh); xh[2] = gload<1024>(l32 < 8 ? voh : OOB, srdx, soh);
  }
  sfor<9>([&](auto tc) { constexpr int t = decltype(tc)::value;
    xr[0][t] = gload<t * 128>(vox, srdx, sox); xr[1][t] = gload<t * 128>(vox, srdx, sox + 8 * DM * 2); });
  unsigned vw[4];
#pragma unroll
  for (int i = 0; i < 4; i++) {
    const unsigned n = (w + 8 * i) * 64 + lane, r = (n * 9710u) >> 19, c = n - 54 * r;
    const unsigned k = 8 * ((r >> 2) & 3) + 4 * (r >> 4) + (r & 3);
    const unsigned sec = (c * 57u) >> 10;
    vw[i] = k * 3456 + c * 16 + sec * (1152 - 288);
  }
  auto loadw = [&](auto Sc, auto Ic) {
    constexpr int S = decltype(Sc)::value, I = decltype(Ic)::value;
    if constexpr (S < 18) { if (I < 3 || w < 3) dma(
(S & 3) * A_SLOT + (w + 8 * I) * 1024, vw[I], srdw, (unsigned)(S * 32 * 3456 + hg * 288)); }
  };
  sfor<4>([&](auto i) { loadw(IC<0>{}, i); });
  unsigned wbs[7];
  auto img_off = [&](unsigned pos) { const unsigned left = 0u - (unsigned)(pos >= 24), odd = hg & 1;
    return ((288u * (pos >> 3) + 32u * ((pos & 7) + odd)) & ~left) | ((288u * (pos - 24) + 256u - 256u * odd) & left); };
  {
    const unsigned lr = (4 * g + q) * 864 + 8 * p;
    sfor<7>([&](auto Cc) { constexpr int C = decltype(Cc)::value;
      const unsigned pos = pos_of<C>(w);
      wbs[C] = lr + img_off(pos);
      pin(wbs[C]); });
  }
  const unsigned xs = (16 * g + (c16 ^ g)) * 16;
  unsigned xb0 = A_XR + tg * 4096 + xs, xb1 = xb0 ^ 64, hb0 = A_HALO + xs, hb1 = hb0 ^ 64; pin(xb0); pin(xb1); pin(hb0); pin(hb1);
  unsigned xw = A_XR + (c8 >> 2) * 8192 + (16 * (c8 & 3) + (i8 ^ c8)) * 16 + w * 1024; pin(xw);
  GAP();
  STAMP(12);
  {
    Stat st = {}; float r, nmr;
    sfor<3>([&](auto ic) { constexpr int i = decltype(ic)::value; waitv_tie<3 + 18 + 2 - i>(xh[i]); stat_acc(xh[i], st); });
    stat_fin<true>(st, lane, r, nmr);
    const unsigned hwa = A_HALO + (l32 >> 2) * 1024 + (16 * (l32 & 3) + ((2 * w + (lane >> 5)) ^ c8)) * 16;
    lds_wr128(hwa, norm8(xh[0], r, nmr)); lds_wr128(hwa + 8192, norm8(xh[1], r, nmr));
    if (l32 < 8) lds_wr128(hwa + 16384, norm8(xh[2], r, nmr));
  }
  GAP();
  float rs[2], nm[2];
  {
    Stat st[2] = {};
    sfor<18>([&](auto ic) { constexpr int i = decltype(ic)::value;
      waitv_tie<3 + (i > 7 ? 3 : 0) + 17 - i>(xr[i & 1][i >> 1]); stat_acc(xr[i & 1][i >> 1], st[i & 1]); GAP();
      if constexpr (i == 7) { sfor<4>([&](auto k) { loadw(IC<1>{}, k); }); } });
    {
      const int jj = (w * 36 >> 3) + g;
      touch(A_DUMMY, (p < 3 && q < 3 && jj < 36) ? (unsigned)((jj * 16 + rt) * 3456 + q * 1152 + (2 * hg + p) * 128) : OOB, srdw, 0u);
      touch(A_DUMMY, (w & 1) && c16 < 9 && g == 0 ? (unsigned)(((w * 36 >> 3) + 4) * 16 + rt) * 3456 + (c16 / 3) * 1152 + (2 * hg + c16 % 3) * 128 : OOB, srdw, 0u);
      touch(A_DUMMY, lane < 11 && w * 11 + lane < 81 ? lane * 128u : OOB, srdo, (unsigned)((hg * 144 + rt * 9) * (DM * 2) + w * 11 * 128));
    }
    stat_fin<false>(st[0], lane, rs[0], nm[0]); stat_fin<false>(st[1], lane, rs[1], nm[1]);
    epoch = epoch_uniform(epoch);
  }
  STAMP(13);
  NormSt ns;
  auto xop = [&](auto Tc, auto Oc) {
    constexpr int T = decltype(Tc)::value, O = decltype(Oc)::value;
    if constexpr (T < 9 && O < 42) norm_op<O>(ns, xr[0][T < 9 ? T : 0], xr[1][T < 9 ? T : 0], rs, nm, xw + (T & 1) * 16384);
  };
  sfor<2>([&](auto r) { constexpr int R = decltype(r)::value; lds_wr128(xw + R * 128, norm8(xr[R][0], rs[R], nm[R])); });
  lds_wr128(xw + 16384, norm8(xr[0][1], rs[0], nm[0]));

  const f32x4 zero4 = {0.f, 0.f, 0.f, 0.f};
  const u64 tag = next_tag(epoch);
  f32x4 acc[7][4], hac[3];
  sfor<4>([&](auto t) { acc[3][decltype(t)::value] = zero4; }); hac[2] = zero4;
  bf16x8 xf[4], xhf;
  bf16x4 wlo[8], whi[8];
  WoLd wl;
  auto rdw = [&](auto GIc, auto Hc) {
    constexpr int GI = decltype(GIc)::value, H = decltype(Hc)::value, S = GI / 7, C = GI % 7;
    if constexpr (GI < 126) {
      const unsigned a = wbs[C] + (S & 1) * A_SLOT + H * 16 * 864;
      if constexpr (H == 0) wlo[GI & 7] = lds_tr(a); else whi[GI & 7] = lds_tr(a);
      if constexpr (H == 1 && (S & 1) == 1) { wbs[C] += (S & 2) ? -2 * A_SLOT : 2 * A_SLOT; pin(wbs[C]); }
    }
  };
  auto rdx = [&](auto Sc, auto Tc) {
    constexpr int S = decltype(Sc)::value, T = decltype(Tc)::value;
    xf[T] = lds_rd128(((S & 1) ? xb1 : xb0) + (S & 3) * 8192 + T * 1024);
  };
  auto rdxh = [&](auto Sc) { constexpr int S = decltype(Sc)::value; xhf = lds_rd128(((S & 1) ? hb1 : hb0) + S * 1024); };
  unsigned ih, ibase;
  unsigned toff[7];
  auto tileoff = [&](int C) { return toff[C]; };
  auto wimg = [&](unsigned a, const f32x4& v) { lds_wr64(a, (u32x2){cvtpk(v[0], v[1]), cvtpk(v[2], v[3])}); };
  STAMP(14);
  waitv<6>(); waitl0(); bar();
  STAMP(1);
  rdw(IC<0>{}, IC<0>{}); rdw(IC<0>{}, IC<1>{}); rdw(IC<1>{}, IC<0>{}); rdw(IC<1>{}, IC<1>{});
  sfor<4>([&](auto t) { rdx(IC<0>{}, t); });
  rdxh(IC<0>{});
  nop<1>();
  GAP();
  sfor<18>([&](auto Sc) {
    constexpr int S = decltype(Sc)::value, G0 = 7 * S;
    auto fill = [&](auto GPc) {
      constexpr int GP = decltype(GPc)::value;
      GAP();
      if constexpr (GP == 21 && S < 17) {
        if constexpr (S < 16) waitv<3 + a_touch(S) + a_touch(S - 1)>(); else waitv<0>();
        bar();
      }
      if constexpr (GP == 12 && S == 17) { waitl0(); bar(); }
      if constexpr (S == 17 && GP == 10) { int tz = tid; pin(tz); wl = wo_setup(wo, hg, w, tz & 63); }
      if constexpr (S == 17 && GP == 11) { int tz = tid; pin(tz); ih = (tz & 15) * A_IMG + 8 * ((tz >> 4) & 3); ibase = ih + (16 + tg * 64) * A_IMG;
        sfor<7>([&](auto Cc) { constexpr int C = decltype(Cc)::value; toff[C] = img_off(pos_of<C>(w)); }); }
      if constexpr (S == 0) xop(IC<1>{}, IC<GP + 21>{});
      else if constexpr ((S & 1) == 0) xop(IC<S / 2 + 1>{}, IC<GP + 8>{});
      else if constexpr (GP >= 22) xop(IC<(S + 3) / 2>{}, IC<GP - 22>{});
      else if constexpr (GP < 4 && S > 1) xop(IC<(S + 1) / 2>{}, IC<38 + GP>{});
      if constexpr (S < 17) {
        if constexpr (GP < 4) rdw(IC<G0 + 2 + (GP >> 1)>{}, IC<(GP & 1)>{});
        if constexpr (GP >= 8 && GP < 12) rdw(IC<G0 + 4 + ((GP - 8) >> 1)>{}, IC<(GP & 1)>{});
        if constexpr (GP == 15 || GP == 16) rdw(IC<G0 + 6>{}, IC<GP - 15>{});
        if constexpr (GP == 21 || GP == 22) rdw(IC<G0 + 7>{}, IC<GP - 21>{});
        if constexpr (GP == 24 || GP == 26) rdw(IC<G0 + 8>{}, IC<(GP - 24) / 2>{});
        if constexpr (GP >= 23 && (GP & 1) == 1) rdx(IC<S + 1>{}, IC<(GP - 23) / 2>{});
        if constexpr (GP == 28) rdxh(IC<S + 1>{});
      } else {
        if constexpr (GP < 10) rdw(IC<G0 + 2 + (GP >> 1)>{}, IC<(GP & 1)>{});
        if constexpr (GP >= 12 && GP < 20) wimg(ibase + tileoff((GP - 12) & 1) + ((GP - 12) >> 1) * 16 * A_IMG, acc[(GP - 12) & 1][(GP - 12) >> 1]);
        if constexpr (GP == 13 || GP == 15) wimg(ih + tileoff((GP - 13) / 2), hac[(GP - 13) / 2]);
        if constexpr (GP >= 20 && GP < 24) wimg(ibase + tileoff(2) + (GP - 20) * 16 * A_IMG, acc[2][GP - 20]);
        if constexpr (GP >= 26) wimg(ibase + tileoff(4) + (GP - 26) * 16 * A_IMG, acc[4][GP - 26]);
        if constexpr (GP == 14 || GP == 17 || GP == 19) wo_load<0, (GP == 14 ? 0 : (GP == 17 ? 1 : 2))>(wl);
        if constexpr (GP == 21 || GP == 24 || GP == 25) wo_load<1, (GP == 21 ? 0 : (GP == 24 ? 1 : 2))>(wl);
      }
      if constexpr (GP == 4 || GP == 6 || GP == 12 || GP == 17) loadw(IC<S + 2>{}, IC<(GP == 4 ? 0 : (GP == 6 ? 1 : (GP == 12 ? 2 : 3)))>{});
      GAP();
    };
    auto mm = [&](f32x4& c, const bf16x8& a, const bf16x8& b) { if constexpr (S == 0) mma0(c, a, b); else mma(c, a, b); };
    const bf16x8 a0 = cat(wlo[G0 & 7], whi[G0 & 7]), a1 = cat(wlo[(G0 + 1) & 7], whi[(G0 + 1) & 7]);
    sfor<4>([&](auto Tc) { constexpr int T = decltype(Tc)::value;
      mm(acc[0][T], a0, xf[T]); fill(IC<2 * T>{}); mm(acc[1][T], a1, xf[T]); fill(IC<2 * T + 1>{}); });
    mm(hac[0], a0, xhf); fill(IC<8>{});
    mm(hac[1], a1, xhf); fill(IC<9>{});
    const bf16x8 a2 = cat(wlo[(G0 + 2) & 7], whi[(G0 + 2) & 7]);
    sfor<4>([&](auto Tc) { constexpr int T = decltype(Tc)::value; mm(acc[2][T], a2, xf[T]); fill(IC<10 + T>{}); });
    const bf16x8 a3 = cat(wlo[(G0 + 3) & 7], whi[(G0 + 3) & 7]);
    mma_alt3(acc[3][0], a3, xf[0], hac[2], a2, xhf, cg); fill(IC<14>{});
    sfor<3>([&](auto Tc) { constexpr int T = decltype(Tc)::value + 1; mma_not3(acc[3][T], a3, xf[T], cg); fill(IC<14 + T>{}); });
    const bf16x8 a4 = cat(wlo[(G0 + 4) & 7], whi[(G0 + 4) & 7]);
    sfor<4>([&](auto Tc) { constexpr int T = decltype(Tc)::value; mm(acc[4][T], a4, xf[T]); fill(IC<18 + T>{}); });
    const bf16x8 a5 = cat(wlo[(G0 + 5) & 7], whi[(G0 + 5) & 7]), a6 = cat(wlo[(G0 + 6) & 7], whi[(G0 + 6) & 7]);
    sfor<4>([&](auto Tc) { constexpr int T = decltype(Tc)::value;
      mm(acc[5][T], a5, xf[T]); fill(IC<22 + 2 * T>{}); mm(acc[6][T], a6, xf[T]); fill(IC<23 + 2 * T>{}); });
  });
  STAMP(2);
  {
    if (cg < 3) { sfor<4>([&](auto Tc) { constexpr int T = decltype(Tc)::value; wimg(ibase + tileoff(3) + T * 16 * A_IMG, acc[3][T]); }); }
    else wimg(ih + tileoff(2), hac[2]);
    sfor<4>([&](auto Tc) { constexpr int T = decltype(Tc)::value;
      wimg(ibase + tileoff(5) + T * 16 * A_IMG, acc[5][T]); wimg(ibase + tileoff(6) + T * 16 * A_IMG, acc[6][T]); });
  }
  GAP();
  att_tail(wl, ws, rt, hg, w, tag
    , st_, sc_
  );
}

__global__ __launch_bounds__(512, 2) void k1_main(const u16* __restrict__ x, const u16* __restrict__ wqkv, const u16* __restrict__ wo,
                                                  const u16* __restrict__ w1, const u16* __restrict__ w2, char* __restrict__ ws, u16* __restrict__ out) {
  const int b = blockIdx.x, xc = b & 7, i = b >> 3;
  const int rt = 4 * (xc >> 1) + (i & 3), j = i >> 2;
  { const int ln = threadIdx.x & 63, wv = threadIdx.x >> 6;
    touch(163584u, (ln < 2) ? (unsigned)((i * 16 + wv * 2 + ln) * 128) : OOB, code_srd(), 0u); }
  if (j < 6) ffn_path(x, w1, w2, ws, out, rt, 6 * (xc & 1) + j);
  else att_path(x, wqkv, wo, ws, rt, 2 * (xc & 1) + (j - 6));
}

extern "C" void kernel_launch(void* const* d_in, const int* in_sizes, int n_in, void* d_out, int out_size, void* d_ws, size_t ws_size, hipStream_t stream) {
  const u16* x = (const u16*)d_in[0]; const u16* wqkv = (const u16*)d_in[1]; const u16* wo = (const u16*)d_in[2];
  const u16* w1 = (const u16*)d_in[4]; const u16* w2 = (const u16*)d_in[5];
  (void)hipFuncSetAttribute((const void*)k1_main, hipFuncAttributeMaxDynamicSharedMemorySize, 163840);
  k1_main<<<256, 512, 163840, stream>>>(x, wqkv, wo, w1, w2, (char*)d_ws, (u16*)d_out);
}
```

```cpp
#include <hip/hip_runtime.h>
#include <utility>

typedef unsigned short u16;
typedef __bf16 bf16x2 __attribute__((ext_vector_type(2)));
typedef __bf16 bf16x4 __attribute__((ext_vector_type(4)));
typedef __bf16 bf16x8 __attribute__((ext_vector_type(8)));
typedef short s16x2 __attribute__((ext_vector_type(2)));
typedef float f32x2 __attribute__((ext_vector_type(2)));
typedef float f32x4 __attribute__((ext_vector_type(4)));
typedef unsigned u32x2 __attribute__((ext_vector_type(2)));
typedef unsigned u32x4 __attribute__((ext_vector_type(4)));
#define LDSP(T) __attribute__((address_space(3))) T*
#define DI __device__ __forceinline__
#define GAP() __builtin_amdgcn_sched_barrier(0)
template <int V> using IC = std::integral_constant<int, V>;
template <class F, int... Is> DI void sfor_(F&& f, std::integer_sequence<int, Is...>) { (f(IC<Is>{}), ...); }
template <int N, class F> DI void sfor(F&& f) { sfor_(f, std::make_integer_sequence<int, N>{}); }

constexpr int SEQ = 2048, DM = 576, DFF = 4608;
constexpr unsigned XN_BYTES = SEQ * DM * 2;
constexpr size_t PART_OFF = 0;
constexpr unsigned PART_BYTES = 16u * XN_BYTES;
constexpr size_t FLAG_OFF = PART_OFF + PART_BYTES;
constexpr size_t ACK_OFF = FLAG_OFF + 16 * 9 * 16 * 8;
constexpr size_t EPOCH_OFF = ACK_OFF + 16 * 16 * 8;
typedef unsigned long long u64;
typedef __attribute__((address_space(1))) u64 gu64;
#define RLX_AGENT __ATOMIC_RELAXED, __HIP_MEMORY_SCOPE_AGENT
constexpr int ST_AUX = 16;
constexpr int TAIL_SLEEP = 4;

DI unsigned cvtpk(float lo, float hi) { f32x2 v = {lo, hi}; return __builtin_bit_cast(unsigned, __builtin_convertvector(v, bf16x2)); }
DI unsigned relupk(unsigned p) { s16x2 z = {0, 0}; return __builtin_bit_cast(unsigned, __builtin_elementwise_max(__builtin_bit_cast(s16x2, p), z)); }
DI float bflo(unsigned p) { return __builtin_bit_cast(float, p << 16); }
DI float bfhi(unsigned p) { return __builtin_bit_cast(float, p & 0xffff0000u); }
DI bf16x4 lds_tr(unsigned addr) { return __builtin_amdgcn_ds_read_tr16_b64_v4bf16((LDSP(bf16x4))(size_t)addr); }
DI bf16x8 lds_rd128(unsigned addr) { return *(LDSP(bf16x8))(size_t)addr; }
typedef bf16x8 bf16x8a8 __attribute__((aligned(8)));
DI bf16x8 lds_rd128a8(unsigned addr) { return *(LDSP(bf16x8a8))(size_t)addr; }
DI u32x2 lds_rd64(unsigned addr) { return *(LDSP(u32x2))(size_t)addr; }
DI void lds_wr128(unsigned addr, u32x4 v) { *(LDSP(u32x4))(size_t)addr = v; }
DI void lds_wr64(unsigned addr, u32x2 v) { *(LDSP(u32x2))(size_t)addr = v; }
DI bf16x8 cat(bf16x4 a, bf16x4 b) { return __builtin_shufflevector(a, b, 0, 1, 2, 3, 4, 5, 6, 7); }
DI f32x4 mfma(bf16x8 a, bf16x8 b, f32x4 c) { return __builtin_amdgcn_mfma_f32_16x16x32_bf16(a, b, c, 0, 0, 0); }
DI void mma(f32x4& c, bf16x8 a, bf16x8 b) { asm volatile("v_mfma_f32_16x16x32_bf16 %0, %1, %2, %0" : "+v"(c) : "v"(a), "v"(b)); }
DI void mma0(f32x4& c, bf16x8 a, bf16x8 b) { asm volatile("v_mfma_f32_16x16x32_bf16 %0, %1, %2, 0" : "=&v"(c) : "v"(a), "v"(b)); }
DI void mma16(f32x4& c, bf16x4 a, u32x2 b) { asm volatile("v_mfma_f32_16x16x16_bf16 %0, %1, %2, %0" : "+v"(c) : "v"(a), "v"(b)); }
DI void mma_not3(f32x4& c, bf16x8 a, bf16x8 b, int cg) {
  asm volatile("s_cmp_eq_u32 %3, 3\n\ts_cbranch_scc1 .Le%=\n\tv_mfma_f32_16x16x32_bf16 %0, %1, %2, %0\n.Le%=:" : "+v"(c) : "v"(a), "v"(b), "s"(cg) : "scc");
}
DI void mma_alt3(f32x4& c, bf16x8 a, bf16x8 b, f32x4& h, bf16x8 ah, bf16x8 bh, int cg) {
  asm volatile("s_cmp_eq_u32 %6, 3\n\ts_cbranch_scc1 .Lh%=\n\tv_mfma_f32_16x16x32_bf16 %0, %2, %3, %0\n\ts_branch .Le%=\n"
               ".Lh%=:\n\tv_mfma_f32_16x16x32_bf16 %1, %4, %5, %1\n.Le%=:" : "+v"(c), "+v"(h) : "v"(a), "v"(b), "v"(ah), "v"(bh), "s"(cg) : "scc");
}
template <int T> DI void mark() {}
template <int T> DI void need() {}
template <int N> DI void nop() { asm volatile("s_nop %0" ::"i"(N)); }
template <int N> DI void waitv() { asm volatile("s_waitcnt vmcnt(%0)" ::"i"(N) : "memory"); }
DI void waitl0() { asm volatile("s_waitcnt lgkmcnt(0)" ::: "memory"); }
DI void bar() { asm volatile("s_barrier" ::: "memory"); }
DI u32x4 make_srd(const void* p, unsigned bytes) {
  size_t a = (size_t)p; u32x4 s;
  s[0] = __builtin_amdgcn_readfirstlane((unsigned)a); s[1] = __builtin_amdgcn_readfirstlane((unsigned)(a >> 32));
  s[2] = bytes; s[3] = 0x00020000u; return s;
}
DI void dma(unsigned lds_dst, unsigned voff, u32x4 srd, unsigned soff) {
  asm volatile("s_mov_b32 m0, %0\n\ts_nop 0\n\tbuffer_load_dwordx4 %1, %2, %3 offen lds" ::"s"(lds_dst), "v"(voff), "s"(srd), "s"(soff) : "memory");
}
DI void dma_sc1(unsigned lds_dst, unsigned voff, u32x4 srd, unsigned soff) {
  asm volatile("s_mov_b32 m0, %0\n\ts_nop 0\n\tbuffer_load_dwordx4 %1, %2, %3 offen sc1 lds" ::"s"(lds_dst), "v"(voff), "s"(srd), "s"(soff) : "memory");
}
template <int CTRL> DI float dppadd(float v) { return v + __builtin_bit_cast(float, __builtin_amdgcn_update_dpp(0, __builtin_bit_cast(int, v), CTRL, 0xF, 0xF, true)); }
DI float red8(float v) { v = dppadd<0xB1>(v); v = dppadd<0x4E>(v); return dppadd<0x141>(v); }
DI float fmx(float a, float b) { return __builtin_elementwise_maximum(a, b); }
DI void swap16(float& a, float& b) {
  auto r = __builtin_amdgcn_permlane16_swap(__builtin_bit_cast(unsigned, a), __builtin_bit_cast(unsigned, b), false, false);
  const unsigned r0 = r[0], r1 = r[1]; a = __builtin_bit_cast(float, r0); b = __builtin_bit_cast(float, r1);
}
DI void swap32(float& a, float& b) {
  auto r = __builtin_amdgcn_permlane32_swap(__builtin_bit_cast(unsigned, a), __builtin_bit_cast(unsigned, b), false, false);
  const unsigned r0 = r[0], r1 = r[1]; a = __builtin_bit_cast(float, r0); b = __builtin_bit_cast(float, r1);
}
template <class T> DI void pin(T& v) { asm volatile("" : "+v"(v)); }
DI float pack_max(float a, float b, float c, float d) { swap16(a, b); a = fmx(a, b); swap16(c, d); c = fmx(c, d); swap32(a, c); return fmx(a, c); }
DI float pack_sum(float a, float b, float c, float d) { swap16(a, b); a = a + b; swap16(c, d); c = c + d; swap32(a, c); return a + c; }
DI void unpack4(float x, float& a, float& b, float& c, float& d) { a = x; c = x; swap32(a, c); b = a; swap16(a, b); d = c; swap16(c, d); }
template <int OFF> DI u32x4 gload(unsigned voff, u32x4 srd, unsigned soff) {
  u32x4 r; asm volatile("buffer_load_dwordx4 %0, %1, %2, %3 offen offset:%4" : "=v"(r) : "v"(voff), "s"(srd), "s"(soff), "i"(OFF)); return r;
}
DI void touch(unsigned lds_dst, unsigned voff, u32x4 srd, unsigned soff) {
  asm volatile("s_mov_b32 m0, %0\n\ts_nop 0\n\tbuffer_load_dword %1, %2, %3 offen lds" ::"s"(lds_dst), "v"(voff), "s"(srd), "s"(soff) : "memory");
}
constexpr unsigned OOB = 0x7fffffffu;
DI u32x4 code_srd() {
  unsigned long long pc; unsigned len;
  asm volatile("s_getpc_b64 %0\n.Lcs%=:\n\ts_mov_b32 %1, .Lkernel_end-.Lcs%=\n\ts_max_i32 %1, %1, 0\n\ts_min_i32 %1, %1, 0x10000" : "=s"(pc), "=s"(len) :: "scc");
  u32x4 r; r[0] = (unsigned)pc; r[1] = (unsigned)(pc >> 32); r[2] = len; r[3] = 0x00020000u; return r;
}
DI u64 poll_load(const gu64* p) { u64 r; asm volatile("global_load_dwordx2 %0, %1, off sc1" : "=v"(r) : "v"(p) : "memory"); return r; }
template <int N> DI void waitv_tie(u64& r) { asm volatile("s_waitcnt vmcnt(%1)" : "+v"(r) : "i"(N)); }
template <int N> DI void waitv_tie(u32x4& r) { asm volatile("s_waitcnt vmcnt(%1)" : "+v"(r) : "i"(N)); }
constexpr unsigned POISON = 0xffffffffu;
DI unsigned nopoison(unsigned v) { return v < POISON - 1 ? v : POISON - 1; }
DI bool clean6(const u32x4 (&v)[6]) {
  unsigned m = 0;
#pragma unroll
  for (int i = 0; i < 6; i++) m = max(max(m, max(v[i][0], v[i][1])), max(v[i][2], v[i][3]));
  return __all(m != POISON) != 0;
}
DI u64 next_tag(u64 e) { return e + (((e ^ 0x9E3779B97F4A7C15ull) * 0xD1342543DE82EF95ull + 0x632BE59BD9B4E019ull) | 1ull); }
DI u64 rd_epoch(char* ws, int rt) {
  return poll_load((const gu64*)(ws + EPOCH_OFF) + rt * 16);
}
DI u64 epoch_uniform(u64 e) {
  asm volatile("" : "+v"(e));
  return ((u64)(unsigned)__builtin_amdgcn_readfirstlane((unsigned)(e >> 32)) << 32) | (unsigned)__builtin_amdgcn_readfirstlane((unsigned)e);
}
DI void set_flag(char* ws, int rt, int n, int src, u64 tag) {
  __hip_atomic_store((gu64*)(ws + FLAG_OFF) + (rt * 9 + n) * 16 + src, tag, RLX_AGENT);
}
typedef short s16x4 __attribute__((ext_vector_type(4)));
struct Stat { f32x4 s1, s2; };
DI void stat_acc(const u32x4& c, Stat& st) {
  const s16x4 ones = {0x3f80, 0x3f80, 0x3f80, 0x3f80};
  const s16x4 a = __builtin_bit_cast(s16x4, (u32x2){c[0], c[1]}), b = __builtin_bit_cast(s16x4, (u32x2){c[2], c[3]});
  st.s1 = __builtin_amdgcn_mfma_f32_4x4x4bf16_1k(a, ones, st.s1, 0, 0, 0);
  st.s2 = __builtin_amdgcn_mfma_f32_4x4x4bf16_1k(a, a, st.s2, 0, 0, 0);
  st.s1 = __builtin_amdgcn_mfma_f32_4x4x4bf16_1k(b, ones, st.s1, 0, 0, 0);
  st.s2 = __builtin_amdgcn_mfma_f32_4x4x4bf16_1k(b, b, st.s2, 0, 0, 0);
}
template <bool WIDE> DI void stat_fin(const Stat& st, int lane, float& r, float& nmr) {
  float s1 = dppadd<0x141>((st.s1[0] + st.s1[1]) + (st.s1[2] + st.s1[3]));
  const float d01 = (lane & 1) ? st.s2[1] : st.s2[0], d23 = (lane & 1) ? st.s2[3] : st.s2[2];
  float s2 = red8((lane & 2) ? d23 : d01);
  if constexpr (WIDE) {
    s1 = dppadd<0x140>(s1); s2 = dppadd<0x140>(s2);
    float t1 = s1, t2 = s2; swap16(s1, t1); swap16(s2, t2); s1 += t1; s2 += t2;
  }
  const float m = s1 * (1.f / DM);
  const float var = fmaxf(s2 * (1.f / DM) - m * m, 0.f);
  r = __builtin_amdgcn_rsqf(var + 1e-5f); nmr = -m * r;
}
struct NormSt { float lo, hi; u32x4 o; };
template <int O> DI void norm_op(NormSt& st, const u32x4& c0, const u32x4& c1, const float (&rs)[2], const float (&nm)[2], unsigned addr) {
  constexpr int R = O / 21, Q = O % 21, J = Q / 5, K = Q % 5;
  const u32x4& c = R ? c1 : c0;
  if constexpr (Q == 20) lds_wr128(addr + R * 128, st.o);
  else if constexpr (K == 0) { st.lo = bflo(c[J]); pin(st.lo); }
  else if constexpr (K == 1) { st.hi = bfhi(c[J]); pin(st.hi); }
  else if constexpr (K == 2) { st.lo = __builtin_fmaf(st.lo, rs[R], nm[R]); pin(st.lo); }
  else if constexpr (K == 3) { st.hi = __builtin_fmaf(st.hi, rs[R], nm[R]); pin(st.hi); }
  else { unsigned t = cvtpk(st.lo, st.hi); pin(t); st.o[J] = t; }
}
DI unsigned norm2(unsigned p, float r, float nmr) { return cvtpk(__builtin_fmaf(bflo(p), r, nmr), __builtin_fmaf(bfhi(p), r, nmr)); }
DI u32x4 norm8(const u32x4& c, float r, float nmr) { return (u32x4){norm2(c[0], r, nmr), norm2(c[1], r, nmr), norm2(c[2], r, nmr), norm2(c[3], r, nmr)}; }

constexpr int p2_first(int j) { return j < 2 ? 0 : (j <= 7 ? 2 * j : j + 8); }
constexpr int p2_count(int j) { return j < 2 ? 0 : (j <= 7 ? 2 : (j + 8 <= 26 ? 1 : 0)); }
constexpr int POLL1 = 16, USE1 = 19, POLL2 = 25, LASTFLAG = 21;
constexpr int inbox_count(int j) { return (j >= USE1 + 1 && j <= USE1 + 3) ? 2 : 0; }
constexpr int vm_allowed(int T, int need) {
  int after = 0;
  auto op = [&](int tag, int n) { if (tag == need) after = 0; else after += n; };
  op(0, 3); op(1, 3); op(2, 3); op(298, 2);
  if (T < 0) return after;
  for (int t = 0; t < 18; t++) {
    if (t <= 14) op(t + 3, 3);
    if (t >= 3 && t < 15) op(297, 1);
    if (t == 15) { op(100, 2); op(101, 2); }
    if (t == 16) op(102, 2);
    if (t == 17) op(103, 2);
    if (t == T) return after;
  }
  if (T == 18) return after;
  for (int j = 0; j < 27; j++) {
    for (int c = 0; c < p2_count(j); c++) op(100 + p2_first(j) + c, 2);
    op(300, inbox_count(j));
    if (j == POLL1) op(301, 1);
    if (j == POLL2) op(302, 1);
    if (j >= 3 && j % 3 == 0) op(200 + j / 3 - 1, 2);
    if (19 + j == T) return after;
  }
  return 0;
}
constexpr int imin(int a, int b) { return a < b ? a : b; }
constexpr int p2_wait(int j) {
  int a = j < 26 ? vm_allowed(19 + j, 100 + j + 1) : 63;
  if (j >= 6 && j % 3 == 0 && j <= LASTFLAG) a = imin(a, vm_allowed(19 + j, 200 + j / 3 - 2));
  return a;
}
constexpr unsigned w1_slot(int s) { constexpr int pi[5] = {2, 0, 1, 4, 3}; return 32768u + 24576u * pi[s % 5]; }
constexpr unsigned w2_slot(int j) { constexpr int pi[10] = {8, 9, 7, 6, 0, 1, 2, 3, 4, 5}; return 16384u * pi[j % 10]; }
constexpr unsigned inbox_at(int a, int i) { return w2_slot(17 + 3 * a + i / 2) + (i & 1) * 1024u; }
constexpr unsigned F_OWN = w2_slot(23);
constexpr unsigned F_DUMMY = 159744;

DI void ffn_path(const u16* x, const u16* w1, const u16* w2, char* ws, u16* out, int rt, int sp) {
  const int tid = threadIdx.x, lane = tid & 63;
  const int w = __builtin_amdgcn_readfirstlane(tid >> 6), tg = w >> 2, cg = w & 3;
  const int g = lane >> 4, q = (lane & 15) >> 2, p = lane & 3;
  const u32x4 srdx = make_srd(x, XN_BYTES), srd1 = make_srd(w1, DM * DFF * 2), srd2 = make_srd(w2, DM * DFF * 2);
  const int i8 = lane >> 3, c8 = lane & 7;
  const unsigned vox = i8 * (DM * 2) + c8 * 16;
  const unsigned sox = (unsigned)((rt * 128 + 16 * w) * (DM * 2));
  u64 epoch = rd_epoch(ws, rt);
  u32x4 xr[2][9];
  sfor<9>([&](auto tc) { constexpr int t = decltype(tc)::value;
    xr[0][t] = gload<t * 128>(vox, srdx, sox); mark<400 + 2 * t>(); xr[1][t] = gload<t * 128>(vox, srdx, sox + 8 * DM * 2); mark<401 + 2 * t>(); });
  const unsigned swz = 16u * (c8 ^ (2 * ((lane >> 4) & 3)));
  const unsigned vo1 = (8 * (i8 >> 2) + (i8 & 3)) * (DFF * 2) + swz, vo2 = i8 * (DM * 2) + swz;
  unsigned so1[3];
#pragma unroll
  for (int i = 0; i < 3; i++) { const int id = 3 * w + i, rg = id & 3; so1[i] = (unsigned)((16 * (rg & 1) + 4 * (rg >> 1)) * (DFF * 2) + (sp * 384 + (id >> 2) * 64) * 2); }
  const unsigned so2 = (unsigned)((sp * 384 + 16 * w) * (DM * 2));
  const int grp = sp >> 2, rr = sp & 3;
  int chunk[9];
#pragma unroll
  for (int pp = 0; pp < 9; pp++)
    chunk[pp] = pp < 6 ? 3 * ((grp + 1 + pp / 3) % 3) + (pp + rr) % 3 : 3 * grp + ((0x918486u >> (6 * rr + 2 * (pp - 6))) & 3);
  const unsigned m1 = (unsigned)(3 * w * 1024), m2 = (unsigned)(2 * w * 1024);
  auto load1 = [&](auto Sc, auto Ic) {
    constexpr int S = decltype(Sc)::value, I = decltype(Ic)::value;
    dma(w1_slot(S) + m1 + I * 1024, vo1, srd1, so1[I] + (unsigned)(S * 32 * DFF * 2)); mark<S>();
  };
  auto load2 = [&](auto Jc, auto Ic) {
    constexpr int J = decltype(Jc)::value, I = decltype(Ic)::value;
    dma(w2_slot(J) + m2 + I * 1024, vo2, srd2, so2 + (unsigned)(((J % 3) * 128 + 8 * I) * (DM * 2)) + (unsigned)chunk[J / 3] * 128); mark<100 + J>();
  };
  sfor<3>([&](auto i) { load1(IC<0>{}, i); });
  float rs[2], nm[2];
  {
    Stat st[2] = {};
    sfor<18>([&](auto ic) { constexpr int i = decltype(ic)::value;
      waitv_tie<3 + (i > 5 ? 3 : 0) + (i > 11 ? 3 : 0) + 17 - i>(xr[i & 1][i >> 1]); need<400 + i>(); stat_acc(xr[i & 1][i >> 1], st[i & 1]); GAP();
      if constexpr (i == 5) sfor<3>([&](auto k) { load1(IC<1>{}, k); });
      if constexpr (i == 11) { sfor<3>([&](auto k) { load1(IC<2>{}, k); }); } });
    {
      const int jj = (w * 36 >> 3) + i8;
      touch(F_DUMMY, (c8 < 6 && lane < 40 && jj < 36) ? (unsigned)((jj * 16 + rt) * (DFF * 2) + c8 * 128) : OOB, srd1, (unsigned)(sp * 768));
      touch(F_DUMMY, lane < 27 ? lane * 128u : OOB, srd2, (unsigned)((sp * 384 + rt * 24 + w * 3) * (DM * 2)));
    }
    stat_fin<false>(st[0], lane, rs[0], nm[0]); stat_fin<false>(st[1], lane, rs[1], nm[1]);
    epoch = epoch_uniform(epoch);
  }
  const unsigned xw = (c8 >> 2) * 8192 + w * 1024 + (16 * (c8 & 3) + (i8 ^ c8)) * 16;
  NormSt ns;
  auto xop = [&](auto Tc, auto Oc) {
    constexpr int T = decltype(Tc)::value, O = decltype(Oc)::value;
    if constexpr (T < 9 && O < 42) norm_op<O>(ns, xr[0][T < 9 ? T : 0], xr[1][T < 9 ? T : 0], rs, nm, xw + (T & 1) * 16384);
  };
  sfor<2>([&](auto r) { constexpr int R = decltype(r)::value;
    lds_wr128(xw + R * 128, norm8(xr[R][0], rs[R], nm[R])); });
  sfor<12>([&](auto o) { xop(IC<1>{}, o); });

  const int row = 4 * g + q;
  const unsigned wb = row * 128 + 32 * (cg ^ ((row >> 1) & 3)) + 8 * p;
  unsigned wbh = wb + 65536, wbh2 = wb + 131072;
  pin(wbh); pin(wbh2);
  auto waddr = [&](auto Oc) { constexpr unsigned O = decltype(Oc)::value; return O < 65536 ? wb + O : (O < 131072 ? wbh + (O - 65536) : wbh2 + (O - 131072)); };
  const unsigned xb0 = tg * 4096 + (16 * g + ((lane & 15) ^ g)) * 16, xb1 = xb0 ^ 64;

  const __amdgpu_buffer_rsrc_t prs = __builtin_amdgcn_make_buffer_rsrc(ws + PART_OFF, 0, PART_BYTES, 0x00020000);
  const unsigned pso = (unsigned)(((rt * 9) * 16 + sp) * 16384 + w * 2048);
  const u64 tag = next_tag(epoch);
  unsigned poff[3];
#pragma unroll
  for (int i = 0; i < 3; i++) {
    const int sg = 6 * w + 2 * i, pc = 3 * sp + (sg >> 4);
    poff[i] = (unsigned)((rt * 9 + (pc >> 2)) * (16 * 16384) + ((pc >> 1) & 1) * 8192 + ((sg >> 2) & 3) * 2048 + (pc & 1) * 1024 + (sg & 3) * 256);
    asm volatile("" : "+s"(poff[i]));
  }
  const unsigned pvo = g * 16384 + (lane & 15) * 16;
  f32x4 acc[6][4];
  bf16x8 xf[2][4];
  bf16x4 wlo[4], whi[4];
  auto rdw1 = [&](auto GIc, auto Hc) {
    constexpr int GI = decltype(GIc)::value, H = decltype(Hc)::value;
    constexpr int O = w1_slot(GI / 6) + (GI % 6) * 4096 + H * 2048;
    if constexpr (H == 0) wlo[GI & 3] = lds_tr(waddr(IC<O>{})); else whi[GI & 3] = lds_tr(waddr(IC<O>{}));
  };
  auto rdx = [&](auto Sc, auto Tc) {
    constexpr int S = decltype(Sc)::value, T = decltype(Tc)::value;
    xf[S & 1][T] = lds_rd128(((S & 1) ? xb1 : xb0) + (S & 3) * 8192 + T * 1024);
  };
  waitv<vm_allowed(-1, 0)>(); need<0>(); waitl0(); bar();
  rdw1(IC<0>{}, IC<0>{}); rdw1(IC<0>{}, IC<1>{});
  sfor<4>([&](auto t) { rdx(IC<0>{}, t); });
  rdw1(IC<1>{}, IC<0>{}); rdw1(IC<1>{}, IC<1>{}); rdw1(IC<2>{}, IC<0>{}); rdw1(IC<2>{}, IC<1>{});
  const unsigned hw = lane * 16 + (tg * 16 + cg) * 1024;
  unsigned hwh = hw + 65536; pin(hwh);
  auto hpack = [&](auto Uc, auto Tc) {
    constexpr int U = decltype(Uc)::value, T = decltype(Tc)::value;
    u32x4 o;
    o[0] = relupk(cvtpk(acc[2 * U][T][0], acc[2 * U][T][1])); o[1] = relupk(cvtpk(acc[2 * U][T][2], acc[2 * U][T][3]));
    o[2] = relupk(cvtpk(acc[2 * U + 1][T][0], acc[2 * U + 1][T][1])); o[3] = relupk(cvtpk(acc[2 * U + 1][T][2], acc[2 * U + 1][T][3]));
    lds_wr128((U == 2 ? hwh : hw + U * 32768) + T * 4096, o);
  };
  GAP();
  sfor<108>([&](auto GIc) {
    constexpr int GI = decltype(GIc)::value, S = GI / 6, SB = GI % 6;
    const bf16x8 a = cat(wlo[GI & 3], whi[GI & 3]);
    sfor<4>([&](auto Tc) {
      constexpr int T = decltype(Tc)::value;
      if constexpr (S == 0) mma0(acc[SB][T], a, xf[0][T]); else mma(acc[SB][T], a, xf[S & 1][T]);
      GAP();
      if constexpr (SB == 3 && T == 0) {
        if constexpr (S < 17) { waitv<vm_allowed(S, S + 1)>(); need<S + 1>(); if constexpr ((S & 1) == 1) asm volatile("s_waitcnt lgkmcnt(4)" ::: "memory");
          bar(); }
        else { waitl0(); bar(); }
      }
      {
        constexpr int GP = SB * 4 + T;
        if constexpr ((S & 1) == 1 && SB >= 3) xop(IC<(S + 3) / 2>{}, IC<GP - 12>{});
        else if constexpr ((S & 1) == 0) { xop(IC<S / 2 + 1>{}, IC<GP + 12 + (GP > 8) + (GP > 16)>{}); if constexpr (GP == 8 || GP == 16) xop(IC<S / 2 + 1>{}, IC<GP + 13 + (GP > 8)>{}); }
        else if constexpr (GP < 4) xop(IC<(S + 1) / 2>{}, IC<GP + 38>{});
      }
      if constexpr (SB == 2 && T == 3 && S >= 3 && S < 15) {
        constexpr int PI = (S >= 3 && S < 15) ? S - 3 : 0;
        __builtin_amdgcn_raw_buffer_store_b128((u32x4){POISON, POISON, POISON, POISON}, prs, pvo, poff[(PI % 6) >> 1] + (PI & 1) * 256 + (PI < 6 ? grp : 3) * 65536, ST_AUX); GAP(); mark<297>();
      }
      if constexpr (T < 2 && GI + 3 < 108) rdw1(IC<GI + 3>{}, Tc);
      if constexpr (T == 2 && SB < 3) {
        if constexpr (S <= 14) load1(IC<S + 3>{}, IC<SB % 3>{});
        else if constexpr (S == 15) { load2(IC<(SB >> 1)>{}, IC<(SB & 1)>{}); if constexpr (SB == 2) load2(IC<1>{}, IC<1>{}); }
        else if constexpr (SB < 2) load2(IC<S - 14>{}, IC<SB>{});
      }
      if constexpr (S < 17 && T >= 2 && (SB == 3 || SB == 4)) rdx(IC<S + 1>{}, IC<(SB - 3) * 2 + (T - 2)>{});
      if constexpr (S == 17 && (SB == 3 || SB == 5)) hpack(IC<(SB - 3) / 2>{}, Tc);
      GAP();
    });
  });
  nop<7>(); GAP();
  sfor<4>([&](auto t) { hpack(IC<2>{}, t); });
  waitv<vm_allowed(18, 100)>(); need<100>(); need<297>(); waitl0(); bar();
  if (tid == 0) __hip_atomic_store((gu64*)(ws + ACK_OFF) + rt * 16 + sp, tag, RLX_AGENT);
  bf16x8 hf[4][12];
  f32x4 oc[4];
  const unsigned hb = lane * 16 + tg * 16384; unsigned hbh = hb + 65536; pin(hbh);
  auto rdh = [&](auto Ic, auto Tc) {
    constexpr int I = decltype(Ic)::value, T = decltype(Tc)::value; constexpr int U = I >> 2;
    hf[T][I] = lds_rd128((U == 2 ? hbh : hb + U * 32768) + T * 4096 + (I & 3) * 1024);
  };
  auto rdw2 = [&](auto GIc, auto Hc) {
    constexpr int GI = decltype(GIc)::value, H = decltype(Hc)::value;
    constexpr int O = w2_slot(GI >> 2) + (GI & 3) * 2048 + H * 8192;
    if constexpr (H == 0) wlo[GI & 3] = lds_tr(waddr(IC<O>{})); else whi[GI & 3] = lds_tr(waddr(IC<O>{}));
  };
  rdw2(IC<0>{}, IC<0>{}); rdw2(IC<0>{}, IC<1>{});
  sfor<4>([&](auto t) { rdh(IC<0>{}, t); });
  rdw2(IC<1>{}, IC<0>{}); rdw2(IC<1>{}, IC<1>{});
  sfor<4>([&](auto t) { rdh(IC<1>{}, t); });
  rdw2(IC<2>{}, IC<0>{}); rdw2(IC<2>{}, IC<1>{});
  unsigned pk[8];
  const u32x4 srdp = make_srd(ws + PART_OFF, PART_BYTES);
  const int c16 = lane & 15;
  const gu64* fp = lane < 32 ? (const gu64*)(ws + FLAG_OFF) + (rt * 9 + ((lane & 16) ? (3 * sp + 2) >> 2 : (3 * sp) >> 2)) * 16 + c16 : (const gu64*)(ws + ACK_OFF) + rt * 16 + c16;
  const int gsrc[2] = {(grp + 2) % 3, (grp + 1) % 3};
  u64 fl; bool pre0, pre1; unsigned pso2;
  auto inbox = [&](auto Ac, auto Ic, unsigned so) {
    constexpr int A = decltype(Ac)::value, I = decltype(Ic)::value;
    if constexpr (I >= 0 && I < 6) dma_sc1(inbox_at(A, I) + m2, pvo, srdp, so + poff[I / 2] + (I & 1) * 256);
    mark<310 + A>();
  };
  auto own_idx = [&](int pp, int half) { return (unsigned)(4 * chunk[pp] + 2 * tg + half - 3 * sp); };
  const unsigned own_w = F_OWN + cg * 1024 + lane * 16;
  auto ready = [&](u64 f, int gs) { return __all(f == tag || (c16 >> 2) != gs || lane >= 32) != 0; };
  auto acked = [&](u64 f) { return __all(f == tag || (c16 >> 2) != grp || lane < 32) != 0; };
  int spin = 0;
  GAP();
  sfor<108>([&](auto GIc) {
    constexpr int GI = decltype(GIc)::value, J = GI >> 2, C = GI & 3, I = GI % 12, N = GI / 12;
    const bf16x8 a = cat(wlo[GI & 3], whi[GI & 3]);
    sfor<4>([&](auto Tc) {
      constexpr int T = decltype(Tc)::value;
      if constexpr (I == 0 && N > 0) { if constexpr (T == 0) { nop<2>(); GAP(); } pk[2 * T] = cvtpk(oc[T][0], oc[T][1]); pk[2 * T + 1] = cvtpk(oc[T][2], oc[T][3]); GAP(); }
      if constexpr (I == 0) mma0(oc[T], a, hf[T][I]); else mma(oc[T], a, hf[T][I]);
      GAP();
      if constexpr (I == 0 && N > 6) { pk[2 * T] = nopoison(pk[2 * T]); pk[2 * T + 1] = nopoison(pk[2 * T + 1]); }
      if constexpr (C == 1 && T == 0 && J < 26) {
        waitv<p2_wait(J)>(); need<101 + J>();
        if constexpr (J >= 6 && J % 3 == 0 && J <= LASTFLAG) need<200 + J / 3 - 2>();
        bar();
        if constexpr (J >= 6 && J % 3 == 0 && J <= LASTFLAG) { if (tid == 0) set_flag(ws, rt, chunk[J >= 6 ? J / 3 - 2 : 0], sp, tag); }
      }
      if constexpr (C == 1 && T == 1 && J == USE1) {
        waitv_tie<vm_allowed(19 + J, 301)>(fl); need<301>();
        pre0 = ready(fl, gsrc[0]);
        if (!acked(fl)) {
          for (; spin < 200000; spin++) { __builtin_amdgcn_s_sleep(1); u64 f2 = poll_load(fp); waitv_tie<0>(f2); if (acked(f2)) break; }
        }
        pso2 = pre0 ? (unsigned)gsrc[0] * 65536u : 0x80000000u;
      }
      if constexpr (C == 0 && T == 0 && (J == POLL1 || J == POLL2)) { fl = poll_load(fp); mark<301>(); }
      if constexpr (GI + 3 < 108 && T < 2) rdw2(IC<GI + 3>{}, Tc);
      if constexpr (GI + 2 < 12) rdh(IC<GI + 2>{}, Tc);
      if constexpr (I == 0 && N > 0 && (T & 1)) {
        unsigned so = pso + (unsigned)chunk[N > 0 ? N - 1 : 0] * (16 * 16384);
        if constexpr (N == 8) so = own_idx(7, T >> 1) < 3u ? 0x80000000u : so;
        __builtin_amdgcn_raw_buffer_store_b128((u32x4){pk[2 * T - 2], pk[2 * T - 1], pk[2 * T], pk[2 * T + 1]}, prs, lane * 16 + (T >> 1) * 1024, so, ST_AUX); GAP(); mark<200 + N - 1>();
      }
      if constexpr (I == 1 && N == 8 && T >= 2) {
        const unsigned oi = own_idx(7, T - 2);
        if (oi < 3u) lds_wr128(own_w + oi * 4096, (u32x4){pk[4 * T - 8], pk[4 * T - 7], pk[4 * T - 6], pk[4 * T - 5]});
      }
      if constexpr (C >= 2 && J < 26) {
        constexpr int cnt = p2_count(J + 1);
        if constexpr (cnt == 1) { if constexpr (T == 2) load2(IC<p2_first(J + 1)>{}, IC<C - 2>{}); }
        if constexpr (cnt == 2) { if constexpr (T >= 2) load2(IC<p2_first(J + 1) + C - 2>{}, IC<T - 2>{}); }
      }
      if constexpr (C >= 2 && J >= USE1 && J < USE1 + 3 && T == 2) inbox(IC<0>{}, IC<2 * (J - USE1) + C - 2>{}, pso2);
      GAP();
    });
  });
  nop<7>(); GAP();
  sfor<2>([&](auto hc) {
    constexpr int H = decltype(hc)::value;
    u32x4 o = {nopoison(cvtpk(oc[2 * H][0], oc[2 * H][1])), nopoison(cvtpk(oc[2 * H][2], oc[2 * H][3])), nopoison(cvtpk(oc[2 * H + 1][0], oc[2 * H + 1][1])), nopoison(cvtpk(oc[2 * H + 1][2], oc[2 * H + 1][3]))};
    const unsigned oi = own_idx(8, H);
    __builtin_amdgcn_raw_buffer_store_b128(o, prs, lane * 16 + H * 1024, oi < 3u ? 0x80000000u : pso + (unsigned)chunk[8] * (16 * 16384), ST_AUX);
    if (oi < 3u) lds_wr128(own_w + oi * 4096, o);
  });
  waitv_tie<2>(fl); need<301>(); need<310>();
  pre1 = ready(fl, gsrc[1]);
  sfor<6>([&](auto i) { inbox(IC<1>{}, i, pre1 ? (unsigned)gsrc[1] * 65536u : 0x80000000u); });
  unsigned ooff[3]; u32x2 xv[3];
#pragma unroll
  for (int i = 0; i < 3; i++) {
    const int sg = 6 * w + 2 * i, pc = 3 * sp + (sg >> 4);
    ooff[i] = (unsigned)((rt * 128 + (pc & 3) * 32 + (g & 1) * 16 + c16) * DM + (pc >> 2) * 64 + (sg & 15) * 4 + (g >> 1) * 4);
    xv[i] = *(const u32x2*)(x + ooff[i]);
  }
  u32x4 sel;
#pragma unroll
  for (int j = 0; j < 4; j++) sel[j] = ((c16 & 7) >> 1) == j ? ((c16 & 1) ? 0x3f800000u : 0x00003f80u) : 0u;
  const u32x4 z4 = {0u, 0u, 0u, 0u};
  const bf16x8 af = __builtin_bit_cast(bf16x8, c16 < 8 ? sel : z4), as = __builtin_bit_cast(bf16x8, c16 < 8 ? z4 : sel);
  f32x4 dd[6];
  unsigned ib = lane * 16 + w * 2048, ibh = ib + 65536, ibh2 = ib + 131072; pin(ibh); pin(ibh2);
  auto rdin = [&](auto Ac, auto Ic) { constexpr unsigned O = inbox_at(decltype(Ac)::value, decltype(Ic)::value);
    return lds_rd128(O < 65536 ? ib + O : (O < 131072 ? ibh + (O - 65536) : ibh2 + (O - 131072))); };
  sfor<6>([&](auto ic) { dd[decltype(ic)::value] = mfma(af, rdin(IC<0>{}, ic), (f32x4){0.f, 0.f, 0.f, 0.f}); });
  waitl0(); bar();
  {
    const bf16x8 af0 = __builtin_bit_cast(bf16x8, lane < 8 ? sel : z4);
    const unsigned orb = F_OWN + c16 * 16 + w * 1536;
    sfor<6>([&](auto ic) { constexpr int i = decltype(ic)::value; dd[i] = mfma(af0, lds_rd128(orb + i * 256), dd[i]); });
  }
  if (!(pre0 && pre1)) {
    for (; spin < 200000; spin++) {
      fl = poll_load(fp); waitv_tie<0>(fl);
      if (ready(fl, gsrc[0]) && ready(fl, gsrc[1])) break;
      __builtin_amdgcn_s_sleep(1);
    }
    sfor<6>([&](auto ic) { pin(dd[decltype(ic)::value]); });
    if (!pre0) sfor<6>([&](auto i) { inbox(IC<0>{}, i, (unsigned)gsrc[0] * 65536u); });
    if (!pre1) sfor<6>([&](auto i) { inbox(IC<1>{}, i, (unsigned)gsrc[1] * 65536u); });
  }
  auto ld6 = [&](u32x4 (&v)[6], unsigned vo, int gs) {
#pragma unroll
    for (int i = 0; i < 6; i++) v[i] = __builtin_amdgcn_raw_buffer_load_b128(prs, vo, poff[i >> 1] + (i & 1) * 256 + gs * 65536, 16);
  };
  u32x4 po[6], pa[6];
  const unsigned pvo_o = g == rr ? OOB : pvo;
  bool oka = false, oko = false;
  for (; spin < 200000; spin++) {
    asm volatile("" ::: "memory");
    if (!oko) ld6(po, pvo_o, grp);
    if (!oka) ld6(pa, pvo, 3);
    oko = clean6(po); oka = clean6(pa);
    if (oka && oko) break;
    __builtin_amdgcn_s_sleep(TAIL_SLEEP);
  }
  waitv<0>();
  if (!pre0) sfor<6>([&](auto ic) { constexpr int i = decltype(ic)::value; dd[i] = mfma(af, rdin(IC<0>{}, ic), dd[i]); });
  sfor<6>([&](auto ic) { constexpr int i = decltype(ic)::value;
    dd[i] = mfma(af, rdin(IC<1>{}, ic), dd[i]); dd[i] = mfma(af, __builtin_bit_cast(bf16x8, po[i]), dd[i]); dd[i] = mfma(as, __builtin_bit_cast(bf16x8, pa[i]), dd[i]); });
#pragma unroll
  for (int i = 0; i < 3; i++) {
    float o[4];
#pragma unroll
    for (int j = 0; j < 4; j++) {
      float fv = dd[2 * i][j], sv = dd[2 * i + 1][j];
      swap32(fv, sv);
      const float gt = fv * __builtin_amdgcn_rcpf(1.f + __builtin_amdgcn_exp2f(-1.4426950408889634f * fv));
      o[j] = ((j & 1) ? bfhi(xv[i][j >> 1]) : bflo(xv[i][j >> 1])) + sv * gt;
    }
    *(u32x2*)(out + ooff[i]) = (u32x2){cvtpk(o[0], o[1]), cvtpk(o[2], o[3])};
  }
  if (sp == 0 && tid == 0) __hip_atomic_store((gu64*)(ws + EPOCH_OFF) + rt * 16, tag, RLX_AGENT);
  asm volatile(".Lkernel_end:");
}

constexpr unsigned A_SLOT = 27648, A_XR = 110592, A_HALO = 143360, A_DUMMY = 161792, A_WOH = 125952, A_IMG = 872, A_OB = 0, A_OS = 296;
constexpr unsigned wo_slot(int n) { return n < 2 ? A_WOH + 18432u * n : (n < 6 ? 38912u + 18432u * (n - 2) : (n < 8 ? 18432u * (n - 6) : A_WOH)); }
constexpr int a_touch(int s) { return s == -1 ? 3 : 0; }
constexpr int wo_allowed(int N, int need) {
  int after = 0;
  auto op = [&](int tag) { if (tag == need) after = 0; else after += 2; };
  for (int n = 0; n < 6; n++) op(n);
  for (int n = 0; n <= N; n++) {
    if (n >= 2) op(20 + n - 1);
    if (n == N) break;
    if (n == 0) { op(6); op(7); }
    if (n == 1) { op(8); op(20); }
  }
  return after;
}
constexpr int item_pos(int cg, int tg, int C) {
  if (cg == 3) { const int i = (C < 3 ? C : C - 1) + 3 * tg, j = i >= 6 ? i - 6 : i; return j == 2 ? 25 : (j == 5 ? 26 : 20 + j - (j > 2)); }
  if (C < 4) return 8 + 4 * cg + (C ^ (2 * tg));
  return (cg == 2 && C == 6) ? 24 : 3 * cg + C - 4;
}

constexpr u64 pos_table(int C) { u64 t = 0; for (int w = 0; w < 8; w++) t |= (u64)item_pos(w & 3, w >> 2, C) << (5 * w); return t; }
template <int C> DI unsigned pos_of(int w) { return (unsigned)(pos_table(C) >> (5 * w)) & 31u; }

struct WoLd { u32x4 srd; unsigned vo1, vo2, so, so2; int w; };
DI WoLd wo_setup(const u16* wo, int hg, int w, int lane) {
  const int i8 = lane >> 3, c8 = lane & 7; const unsigned swz = 16u * (c8 ^ (2 * ((lane >> 4) & 3)));
  return {make_srd(wo, DM * DM * 2), (8 * (i8 >> 2) + (i8 & 3)) * (DM * 2) + swz, i8 * (DM * 2) + swz,
          (unsigned)((hg * 144 + 32 * (w >> 1) + 4 * (w & 1)) * (DM * 2)), (unsigned)((hg * 144 + 128 + 8 * w) * (DM * 2)), w};
}
template <int N, int I> DI void wo_load(const WoLd& l) {
  if constexpr (I < 2) dma(wo_slot(N) + (2 * l.w + I) * 1024, l.vo1, l.srd, l.so + I * 16 * DM * 2 + N * 128);
  else if (l.w < 2) dma(wo_slot(N) + (16 + l.w) * 1024, l.vo2, l.srd, l.so2 + N * 128);
  mark<600 + N>();
}

DI void att_tail(const WoLd& wl, char* ws, int rt, int hg, int w, u64 tag) {
  int tid = threadIdx.x; pin(tid);
  const int lane = tid & 63, tg = w >> 2, cg = w & 3;
  const int g = lane >> 4, q = (lane & 15) >> 2, p = lane & 3, c16 = lane & 15;
  const f32x4 zero4 = {0.f, 0.f, 0.f, 0.f};
  bf16x4 wlo[4], whi[4];
  auto wimg = [&](unsigned a, const f32x4& v) { lds_wr64(a, (u32x2){cvtpk(v[0], v[1]), cvtpk(v[2], v[3])}); };
  const gu64* ap = (const gu64*)(ws + ACK_OFF) + rt * 16 + (c16 < 12 ? c16 : 0);
  u64 ack = poll_load(ap); mark<620>();
  auto loado = [&](auto Nc, auto Ic) { wo_load<decltype(Nc)::value, decltype(Ic)::value>(wl); };
  {
    const int hp = w >> 1, par = w & 1;
    const unsigned r0 = (2 * c16 + par + 16) * A_IMG + 72 * hp, rm = (unsigned)max(2 * c16 + par - 16, 0) * A_IMG + 72 * hp + 288;
    unsigned rq[3] = {r0 + 8 * g, r0 + 8 * g + 40, r0 + 32};
    unsigned rk[3] = {rq[0] + 288, rq[2] + 288, rq[1] + 288};
    unsigned rkm[3] = {rm + 8 * g, rm + 32, rm + 8 * g + 40};
    const int kr = 4 * g + q;
    unsigned rv = (2 * kr + par + 16) * A_IMG + 576 + 72 * hp + 8 * p, rv3 = rv + 96 * A_IMG;
    unsigned rvm = (unsigned)max(2 * kr + par - 16, 0) * A_IMG + 576 + 72 * hp + 8 * p;
    unsigned rq3[3], rk3[3];
#pragma unroll
    for (int i = 0; i < 3; i++) { rq3[i] = rq[i] + 96 * A_IMG; pin(rq[i]); pin(rq3[i]); rk3[i] = rk[i] + 96 * A_IMG; pin(rk[i]); pin(rk3[i]); pin(rkm[i]); }
    pin(rv); pin(rv3); pin(rvm);
    f32x4 biasA, biasA0, biasB; unsigned mA[2], mA0[2], mB[2];
#pragma unroll
    for (int j = 0; j < 4; j++) {
      const int kk = 4 * g + j; const bool va = kk >= c16 + 9, vb = (kk <= c16) && (kk + 7 >= c16);
      biasA[j] = va ? 0.f : -1e30f; biasB[j] = vb ? 0.f : -1e30f; biasA0[j] = (va && rt > 0) ? 0.f : -1e30f;
      const unsigned bm = (j & 1) ? 0xffff0000u : 0x0000ffffu;
      if ((j & 1) == 0) { mA[j >> 1] = 0; mB[j >> 1] = 0; }
      mA[j >> 1] |= va ? bm : 0u; mB[j >> 1] |= vb ? bm : 0u;
    }
    mA0[0] = rt > 0 ? mA[0] : 0u; mA0[1] = rt > 0 ? mA[1] : 0u;
    pin(biasA); pin(biasA0); pin(biasB); pin(mA[0]); pin(mA[1]); pin(mA0[0]); pin(mA0[1]); pin(mB[0]); pin(mB[1]);
    unsigned qmask = g == 0 ? 0xffffffffu : 0u; pin(qmask);
    unsigned ow0 = A_OB + (2 * c16 + par) * A_OS + 72 * hp, owa = ow0 + 8 * g, owb = owa + 40; pin(ow0); pin(owa); pin(owb);
    waitl0(); bar();
    auto asb = [](u32x2 a, u32x2 b) { return __builtin_bit_cast(bf16x8, (u32x4){a[0], a[1], b[0], b[1]}); };
    typedef unsigned u32x8 __attribute__((ext_vector_type(8)));
    typedef unsigned u32x16 __attribute__((ext_vector_type(16)));
    u32x8 kb[5]; u32x16 vb[3]; u32x2 qm[4][2], q4[4];
    auto rdk = [&](auto Uc) { constexpr int U = decltype(Uc)::value;
      sfor<3>([&](auto jc) { constexpr int j = decltype(jc)::value;
        const u32x2 t = lds_rd64(U == 0 ? rkm[j] : (U < 4 ? rk[j] + (U > 0 ? U - 1 : 0) * 32 * A_IMG : rk3[j]));
        kb[U][2 * j] = t[0]; kb[U][2 * j + 1] = t[1]; });
      kb[U][6] = 0u; kb[U][7] = 0u; };
    rdk(IC<0>{});
    sfor<4>([&](auto Uc) { constexpr int U = decltype(Uc)::value; constexpr unsigned ro = U < 3 ? U * 32 * A_IMG : 0;
      q4[U] = lds_rd64((U < 3 ? rq[2] : rq3[2]) + ro);
      sfor<2>([&](auto Hc) { constexpr int H = decltype(Hc)::value; qm[U][H] = lds_rd64((U < 3 ? rq[H] : rq3[H]) + ro); });
      rdk(IC<U + 1>{});
      GAP(); });
    sfor<5>([&](auto Uc) { constexpr int U = decltype(Uc)::value;
      sfor<3>([&](auto Dc) { constexpr int D = decltype(Dc)::value; constexpr unsigned co = D == 0 ? 0 : (D == 1 ? 40 : 32);
        const u32x2 t = __builtin_bit_cast(u32x2, lds_tr((U == 0 ? rvm : (U < 4 ? rv + (U > 0 ? U - 1 : 0) * 32 * A_IMG : rv3)) + co));
        vb[D][2 * U] = t[0]; vb[D][2 * U + 1] = t[1]; }); });
    GAP();
    auto kfrag = [&](auto Uc, auto Hc) { constexpr int U = decltype(Uc)::value, O = 2 * decltype(Hc)::value;
      return __builtin_bit_cast(bf16x8, __builtin_shufflevector(kb[U], kb[U], O, O + 1, O + 2, O + 3)); };
    auto vfrag = [&](auto Uc, auto Dc) { constexpr int O = 2 * decltype(Uc)::value, D = decltype(Dc)::value;
      return __builtin_bit_cast(bf16x8, __builtin_shufflevector(vb[D], vb[D], O, O + 1, O + 2, O + 3)); };
    f32x4 sa[8], sb[8];
    sfor<8>([&](auto Ic) { constexpr int I = decltype(Ic)::value, U = I >> 1, H = I & 1;
      const bf16x8 qf = H ? asb((u32x2){0u, q4[U][1] & qmask}, qm[U][1]) : asb(qm[U][0], (u32x2){q4[U][0] & qmask, 0u});
      sa[I] = mfma(kfrag(IC<U>{}, IC<H>{}), qf, U == 0 ? biasA0 : biasA); sb[I] = mfma(kfrag(IC<U + 1>{}, IC<H>{}), qf, biasB); });
    GAP();
    float v[8][4], mx[8];
    sfor<8>([&](auto Ic) { constexpr int I = decltype(Ic)::value;
#pragma unroll
      for (int j = 0; j < 4; j++) v[I][j] = fmx(sa[I][j], sb[I][j]);
      mx[I] = fmx(fmx(fmx(v[I][0], v[I][1]), v[I][2]), v[I][3]); });
    constexpr float SC = 0.23570226039551584f * 1.4426950408889634f;
    sfor<2>([&](auto hc) { constexpr int B = 4 * decltype(hc)::value;
      unpack4(-SC * pack_max(mx[B], mx[B + 1], mx[B + 2], mx[B + 3]), mx[B], mx[B + 1], mx[B + 2], mx[B + 3]); });
    GAP();
    waitl0(); bar();
    sfor<3>([&](auto i) { loado(IC<2>{}, i); }); sfor<3>([&](auto i) { loado(IC<3>{}, i); });
    GAP();
    float sm[8];
    sfor<8>([&](auto Ic) { constexpr int I = decltype(Ic)::value;
#pragma unroll
      for (int j = 0; j < 4; j++) v[I][j] = __builtin_amdgcn_exp2f(__builtin_fmaf(v[I][j], SC, mx[I]));
      sm[I] = (v[I][0] + v[I][1]) + (v[I][2] + v[I][3]); });
    sfor<2>([&](auto hc) { constexpr int B = 4 * decltype(hc)::value;
      unpack4(__builtin_amdgcn_rcpf(pack_sum(sm[B], sm[B + 1], sm[B + 2], sm[B + 3])), sm[B], sm[B + 1], sm[B + 2], sm[B + 3]); });
    GAP();
    sfor<3>([&](auto i) { loado(IC<4>{}, i); }); sfor<3>([&](auto i) { loado(IC<5>{}, i); });
    GAP();
    bf16x8 pf[8];
    sfor<8>([&](auto Ic) { constexpr int I = decltype(Ic)::value, U = I >> 1;
      const unsigned p0 = cvtpk(v[I][0] * sm[I], v[I][1] * sm[I]), p1 = cvtpk(v[I][2] * sm[I], v[I][3] * sm[I]);
      pf[I] = __builtin_bit_cast(bf16x8, (u32x4){p0 & (U == 0 ? mA0[0] : mA[0]), p1 & (U == 0 ? mA0[1] : mA[1]), p0 & mB[0], p1 & mB[1]}); });
    GAP();
    f32x4 o0[8], o1[8];
    sfor<8>([&](auto Ic) { constexpr int I = decltype(Ic)::value; o0[I] = mfma(vfrag(IC<(I >> 1)>{}, IC<(I & 1)>{}), pf[I], zero4); });
    sfor<8>([&](auto Ic) { constexpr int I = decltype(Ic)::value; o1[I] = mfma(vfrag(IC<(I >> 1)>{}, IC<2>{}), pf[I], zero4); });
    GAP();
    sfor<8>([&](auto Ic) { constexpr int I = decltype(Ic)::value; wimg(((I & 1) ? owb : owa) + (I >> 1) * 32 * A_OS, o0[I]); });
    if (g == 0) {
      sfor<4>([&](auto Uc) { constexpr int U = decltype(Uc)::value;
        lds_wr64(ow0 + 32 + U * 32 * A_OS, (u32x2){cvtpk(o1[2 * U][0], o1[2 * U][1]), cvtpk(o1[2 * U + 1][2], o1[2 * U + 1][3])}); });
    }
  }
  const int row = 4 * g + q;
  const unsigned ow = row * 128 + 32 * (cg ^ ((row >> 1) & 3)) + 8 * p; unsigned owh = ow + 65536, owh2 = ow + A_WOH; pin(owh); pin(owh2);
  unsigned ob = A_OB + (tg * 64 + c16) * A_OS + 16 * g, ob4 = A_OB + (tg * 64 + c16) * A_OS + 256 + 8 * g; pin(ob); pin(ob4);
  waitv_tie<8>(ack); need<620>(); need<600>(); need<601>();
  for (int spin = 0; spin < 200000 && !__all(ack == tag); spin++) { __builtin_amdgcn_s_sleep(1); ack = poll_load(ap); waitv_tie<0>(ack); }
  waitl0(); bar();
  auto rdo = [&](auto GIc, auto Hc) {
    constexpr int GI = decltype(GIc)::value, H = decltype(Hc)::value, N = GI / 5, S = GI % 5;
    constexpr unsigned O = wo_slot(N) + S * 4096 + H * 2048;
    const unsigned a = O >= A_WOH ? owh2 + (O - A_WOH) : (O >= 65536 ? owh + (O - 65536) : ow + O);
    if constexpr (H == 0) wlo[GI & 3] = lds_tr(a); else if constexpr (S < 4) whi[GI & 3] = lds_tr(a);
  };
  bf16x8 of[4][4]; u32x2 of4[4];
  sfor<5>([&](auto Sc) { constexpr int S = decltype(Sc)::value;
    if constexpr (S < 3) { rdo(Sc, IC<0>{}); rdo(Sc, IC<1>{}); }
    sfor<4>([&](auto Tc) { constexpr int T = decltype(Tc)::value;
      if constexpr (S < 4) of[T][S & 3] = lds_rd128a8(ob + T * 16 * A_OS + 64 * S); else of4[T] = lds_rd64(ob4 + T * 16 * A_OS); });
    GAP(); });
  const __amdgpu_buffer_rsrc_t prs = __builtin_amdgcn_make_buffer_rsrc(ws + PART_OFF, 0, PART_BYTES, 0x00020000);
  const unsigned pso = (unsigned)(((rt * 9) * 16 + 12 + hg) * 16384 + w * 2048);
  f32x4 oc[4]; unsigned pk[8];
  GAP();
  sfor<45>([&](auto GIc) {
    constexpr int GI = decltype(GIc)::value, N = GI / 5, S = GI % 5;
    const bf16x4 alo = wlo[GI & 3];
    bf16x8 a; if constexpr (S < 4) a = cat(alo, whi[GI & 3]);
    sfor<4>([&](auto Tc) {
      constexpr int T = decltype(Tc)::value;
      if constexpr (S == 0 && N > 0) { if constexpr (T == 0) { nop<2>(); GAP(); } pk[2 * T] = cvtpk(oc[T][0], oc[T][1]); pk[2 * T + 1] = cvtpk(oc[T][2], oc[T][3]); GAP(); }
      if constexpr (S == 0) mma0(oc[T], a, of[T][0]); else if constexpr (S < 4) mma(oc[T], a, of[T][S & 3]); else mma16(oc[T], alo, of4[T]);
      GAP();
      if constexpr (S == 0 && N > 0) { pk[2 * T] = nopoison(pk[2 * T]); pk[2 * T + 1] = nopoison(pk[2 * T + 1]); }
      if constexpr (S == 2 && T == 0 && (N == 0 || N == 1 || N == 4)) {
        if constexpr (N == 0) waitl0();
        waitv<wo_allowed(N, N == 0 ? 1 : (N == 1 ? 5 : 8))>(); need<600 + (N == 0 ? 1 : (N == 1 ? 5 : 8))>(); bar();
      }
      if constexpr (GI + 3 < 45 && T < 2) rdo(IC<GI + 3>{}, Tc);
      if constexpr (N == 0 && S >= 3 && T > 0) loado(IC<3 + S>{}, IC<T - 1>{});
      if constexpr (N == 1 && S == 2 && T > 0) loado(IC<8>{}, IC<T - 1>{});
      if constexpr (S == (N == 1 ? 3 : 0) && N > 0 && (T & 1))
        __builtin_amdgcn_raw_buffer_store_b128((u32x4){pk[2 * T - 2], pk[2 * T - 1], pk[2 * T], pk[2 * T + 1]}, prs, lane * 16 + (T >> 1) * 1024, pso + (N - 1) * 16 * 16384, ST_AUX);
      GAP();
    });
  });
  nop<7>(); GAP();
  sfor<2>([&](auto hc) {
    constexpr int H = decltype(hc)::value;
    u32x4 o = {nopoison(cvtpk(oc[2 * H][0], oc[2 * H][1])), nopoison(cvtpk(oc[2 * H][2], oc[2 * H][3])), nopoison(cvtpk(oc[2 * H + 1][0], oc[2 * H + 1][1])), nopoison(cvtpk(oc[2 * H + 1][2], oc[2 * H + 1][3]))};
    __builtin_amdgcn_raw_buffer_store_b128(o, prs, lane * 16 + H * 1024, pso + 8 * 16 * 16384, ST_AUX);
  });
}

DI void att_path(const u16* x, const u16* wqkv, const u16* wo, char* ws, int rt, int hg) {
  const int tid = threadIdx.x, lane = tid & 63;
  const int w = __builtin_amdgcn_readfirstlane(tid >> 6), tg = w >> 2, cg = w & 3;
  const int g = lane >> 4, q = (lane & 15) >> 2, p = lane & 3, c16 = lane & 15;
  const u32x4 srdx = make_srd(x, XN_BYTES), srdw = make_srd(wqkv, DM * 3 * DM * 2), srdo = make_srd(wo, DM * DM * 2);
  const int i8 = lane >> 3, c8 = lane & 7, l32 = lane & 31;
  const unsigned vox = i8 * (DM * 2) + c8 * 16;
  const unsigned sox = (unsigned)((rt * 128 + 16 * w) * (DM * 2));
  u64 epoch = rd_epoch(ws, rt);
  u32x4 xr[2][9], xh[3];
  {
    const unsigned soh = (unsigned)((rt > 0 ? rt * 128 - 16 + 2 * w : 0) * (DM * 2)), voh = (lane >> 5) * (DM * 2) + l32 * 16;
    xh[0] = gload<0>(voh, srdx, soh); xh[1] = gload<512>(voh, srdx, soh); xh[2] = gload<1024>(l32 < 8 ? voh : OOB, srdx, soh); mark<430>();
  }
  sfor<9>([&](auto tc) { constexpr int t = decltype(tc)::value;
    xr[0][t] = gload<t * 128>(vox, srdx, sox); mark<400 + 2 * t>(); xr[1][t] = gload<t * 128>(vox, srdx, sox + 8 * DM * 2); mark<401 + 2 * t>(); });
  unsigned vw[4];
#pragma unroll
  for (int i = 0; i < 4; i++) {
    const unsigned n = (w + 8 * i) * 64 + lane, r = (n * 9710u) >> 19, c = n - 54 * r;
    const unsigned k = 8 * ((r >> 2) & 3) + 4 * (r >> 4) + (r & 3);
    const unsigned sec = (c * 57u) >> 10;
    vw[i] = k * 3456 + c * 16 + sec * (1152 - 288);
  }
  auto loadw = [&](auto Sc, auto Ic) {
    constexpr int S = decltype(Sc)::value, I = decltype(Ic)::value;
    if constexpr (S < 18) { if (I < 3 || w < 3) dma((S & 3) * A_SLOT + (w + 8 * I) * 1024, vw[I], srdw, (unsigned)(S * 32 * 3456 + hg * 288)); mark<500 + S>(); }
  };
  sfor<4>([&](auto i) { loadw(IC<0>{}, i); });
  unsigned wbs[7];
  auto img_off = [&](unsigned pos) { const unsigned left = 0u - (unsigned)(pos >= 24), odd = hg & 1;
    return ((288u * (pos >> 3) + 32u * ((pos & 7) + odd)) & ~left) | ((288u * (pos - 24) + 256u - 256u * odd) & left); };
  {
    const unsigned lr = (4 * g + q) * 864 + 8 * p;
    sfor<7>([&](auto Cc) { constexpr int C = decltype(Cc)::value;
      const unsigned pos = pos_of<C>(w);
      wbs[C] = lr + img_off(pos);
      pin(wbs[C]); });
  }
  const unsigned xs = (16 * g + (c16 ^ g)) * 16;
  unsigned xb0 = A_XR + tg * 4096 + xs, xb1 = xb0 ^ 64, hb0 = A_HALO + xs, hb1 = hb0 ^ 64; pin(xb0); pin(xb1); pin(hb0); pin(hb1);
  unsigned xw = A_XR + (c8 >> 2) * 8192 + (16 * (c8 & 3) + (i8 ^ c8)) * 16 + w * 1024; pin(xw);
  GAP();
  {
    Stat st = {}; float r, nmr;
    sfor<3>([&](auto ic) { constexpr int i = decltype(ic)::value; waitv_tie<3 + 18 + 2 - i>(xh[i]); if constexpr (i == 2) need<430>(); stat_acc(xh[i], st); });
    stat_fin<true>(st, lane, r, nmr);
    const unsigned hwa = A_HALO + (l32 >> 2) * 1024 + (16 * (l32 & 3) + ((2 * w + (lane >> 5)) ^ c8)) * 16;
    lds_wr128(hwa, norm8(xh[0], r, nmr)); lds_wr128(hwa + 8192, norm8(xh[1], r, nmr));
    if (l32 < 8) lds_wr128(hwa + 16384, norm8(xh[2], r, nmr));
  }
  GAP();
  float rs[2], nm[2];
  {
    Stat st[2] = {};
    sfor<18>([&](auto ic) { constexpr int i = decltype(ic)::value;
      waitv_tie<3 + (i > 7 ? 3 : 0) + 17 - i>(xr[i & 1][i >> 1]); need<400 + i>(); stat_acc(xr[i & 1][i >> 1], st[i & 1]); GAP();
      if constexpr (i == 7) { sfor<4>([&](auto k) { loadw(IC<1>{}, k); }); } });
    {
      const int jj = (w * 36 >> 3) + g;
      touch(A_DUMMY, (p < 3 && q < 3 && jj < 36) ? (unsigned)((jj * 16 + rt) * 3456 + q * 1152 + (2 * hg + p) * 128) : OOB, srdw, 0u);
      touch(A_DUMMY, (w & 1) && c16 < 9 && g == 0 ? (unsigned)(((w * 36 >> 3) + 4) * 16 + rt) * 3456 + (c16 / 3) * 1152 + (2 * hg + c16 % 3) * 128 : OOB, srdw, 0u);
      touch(A_DUMMY, lane < 11 && w * 11 + lane < 81 ? lane * 128u : OOB, srdo, (unsigned)((hg * 144 + rt * 9) * (DM * 2) + w * 11 * 128));
    }
    stat_fin<false>(st[0], lane, rs[0], nm[0]); stat_fin<false>(st[1], lane, rs[1], nm[1]);
    epoch = epoch_uniform(epoch);
  }
  NormSt ns;
  auto xop = [&](auto Tc, auto Oc) {
    constexpr int T = decltype(Tc)::value, O = decltype(Oc)::value;
    if constexpr (T < 9 && O < 42) norm_op<O>(ns, xr[0][T < 9 ? T : 0], xr[1][T < 9 ? T : 0], rs, nm, xw + (T & 1) * 16384);
  };
  sfor<2>([&](auto r) { constexpr int R = decltype(r)::value; lds_wr128(xw + R * 128, norm8(xr[R][0], rs[R], nm[R])); });
  lds_wr128(xw + 16384, norm8(xr[0][1], rs[0], nm[0]));

  const f32x4 zero4 = {0.f, 0.f, 0.f, 0.f};
  const u64 tag = next_tag(epoch);
  f32x4 acc[7][4], hac[3];
  sfor<4>([&](auto t) { acc[3][decltype(t)::value] = zero4; }); hac[2] = zero4;
  bf16x8 xf[4], xhf;
  bf16x4 wlo[8], whi[8];
  WoLd wl;
  auto rdw = [&](auto GIc, auto Hc) {
    constexpr int GI = decltype(GIc)::value, H = decltype(Hc)::value, S = GI / 7, C = GI % 7;
    if constexpr (GI < 126) {
      const unsigned a = wbs[C] + (S & 1) * A_SLOT + H * 16 * 864;
      if constexpr (H == 0) wlo[GI & 7] = lds_tr(a); else whi[GI & 7] = lds_tr(a);
      if constexpr (H == 1 && (S & 1) == 1) { wbs[C] += (S & 2) ? -2 * A_SLOT : 2 * A_SLOT; pin(wbs[C]); }
    }
  };
  auto rdx = [&](auto Sc, auto Tc) {
    constexpr int S = decltype(Sc)::value, T = decltype(Tc)::value;
    xf[T] = lds_rd128(((S & 1) ? xb1 : xb0) + (S & 3) * 8192 + T * 1024);
  };
  auto rdxh = [&](auto Sc) { constexpr int S = decltype(Sc)::value; xhf = lds_rd128(((S & 1) ? hb1 : hb0) + S * 1024); };
  unsigned ih, ibase;
  unsigned toff[7];
  auto tileoff = [&](int C) { return toff[C]; };
  auto wimg = [&](unsigned a, const f32x4& v) { lds_wr64(a, (u32x2){cvtpk(v[0], v[1]), cvtpk(v[2], v[3])}); };
  waitv<6>(); need<500>(); waitl0(); bar();
  rdw(IC<0>{}, IC<0>{}); rdw(IC<0>{}, IC<1>{}); rdw(IC<1>{}, IC<0>{}); rdw(IC<1>{}, IC<1>{});
  sfor<4>([&](auto t) { rdx(IC<0>{}, t); });
  rdxh(IC<0>{});
  nop<1>();
  GAP();
  sfor<18>([&](auto Sc) {
    constexpr int S = decltype(Sc)::value, G0 = 7 * S;
    auto fill = [&](auto GPc) {
      constexpr int GP = decltype(GPc)::value;
      GAP();
      if constexpr (GP == 21 && S < 17) {
        if constexpr (S < 16) waitv<3 + a_touch(S) + a_touch(S - 1)>(); else waitv<0>();
        need<501 + S>();
        bar();
      }
      if constexpr (GP == 12 && S == 17) { waitl0(); bar(); }
      if constexpr (S == 17 && GP == 10) { int tz = tid; pin(tz); wl = wo_setup(wo, hg, w, tz & 63); }
      if constexpr (S == 17 && GP == 11) { int tz = tid; pin(tz); ih = (tz & 15) * A_IMG + 8 * ((tz >> 4) & 3); ibase = ih + (16 + tg * 64) * A_IMG;
        sfor<7>([&](auto Cc) { constexpr int C = decltype(Cc)::value; toff[C] = img_off(pos_of<C>(w)); }); }
      if constexpr (S == 0) xop(IC<1>{}, IC<GP + 21>{});
      else if constexpr ((S & 1) == 0) xop(IC<S / 2 + 1>{}, IC<GP + 8>{});
      else if constexpr (GP >= 22) xop(IC<(S + 3) / 2>{}, IC<GP - 22>{});
      else if constexpr (GP < 4 && S > 1) xop(IC<(S + 1) / 2>{}, IC<38 + GP>{});
      if constexpr (S < 17) {
        if constexpr (GP < 4) rdw(IC<G0 + 2 + (GP >> 1)>{}, IC<(GP & 1)>{});
        if constexpr (GP >= 8 && GP < 12) rdw(IC<G0 + 4 + ((GP - 8) >> 1)>{}, IC<(GP & 1)>{});
        if constexpr (GP == 15 || GP == 16) rdw(IC<G0 + 6>{}, IC<GP - 15>{});
        if constexpr (GP == 21 || GP == 22) rdw(IC<G0 + 7>{}, IC<GP - 21>{});
        if constexpr (GP == 24 || GP == 26) rdw(IC<G0 + 8>{}, IC<(GP - 24) / 2>{});
        if constexpr (GP >= 23 && (GP & 1) == 1) rdx(IC<S + 1>{}, IC<(GP - 23) / 2>{});
        if constexpr (GP == 28) rdxh(IC<S + 1>{});
      } else {
        if constexpr (GP < 10) rdw(IC<G0 + 2 + (GP >> 1)>{}, IC<(GP & 1)>{});
        if constexpr (GP >= 12 && GP < 20) wimg(ibase + tileoff((GP - 12) & 1) + ((GP - 12) >> 1) * 16 * A_IMG, acc[(GP - 12) & 1][(GP - 12) >> 1]);
        if constexpr (GP == 13 || GP == 15) wimg(ih + tileoff((GP - 13) / 2), hac[(GP - 13) / 2]);
        if constexpr (GP >= 20 && GP < 24) wimg(ibase + tileoff(2) + (GP - 20) * 16 * A_IMG, acc[2][GP - 20]);
        if constexpr (GP >= 26) wimg(ibase + tileoff(4) + (GP - 26) * 16 * A_IMG, acc[4][GP - 26]);
        if constexpr (GP == 14 || GP == 17 || GP == 19) wo_load<0, (GP == 14 ? 0 : (GP == 17 ? 1 : 2))>(wl);
        if constexpr (GP == 21 || GP == 24 || GP == 25) wo_load<1, (GP == 21 ? 0 : (GP == 24 ? 1 : 2))>(wl);
      }
      if constexpr (GP == 4 || GP == 6 || GP == 12 || GP == 17) loadw(IC<S + 2>{}, IC<(GP == 4 ? 0 : (GP == 6 ? 1 : (GP == 12 ? 2 : 3)))>{});
      GAP();
    };
    auto mm = [&](f32x4& c, const bf16x8& a, const bf16x8& b) { if constexpr (S == 0) mma0(c, a, b); else mma(c, a, b); };
    const bf16x8 a0 = cat(wlo[G0 & 7], whi[G0 & 7]), a1 = cat(wlo[(G0 + 1) & 7], whi[(G0 + 1) & 7]);
    sfor<4>([&](auto Tc) { constexpr int T = decltype(Tc)::value;
      mm(acc[0][T], a0, xf[T]); fill(IC<2 * T>{}); mm(acc[1][T], a1, xf[T]); fill(IC<2 * T + 1>{}); });
    mm(hac[0], a0, xhf); fill(IC<8>{});
    mm(hac[1], a1, xhf); fill(IC<9>{});
    const bf16x8 a2 = cat(wlo[(G0 + 2) & 7], whi[(G0 + 2) & 7]);
    sfor<4>([&](auto Tc) { constexpr int T = decltype(Tc)::value; mm(acc[2][T], a2, xf[T]); fill(IC<10 + T>{}); });
    const bf16x8 a3 = cat(wlo[(G0 + 3) & 7], whi[(G0 + 3) & 7]);
    mma_alt3(acc[3][0], a3, xf[0], hac[2], a2, xhf, cg); fill(IC<14>{});
    sfor<3>([&](auto Tc) { constexpr int T = decltype(Tc)::value + 1; mma_not3(acc[3][T], a3, xf[T], cg); fill(IC<14 + T>{}); });
    const bf16x8 a4 = cat(wlo[(G0 + 4) & 7], whi[(G0 + 4) & 7]);
    sfor<4>([&](auto Tc) { constexpr int T = decltype(Tc)::value; mm(acc[4][T], a4, xf[T]); fill(IC<18 + T>{}); });
    const bf16x8 a5 = cat(wlo[(G0 + 5) & 7], whi[(G0 + 5) & 7]), a6 = cat(wlo[(G0 + 6) & 7], whi[(G0 + 6) & 7]);
    sfor<4>([&](auto Tc) { constexpr int T = decltype(Tc)::value;
      mm(acc[5][T], a5, xf[T]); fill(IC<22 + 2 * T>{}); mm(acc[6][T], a6, xf[T]); fill(IC<23 + 2 * T>{}); });
  });
  {
    if (cg < 3) { sfor<4>([&](auto Tc) { constexpr int T = decltype(Tc)::value; wimg(ibase + tileoff(3) + T * 16 * A_IMG, acc[3][T]); }); }
    else wimg(ih + tileoff(2), hac[2]);
    sfor<4>([&](auto Tc) { constexpr int T = decltype(Tc)::value;
      wimg(ibase + tileoff(5) + T * 16 * A_IMG, acc[5][T]); wimg(ibase + tileoff(6) + T * 16 * A_IMG, acc[6][T]); });
  }
  GAP();
  att_tail(wl, ws, rt, hg, w, tag);
}

__global__ __launch_bounds__(512, 2) void k1_main(const u16* __restrict__ x, const u16* __restrict__ wqkv, const u16* __restrict__ wo,
                                                  const u16* __restrict__ w1, const u16* __restrict__ w2, char* __restrict__ ws, u16* __restrict__ out) {
  const int b = blockIdx.x, xc = b & 7, i = b >> 3;
  const int rt = 8 * (xc >> 2) + (i & 7), j = i >> 3;
  { const int ln = threadIdx.x & 63, wv = threadIdx.x >> 6;
    touch(163584u, (ln < 2) ? (unsigned)((i * 16 + wv * 2 + ln) * 128) : OOB, code_srd(), 0u); }
  if (j < 3) ffn_path(x, w1, w2, ws, out, rt, 3 * (xc & 3) + j);
  else att_path(x, wqkv, wo, ws, rt, xc & 3);
}

extern "C" void kernel_launch(void* const* d_in, const int* in_sizes, int n_in, void* d_out, int out_size, void* d_ws, size_t ws_size, hipStream_t stream) {
  const u16* x = (const u16*)d_in[0]; const u16* wqkv = (const u16*)d_in[1]; const u16* wo = (const u16*)d_in[2];
  const u16* w1 = (const u16*)d_in[4]; const u16* w2 = (const u16*)d_in[5];
  (void)hipFuncSetAttribute((const void*)k1_main, hipFuncAttributeMaxDynamicSharedMemorySize, 163840);
  k1_main<<<256, 512, 163840, stream>>>(x, wqkv, wo, w1, w2, (char*)d_ws, (u16*)d_out);
}
```
